```python
import torch

def _softmax(x, axis=-1):
    return torch.softmax(x, dim=axis)
def _gelu(x):
    return torch.nn.functional.gelu(x, approximate="tanh")
def _silu(x):
    return torch.nn.functional.silu(x)
def _sigmoid(x):
    return torch.sigmoid(x)
def _layernorm(x, axis=-1, eps=1e-5):
    m = x.mean(dim=axis, keepdim=True)
    v = x.var(dim=axis, keepdim=True, unbiased=False)
    return (x - m) / torch.sqrt(v + eps)
def _scatter_add(tgt, idx, src, axis=0):
    return tgt.clone().index_add_(axis, idx, src)
def _segment_sum(src, seg, n):
    out = torch.zeros((n,) + src.shape[1:], dtype=src.dtype)
    return out.index_add_(0, seg, src)
def _pad(x, pw):
    flat = tuple(v for pair in reversed(pw) for v in pair)
    return torch.nn.functional.pad(x, flat)

kernel_name = 'parallel_sum__ssm_conv_s5__glu_gated_swiglu__prompt'

def setup_inputs():

    _w = (lambda a: (lambda t: t.to(torch.bfloat16) if t.is_floating_point() else t)(torch.as_tensor(a)))
    def _A_setup():
        B, S, D = 1, 2048, 640
        P = D
        Lambda_re = -0.5 * (lambda _x: torch.exp(torch.as_tensor(_x)))((lambda *s: torch.randn(*s, dtype=torch.bfloat16) * 0.2)(P))
        Lambda_im = (lambda *s: torch.randn(*s, dtype=torch.bfloat16) * 0.8)(P) * 1.0
        B_re = (lambda *s: torch.randn(*s, dtype=torch.bfloat16) * 0.2)(P, D) * (D ** -0.5)
        B_im = (lambda *s: torch.randn(*s, dtype=torch.bfloat16) * 0.2)(P, D) * (D ** -0.5)
        C_re = (lambda *s: torch.randn(*s, dtype=torch.bfloat16) * 0.2)(D, P) * (P ** -0.5)
        C_im = (lambda *s: torch.randn(*s, dtype=torch.bfloat16) * 0.2)(D, P) * (P ** -0.5)
        log_dt = (lambda *s: torch.randn(*s, dtype=torch.bfloat16) * 0.2)(P) * 0.3 - 2.0
        Dskip = (lambda *s: torch.randn(*s, dtype=torch.bfloat16) * 0.05)(D)
        return {
            "x": _w((lambda *s: torch.randn(*s, dtype=torch.bfloat16) * 0.05)(B, S, D)),
            "Lambda_re": _w(Lambda_re),
            "Lambda_im": _w(Lambda_im),
            "B_re": _w(B_re),
            "B_im": _w(B_im),
            "C_re": _w(C_re),
            "C_im": _w(C_im),
            "log_dt": _w(log_dt),
            "Dskip": _w(Dskip),
        }
    _A_in = _A_setup()

    def _B_setup():
        B, S, D, F = 1, 2048, 640, 1728
        return {
            "x": _w((lambda *s: torch.randn(*s, dtype=torch.bfloat16) * 0.05)(B, S, D)),
            "w1": _w((lambda *s: torch.randn(*s, dtype=torch.bfloat16) * 0.4)(D, F) * (2.0 / D) ** 0.5),
            "w3": _w((lambda *s: torch.randn(*s, dtype=torch.bfloat16) * 0.4)(D, F) * (2.0 / D) ** 0.5),
            "w2": _w((lambda *s: torch.randn(*s, dtype=torch.bfloat16) * 0.4)(F, D) * (2.0 / F) ** 0.5),
        }
    _B_in = _B_setup()

    return {
        "A_x": _A_in["x"],
        "A_Lambda_re": _A_in["Lambda_re"],
        "A_Lambda_im": _A_in["Lambda_im"],
        "A_B_re": _A_in["B_re"],
        "A_B_im": _A_in["B_im"],
        "A_C_re": _A_in["C_re"],
        "A_C_im": _A_in["C_im"],
        "A_log_dt": _A_in["log_dt"],
        "A_Dskip": _A_in["Dskip"],
        "B_x": _B_in["x"],
        "B_w1": _B_in["w1"],
        "B_w3": _B_in["w3"],
        "B_w2": _B_in["w2"],
    }

def reference(A_x, A_Lambda_re, A_Lambda_im, A_B_re, A_B_im, A_C_re, A_C_im, A_log_dt, A_Dskip, B_x, B_w1, B_w3, B_w2):
    def _A_ref(x, Lambda_re, Lambda_im, B_re, B_im, C_re, C_im, log_dt, Dskip):
        Bsz, L, H = x.shape
        Lam = Lambda_re + 1j * Lambda_im
        Bmat = B_re + 1j * B_im
        Cmat = C_re + 1j * C_im
        dt = torch.exp(log_dt)
        Lam_bar = torch.exp(dt * Lam)
        B_bar = ((Lam_bar - 1.0) / Lam)[:, None] * Bmat
        Bu = torch.einsum("bld,pd->blp", x + 0j, B_bar)
        h = torch.zeros_like(Bu[:, 0, :])
        hs = []
        for t in range(L):
            h = Lam_bar[None, :] * h + Bu[:, t, :]
            hs.append(h)
        hseq = torch.stack(hs, 1)
        y = 2.0 * torch.einsum("blp,dp->bld", hseq, Cmat).real + x * Dskip[None, None, :]
        return y

    def _B_ref(x, w1, w3, w2):
        gate = torch.einsum("bsd,df->bsf", x, w1)
        up = torch.einsum("bsd,df->bsf", x, w3)
        h = _silu(gate) * up
        out = torch.einsum("bsf,fd->bsd", h, w2)
        return out

    x = A_x
    xn = _layernorm(x, axis=-1)
    return x + _A_ref(xn, A_Lambda_re, A_Lambda_im, A_B_re, A_B_im, A_C_re, A_C_im, A_log_dt, A_Dskip) + _B_ref(xn, B_w1, B_w3, B_w2)
```

```cpp
#include <hip/hip_runtime.h>
#include <hip/hip_bf16.h>
typedef unsigned short u16;
__device__ __forceinline__ float bf2f(u16 v){ return __uint_as_float(((unsigned)v)<<16); }
__device__ __forceinline__ u16 f2bf(float f){ unsigned u=__float_as_uint(f); u += 0x7FFF + ((u>>16)&1); return (u16)(u>>16); }
__device__ __forceinline__ float rbf(float f){ return bf2f(f2bf(f)); }
constexpr int S=2048, D=640, P=640, F=1728;

__global__ void ln_k(const u16* x, u16* xn){
  int row=blockIdx.x; int t=threadIdx.x; __shared__ float red[64];
  float s=0; for(int i=t;i<D;i+=64) s+=bf2f(x[row*D+i]);
  for(int o=32;o;o>>=1) s+=__shfl_xor(s,o);
  float mf=s/D; float v=0; for(int i=t;i<D;i+=64){float d=bf2f(x[row*D+i])-mf; v+=d*d;}
  for(int o=32;o;o>>=1) v+=__shfl_xor(v,o);
  float m=rbf(mf); float var=rbf(v/D); float sd=rbf(sqrtf(rbf(var+1e-5f)));
  for(int i=t;i<D;i+=64){ float d=rbf(bf2f(x[row*D+i])-m); xn[row*D+i]=f2bf(d/sd); }
}
__global__ void gu_k(const u16* xn,const u16* w1,const u16* w3,u16* h){
  int f=blockIdx.x*64+threadIdx.x; int s=blockIdx.y;
  float g=0,u=0; for(int k=0;k<D;k++){float a=bf2f(xn[s*D+k]); g+=a*bf2f(w1[k*F+f]); u+=a*bf2f(w3[k*F+f]);}
  g=rbf(g); u=rbf(u); float sl=rbf(g/(1.f+__expf(-g))); h[s*F+f]=f2bf(sl*u);
}
__global__ void bu_k(const u16* xn,const u16* bre,const u16* bim,float* bu){
  int p=blockIdx.x*64+threadIdx.x; int s=blockIdx.y;
  float g=0,u=0; for(int k=0;k<D;k++){float a=bf2f(xn[s*D+k]); g+=a*bf2f(bre[p*D+k]); u+=a*bf2f(bim[p*D+k]);}
  bu[(s*P+p)*2]=g; bu[(s*P+p)*2+1]=u;
}
__global__ void scan_k(const u16* lre,const u16* lim,const u16* ldt,float* bu){
  int p=blockIdx.x*64+threadIdx.x;
  float dt=rbf(expf(bf2f(ldt[p]))); float lr=bf2f(lre[p]), li=bf2f(lim[p]);
  float e=expf(dt*lr); float sn,cs; sincosf(dt*li,&sn,&cs); float ar=e*cs, ai=e*sn;
  float nr=ar-1.f, ni=ai; float den=lr*lr+li*li; float cr=(nr*lr+ni*li)/den, ci=(ni*lr-nr*li)/den;
  float hr=0,hi=0;
  for(int s=0;s<S;s++){ float ur=bu[(s*P+p)*2], ui=bu[(s*P+p)*2+1];
    float br=cr*ur-ci*ui, bi=cr*ui+ci*ur;
    float nhr=ar*hr-ai*hi+br, nhi=ar*hi+ai*hr+bi; hr=nhr;hi=nhi;
    bu[(s*P+p)*2]=hr; bu[(s*P+p)*2+1]=hi; }
}
__global__ void fin_k(const u16* x,const u16* xn,const u16* h,const float* hs,const u16* w2,const u16* cre,const u16* cim,const u16* dsk,float* out){
  int d=blockIdx.x*64+threadIdx.x; int s=blockIdx.y;
  float o=0; for(int k=0;k<F;k++) o+=bf2f(h[s*F+k])*bf2f(w2[k*D+d]);
  float y=0; for(int p=0;p<P;p++) y+=rbf(hs[(s*P+p)*2])*bf2f(cre[d*P+p])-rbf(hs[(s*P+p)*2+1])*bf2f(cim[d*P+p]);
  float ya=2.f*y+rbf(bf2f(xn[s*D+d])*bf2f(dsk[d]));
  out[s*D+d]=bf2f(x[s*D+d])+ya+rbf(o);
}
extern "C" void kernel_launch(void* const* d_in, const int* in_sizes, int n_in, void* d_out, int out_size, void* d_ws, size_t ws_size, hipStream_t stream){
  const u16* x=(const u16*)d_in[0]; const u16* lre=(const u16*)d_in[1]; const u16* lim=(const u16*)d_in[2];
  const u16* bre=(const u16*)d_in[3]; const u16* bim=(const u16*)d_in[4]; const u16* cre=(const u16*)d_in[5]; const u16* cim=(const u16*)d_in[6];
  const u16* ldt=(const u16*)d_in[7]; const u16* dsk=(const u16*)d_in[8]; const u16* w1=(const u16*)d_in[10]; const u16* w3=(const u16*)d_in[11]; const u16* w2=(const u16*)d_in[12];
  char* ws=(char*)d_ws; u16* xn=(u16*)ws; u16* h=(u16*)(ws+4*1024*1024); float* bu=(float*)(ws+16*1024*1024);
  ln_k<<<S,64,0,stream>>>(x,xn);
  gu_k<<<dim3(F/64,S),64,0,stream>>>(xn,w1,w3,h);
  bu_k<<<dim3(P/64,S),64,0,stream>>>(xn,bre,bim,bu);
  scan_k<<<P/64,64,0,stream>>>(lre,lim,ldt,bu);
  fin_k<<<dim3(D/64,S),64,0,stream>>>(x,xn,h,bu,w2,cre,cim,dsk,(float*)d_out);
}
```

```cpp
#include <hip/hip_runtime.h>
#include <hip/hip_bf16.h>
typedef unsigned short u16;
__device__ __forceinline__ float bf2f(u16 v){ return __uint_as_float(((unsigned)v)<<16); }
__device__ __forceinline__ u16 f2bf(float f){ unsigned u=__float_as_uint(f); u += 0x7FFF + ((u>>16)&1); return (u16)(u>>16); }
__device__ __forceinline__ float rbf(float f){ return bf2f(f2bf(f)); }
constexpr int S=2048, D=640, P=640, F=1728;

__global__ void ln_k(const u16* x, u16* xn){
  int row=blockIdx.x; int t=threadIdx.x; __shared__ float red[64];
  float s=0; for(int i=t;i<D;i+=64) s+=bf2f(x[row*D+i]);
  for(int o=32;o;o>>=1) s+=__shfl_xor(s,o);
  float mf=s/D; float v=0; for(int i=t;i<D;i+=64){float d=bf2f(x[row*D+i])-mf; v+=d*d;}
  for(int o=32;o;o>>=1) v+=__shfl_xor(v,o);
  float m=rbf(mf); float var=rbf(v/D); float sd=rbf(sqrtf(rbf(var+1e-5f)));
  for(int i=t;i<D;i+=64){ float d=rbf(bf2f(x[row*D+i])-m); xn[row*D+i]=f2bf(d/sd); }
}
__global__ void gu_k(const u16* xn,const u16* w1,const u16* w3,u16* h){
  int f=blockIdx.x*64+threadIdx.x; int s=blockIdx.y;
  float g=0,u=0; for(int k=0;k<D;k++){float a=bf2f(xn[s*D+k]); g+=a*bf2f(w1[k*F+f]); u+=a*bf2f(w3[k*F+f]);}
  g=rbf(g); u=rbf(u); float sl=rbf(g/(1.f+__expf(-g))); h[s*F+f]=f2bf(sl*u);
}
__global__ void bu_k(const u16* xn,const u16* bre,const u16* bim,float* bu){
  int p=blockIdx.x*64+threadIdx.x; int s=blockIdx.y;
  float g=0,u=0; for(int k=0;k<D;k++){float a=bf2f(xn[s*D+k]); g+=a*bf2f(bre[p*D+k]); u+=a*bf2f(bim[p*D+k]);}
  bu[(s*P+p)*2]=g; bu[(s*P+p)*2+1]=u;
}
__global__ void scan_k(const u16* lre,const u16* lim,const u16* ldt,float* bu){
  int p=blockIdx.x*64+threadIdx.x;
  float dt=rbf(expf(bf2f(ldt[p]))); float lr=bf2f(lre[p]), li=bf2f(lim[p]);
  float e=expf(dt*lr); float sn,cs; sincosf(dt*li,&sn,&cs); float ar=e*cs, ai=e*sn;
  float nr=ar-1.f, ni=ai; float den=lr*lr+li*li; float cr=(nr*lr+ni*li)/den, ci=(ni*lr-nr*li)/den;
  float hr=0,hi=0;
  for(int s=0;s<S;s++){ float ur=bu[(s*P+p)*2], ui=bu[(s*P+p)*2+1];
    float br=cr*ur-ci*ui, bi=cr*ui+ci*ur;
    float nhr=ar*hr-ai*hi+br, nhi=ar*hi+ai*hr+bi; hr=nhr;hi=nhi;
    bu[(s*P+p)*2]=hr; bu[(s*P+p)*2+1]=hi; }
}
__global__ void fin_k(const u16* x,const u16* xn,const u16* h,const float* hs,const u16* w2,const u16* cre,const u16* cim,const u16* dsk,float* out){
  int d=blockIdx.x*64+threadIdx.x; int s=blockIdx.y;
  float o=0; for(int k=0;k<F;k++) o+=bf2f(h[s*F+k])*bf2f(w2[k*D+d]);
  float y=0; for(int p=0;p<P;p++) y+=rbf(hs[(s*P+p)*2])*bf2f(cre[d*P+p])-rbf(hs[(s*P+p)*2+1])*bf2f(cim[d*P+p]);
  float ya=2.f*y+rbf(bf2f(xn[s*D+d])*bf2f(dsk[d]));
  out[s*D+d]=bf2f(x[s*D+d])+ya+rbf(o);
}

__global__ void probe_k(const unsigned* p, int n, int strideDw, unsigned* sink){
  unsigned acc=0; unsigned off=0;
  for(int i=0;i<n;i++){ unsigned v;
    const unsigned* q=p+(size_t)i*strideDw+off;
    asm volatile("global_load_dword %0, %1, off\n s_waitcnt vmcnt(0)":"=&v"(v):"v"(q):"memory");
    acc+=v; off=(v==0x12345678u && acc==0x9abcdef0u)?1u:0u; }
  sink[threadIdx.x]=acc;
}
extern "C" void kernel_launch(void* const* d_in, const int* in_sizes, int n_in, void* d_out, int out_size, void* d_ws, size_t ws_size, hipStream_t stream){
  const u16* x=(const u16*)d_in[0]; const u16* lre=(const u16*)d_in[1]; const u16* lim=(const u16*)d_in[2];
  const u16* bre=(const u16*)d_in[3]; const u16* bim=(const u16*)d_in[4]; const u16* cre=(const u16*)d_in[5]; const u16* cim=(const u16*)d_in[6];
  const u16* ldt=(const u16*)d_in[7]; const u16* dsk=(const u16*)d_in[8]; const u16* w1=(const u16*)d_in[10]; const u16* w3=(const u16*)d_in[11]; const u16* w2=(const u16*)d_in[12];
  char* ws=(char*)d_ws; u16* xn=(u16*)ws; u16* h=(u16*)(ws+4*1024*1024); float* bu=(float*)(ws+16*1024*1024);
  probe_k<<<1,1,0,stream>>>((const unsigned*)d_in[9],0,256,(unsigned*)(ws+(64<<20)));
  ln_k<<<S,64,0,stream>>>(x,xn);
  gu_k<<<dim3(F/64,S),64,0,stream>>>(xn,w1,w3,h);
  bu_k<<<dim3(P/64,S),64,0,stream>>>(xn,bre,bim,bu);
  scan_k<<<P/64,64,0,stream>>>(lre,lim,ldt,bu);
  fin_k<<<dim3(D/64,S),64,0,stream>>>(x,xn,h,bu,w2,cre,cim,dsk,(float*)d_out);
}
```

```cpp
#include <hip/hip_runtime.h>
#include <stdint.h>

struct Buf { const void* p; uint32_t bytes; uint32_t fmt; };
struct Args { Buf b[14]; uint8_t job[80]; };

__global__ __launch_bounds__(256, 1) void fused_ln_s5_swiglu(Args args) {
    __shared__ __attribute__((aligned(16))) char lds[163840];
    asm volatile(
"s_load_dwordx16 s[4:19], %0, 0x0\n"
"s_load_dwordx16 s[20:35], %0, 0x40\n"
"s_load_dwordx16 s[36:51], %0, 0x80\n"
"s_load_dwordx8 s[52:59], %0, 0xc0\n"
"s_mov_b32 s3, %1\n"
"v_mov_b32 v254, %2\n"
"v_lshrrev_b32 v232, 6, v254\n"
"v_and_b32 v253, 63, v254\n"
"v_readfirstlane_b32 s64, v232\n"
"s_getpc_b64 s[80:81]\n"
"L%=_pc:\n"
"s_lshr_b32 s82, s3, 3\n"
"v_min_u32 v233, 4, v253\n"
"s_lshl_b32 s82, s82, 2\n"
"s_add_u32 s82, s82, s64\n"
"s_mul_i32 s82, s82, 5\n"
"v_add_u32 v233, s82, v233\n"
"v_lshlrev_b32 v233, 7, v233\n"
"v_min_u32 v233, L%=_end-L%=_pc-8, v233\n"
"global_load_dword v252, v233, s[80:81]\n"
"s_lshr_b32 s84, s3, 6\n"
"s_lshl_b32 s86, s84, 2\n"
"s_bfe_u32 s87, s3, 0x20001\n"
"s_add_u32 s67, s86, s87\n"
"s_bfe_u32 s85, s3, 0x30003\n"
"s_and_b32 s86, s3, 1\n"
"s_lshl_b32 s86, s86, 3\n"
"s_add_u32 s85, s85, s86\n"
"s_lshl_b32 s86, s67, 4\n"
"s_add_u32 s85, s85, s86\n"
"s_max_u32 s85, s85, 0xb0\n"
"s_sub_u32 s79, s85, 0xb0\n"
"s_and_b32 s85, s79, 0xfc\n"
"s_load_dword s75, %0, s85 offset:0xe0\n"
"s_cmp_ge_u32 s67, 11\n"
"s_cbranch_scc1 L%=_S_start\n"
"L%=_M_start:\n"
"s_and_b32 s83, s3, 7\n"
"s_lshr_b32 s82, s3, 3\n"
"s_and_b32 s85, s83, 1\n"
"s_lshl_b32 s86, s85, 3\n"
"s_and_b32 s85, s82, 7\n"
"s_add_u32 s66, s86, s85\n"
"s_and_b32 s86, s83, 3\n"
"s_lshl_b32 s86, s86, 2\n"
"s_and_b32 s87, s82, 3\n"
"s_add_u32 s98, s86, s87\n"
"s_lshr_b32 s97, s82, 2\n"
"s_lshl_b32 s65, s64, 12\n"
"s_mul_i32 s88, s64, 0x1400\n"
"s_lshl_b32 s2, s67, 4\n"
"s_add_u32 s2, s2, s66\n"
"v_lshrrev_b32 v234, 3, v253\n"
"v_and_b32 v235, 7, v253\n"
"v_lshrrev_b32 v236, 1, v234\n"
"v_xor_b32 v237, v235, v236\n"
"v_lshrrev_b32 v238, 2, v234\n"
"v_and_b32 v239, 3, v234\n"
"v_lshl_add_u32 v238, v238, 4, v239\n"
"v_mul_u32_u24 v238, 0x500, v238\n"
"v_lshl_add_u32 v129, v237, 4, v238\n"
"v_xor_b32 v130, 64, v129\n"
"v_lshlrev_b32 v128, 4, v253\n"
"s_lshl_b32 s82, s66, 2\n"
"s_add_u32 s82, s82, s64\n"
"s_mul_i32 s68, s82, 0xa000\n"
"s_add_u32 s69, s68, 0x1400\n"
"s_add_u32 s70, s68, 0x2800\n"
"s_add_u32 s71, s68, 0x3c00\n"
"s_and_b32 s82, s64, 1\n"
"s_lshl_b32 s83, s82, 3\n"
"s_and_b32 s84, s64, 2\n"
"v_add_u32 v238, s83, v234\n"
"v_mul_u32_u24 v238, 0xd80, v238\n"
"v_and_b32 v239, 1, v236\n"
"v_or_b32 v239, s84, v239\n"
"v_bfe_u32 v240, v253, 1, 2\n"
"v_xor_b32 v239, v239, v240\n"
"v_lshl_add_u32 v238, v239, 5, v238\n"
"v_and_b32 v239, 1, v253\n"
"v_lshl_add_u32 v131, v239, 4, v238\n"
"s_lshl_b32 s83, s64, 3\n"
"s_lshl_b32 s82, s82, 2\n"
"v_add_u32 v238, s83, v234\n"
"v_mul_u32_u24 v238, 0x500, v238\n"
"v_xor_b32 v239, s82, v237\n"
"v_lshl_add_u32 v132, v239, 4, v238\n"
"s_mov_b32 s93, s67\n"
"s_mov_b32 s95, s67\n"
"s_add_u32 s96, s67, 11\n"
"s_mul_i32 s73, s67, 0xa000\n"
"s_lshl_b32 s72, s67, 7\n"
"s_waitcnt lgkmcnt(0)\n"
"s_cmp_ge_u32 s64, 2\n"
"s_cselect_b32 s8, s12, s8\n"
"s_cselect_b32 s9, s13, s9\n"
"v_mov_b32 v251, 0\n"
"buffer_load_dwordx2 v[230:231], v251, s[24:27], 0 offen sc1\n"
"s_lshl_b32 s80, s93, 6\n"
"v_and_b32 v232, 31, v253\n"
"v_lshlrev_b32 v232, 1, v232\n"
"buffer_load_ushort v124, v232, s[44:47], s80 offen\n"
"buffer_load_ushort v125, v232, s[48:51], s80 offen\n"
"buffer_load_ushort v126, v232, s[52:55], s80 offen\n"
"s_lshl_b32 s80, s66, 4\n"
"s_add_u32 s80, s80, s67\n"
"s_lshl_b32 s80, s80, 2\n"
"s_add_u32 s80, s80, s64\n"
"s_mul_i32 s80, s80, 0xa00\n"
"v_lshrrev_b32 v232, 5, v253\n"
"v_mul_u32_u24 v232, 0x500, v232\n"
"v_and_b32 v233, 31, v253\n"
"v_lshl_add_u32 v234, v233, 4, v232\n"
"v_lshl_add_u32 v232, v233, 3, v232\n"
"buffer_load_dwordx4 v[96:99], v234, s[4:7], s80 offen\n"
"buffer_load_dwordx4 v[100:103], v234, s[4:7], s80 offen offset:512\n"
"buffer_load_dwordx2 v[104:105], v232, s[4:7], s80 offen offset:1024\n"
"s_add_u32 m0, s65, 0x10000\n"
"s_nop 0\n"
"buffer_load_dwordx4 v129, s[4:7], s68 offen lds\n"
"s_add_u32 m0, s65, 0x10400\n"
"s_nop 0\n"
"buffer_load_dwordx4 v130, s[4:7], s69 offen lds\n"
"s_add_u32 m0, s65, 0x10800\n"
"s_nop 0\n"
"buffer_load_dwordx4 v129, s[4:7], s70 offen lds\n"
"s_add_u32 m0, s65, 0x10c00\n"
"s_nop 0\n"
"buffer_load_dwordx4 v130, s[4:7], s71 offen lds\n"
"v_add_u32 v202, 0xd400, v131\n"
"v_add_u32 v203, 0x1a800, v131\n"
"v_add_u32 v204, 0x27c00, v131\n"
"s_add_u32 m0, s65, 0x18000\n"
"s_nop 0\n"
"buffer_load_dwordx4 v131, s[8:11], s72 offen lds\n"
"buffer_load_dwordx4 v202, s[8:11], s72 offen offset:1024 lds\n"
"buffer_load_dwordx4 v203, s[8:11], s72 offen offset:2048 lds\n"
"buffer_load_dwordx4 v204, s[8:11], s72 offen offset:3072 lds\n"
"s_add_u32 s72, s72, 0x36000\n"
"v_and_b32 v232, 31, v253\n"
"v_lshrrev_b32 v233, 5, v253\n"
"v_bfe_u32 v234, v232, 1, 3\n"
"v_xor_b32 v234, v234, v233\n"
"v_lshlrev_b32 v234, 4, v234\n"
"v_and_b32 v235, 7, v232\n"
"v_lshl_add_u32 v234, v235, 7, v234\n"
"v_lshrrev_b32 v235, 3, v232\n"
"v_lshl_add_u32 v236, v235, 10, v234\n"
"v_add_u32 v236, s65, v236\n"
"v_add_u32 v133, 0x8000, v236\n"
"v_lshl_add_u32 v236, v235, 12, v234\n"
"v_add_u32 v137, 0x18000, v236\n"
"v_xor_b32 v134, 32, v133\n"
"v_xor_b32 v138, 32, v137\n"
"v_xor_b32 v135, 64, v133\n"
"v_xor_b32 v139, 64, v137\n"
"v_xor_b32 v136, 0x60, v133\n"
"v_xor_b32 v140, 0x60, v137\n"
"v_bfe_u32 v234, v253, 4, 1\n"
"v_lshlrev_b32 v234, 1, v234\n"
"v_or_b32 v235, v234, v233\n"
"v_lshlrev_b32 v235, 12, v235\n"
"v_bfe_u32 v236, v253, 2, 2\n"
"v_lshl_add_u32 v235, v236, 7, v235\n"
"v_lshrrev_b32 v236, 1, v236\n"
"v_or_b32 v236, v236, v234\n"
"v_lshl_add_u32 v235, v236, 5, v235\n"
"v_and_b32 v236, 3, v253\n"
"v_lshl_add_u32 v235, v236, 3, v235\n"
"v_add_u32 v141, 0x18000, v235\n"
"v_xor_b32 v142, 32, v141\n"
"v_xor_b32 v143, 64, v141\n"
"v_xor_b32 v144, 0x60, v141\n"
"v_and_b32 v238, 31, v253\n"
"s_lshl_b32 s80, s64, 1\n"
"v_lshrrev_b32 v239, 5, v253\n"
"v_add_u32 v239, s80, v239\n"
"v_lshlrev_b32 v215, 3, v238\n"
"v_add_u32 v215, 0x14000, v215\n"
"v_lshl_add_u32 v214, v239, 8, v215\n"
"v_lshlrev_b32 v240, 4, v238\n"
"v_cmp_eq_u32 vcc, 7, v239\n"
"v_mov_b32 v241, 0x40000000\n"
"s_mul_i32 s80, s66, 0x2800\n"
"v_add_u32 v242, s80, v240\n"
"v_cndmask_b32 v216, v241, v242, vcc\n"
"v_mul_u32_u24 v242, 0x5000, v239\n"
"v_add_u32 v242, v242, v240\n"
"v_lshlrev_b32 v239, 1, v239\n"
"v_cmp_gt_u32 vcc, s66, v239\n"
"v_add_u32 v239, 1, v239\n"
"v_add_u32 v240, 0x2800, v242\n"
"v_cndmask_b32 v217, v241, v242, vcc\n"
"v_cndmask_b32 v220, 0, -1, vcc\n"
"v_cmp_gt_u32 vcc, s66, v239\n"
"v_mov_b32 v242, 0\n"
"v_mov_b32 v243, 0\n"
"v_cndmask_b32 v218, v241, v240, vcc\n"
"v_cndmask_b32 v222, 0, -1, vcc\n"
"s_lshl_b32 s80, s64, 10\n"
"s_add_u32 s80, s80, 0x17000\n"
"v_lshrrev_b32 v240, 5, v253\n"
"v_lshl_add_u32 v240, v238, 1, v240\n"
"v_lshl_add_u32 v227, v240, 4, s80\n"
"v_lshrrev_b32 v240, 4, v253\n"
"v_bfe_u32 v241, v253, 2, 2\n"
"v_lshl_add_u32 v240, v240, 3, v241\n"
"v_and_b32 v241, 1, v253\n"
"v_lshl_add_u32 v240, v240, 1, v241\n"
"v_bfe_u32 v241, v253, 1, 1\n"
"v_lshl_add_u32 v240, v240, 1, v241\n"
"v_lshl_add_u32 v228, v240, 3, s80\n"
"v_lshlrev_b32 v240, 3, v254\n"
"v_add_u32 v240, 0x14000, v240\n"
"ds_write_b64 v240, v[242:243]\n"
"v_cmp_gt_u32 vcc, 32, v253\n"
"v_mov_b32 v150, 0\n"
"v_mov_b32 v151, 0\n"
"v_cndmask_b32 v246, 0, -1.0, vcc\n"
"v_mov_b32 v172, 0\n"
"v_mov_b32 v173, 0\n"
"v_bfe_u32 v240, v253, 4, 1\n"
"v_mul_u32_u24 v240, 0x17800, v240\n"
"v_lshrrev_b32 v241, 5, v253\n"
"v_lshl_add_u32 v240, v241, 8, v240\n"
"v_and_b32 v241, 15, v253\n"
"v_lshl_add_u32 v145, v241, 4, v240\n"
"s_lshl_b32 s80, s66, 2\n"
"s_add_u32 s80, s80, s64\n"
"s_mul_i32 s80, s80, 0x2f000\n"
"s_lshl_b32 s81, s95, 11\n"
"s_add_u32 s80, s80, s81\n"
"s_add_u32 s75, s80, 0x300000\n"
"v_bfe_u32 v240, v253, 2, 1\n"
"v_lshlrev_b32 v240, 4, v240\n"
"v_and_b32 v241, 3, v253\n"
"v_add_u32 v240, v240, v241\n"
"v_bfe_u32 v241, v253, 3, 2\n"
"v_lshl_add_u32 v240, v241, 2, v240\n"
"s_lshl_b32 s80, s66, 2\n"
"s_add_u32 s80, s80, s64\n"
"s_lshl_b32 s80, s80, 9\n"
"s_add_u32 s80, s80, 0x50000\n"
"v_lshl_add_u32 v247, v240, 4, s80\n"
"s_lshl_b32 s80, s64, 7\n"
"s_add_u32 s80, s80, 0x5000\n"
"v_lshl_add_u32 v250, v240, 2, s80\n"
"v_lshrrev_b32 v241, 5, v253\n"
"v_lshl_add_u32 v251, v241, 6, s80\n"
"v_and_b32 v241, 31, v253\n"
"v_lshrrev_b32 v240, 5, v253\n"
"v_lshl_add_u32 v240, v241, 1, v240\n"
"v_lshlrev_b32 v248, 4, v240\n"
"v_lshlrev_b32 v249, 4, v241\n"
"v_add_u32 v248, 0x8000, v248\n"
"v_add_u32 v249, 0xec00, v249\n"
"s_cmp_ge_u32 s64, 2\n"
"s_cselect_b32 s32, s36, s32\n"
"s_cselect_b32 s33, s37, s33\n"
"s_waitcnt vmcnt(14)\n"
"v_readfirstlane_b32 s76, v230\n"
"v_readfirstlane_b32 s77, v231\n"
"s_and_b32 s76, s76, 0xfffffffc\n"
"s_add_u32 s76, s76, 4\n"
"s_addc_u32 s77, s77, 0\n"
"s_mul_i32 s78, s76, 3\n"
"s_add_u32 s78, s78, s77\n"
"v_mov_b32 v230, s76\n"
"v_mov_b32 v231, s77\n"
"v_cmp_eq_u32 vcc, 0, v253\n"
"v_mov_b32 v229, 0x40000000\n"
"s_nop 0\n"
"v_cndmask_b32 v229, v229, 0, vcc\n"
"v_mov_b32 v232, 0\n"
"v_mov_b32 v234, 0\n"
"s_add_u32 m0, s65, 0x7f80\n"
"s_waitcnt vmcnt(10)\n"
"v_dot2c_f32_bf16 v232, 0x3f803f80, v96\n"
"buffer_load_dwordx4 v129, s[4:7], s68 offen offset:128 lds\n"
"v_dot2c_f32_bf16 v234, v96, v96\n"
"v_dot2c_f32_bf16 v232, 0x3f803f80, v97\n"
"v_dot2c_f32_bf16 v234, v97, v97\n"
"v_dot2c_f32_bf16 v232, 0x3f803f80, v98\n"
"v_dot2c_f32_bf16 v234, v98, v98\n"
"s_add_u32 m0, s65, 0x8380\n"
"v_dot2c_f32_bf16 v232, 0x3f803f80, v99\n"
"buffer_load_dwordx4 v130, s[4:7], s69 offen offset:128 lds\n"
"v_dot2c_f32_bf16 v234, v99, v99\n"
"s_waitcnt vmcnt(11)\n"
"v_dot2c_f32_bf16 v232, 0x3f803f80, v100\n"
"v_dot2c_f32_bf16 v234, v100, v100\n"
"v_dot2c_f32_bf16 v232, 0x3f803f80, v101\n"
"v_dot2c_f32_bf16 v234, v101, v101\n"
"s_add_u32 m0, s65, 0x8780\n"
"v_dot2c_f32_bf16 v232, 0x3f803f80, v102\n"
"buffer_load_dwordx4 v129, s[4:7], s70 offen offset:128 lds\n"
"v_dot2c_f32_bf16 v234, v102, v102\n"
"v_dot2c_f32_bf16 v232, 0x3f803f80, v103\n"
"v_dot2c_f32_bf16 v234, v103, v103\n"
"s_waitcnt vmcnt(11)\n"
"v_dot2c_f32_bf16 v232, 0x3f803f80, v104\n"
"v_dot2c_f32_bf16 v234, v104, v104\n"
"s_add_u32 m0, s65, 0x8b80\n"
"v_dot2c_f32_bf16 v232, 0x3f803f80, v105\n"
"buffer_load_dwordx4 v130, s[4:7], s71 offen offset:128 lds\n"
"v_dot2c_f32_bf16 v234, v105, v105\n"
"s_nop 0\n"
"v_add_f32_dpp v232, v232, v232 row_shr:1 row_mask:0xf bank_mask:0xf bound_ctrl:1\n"
"s_nop 0\n"
"v_add_f32_dpp v234, v234, v234 row_shr:1 row_mask:0xf bank_mask:0xf bound_ctrl:1\n"
"s_lshl_b32 s80, s66, 4\n"
"s_add_u32 s80, s80, s67\n"
"s_lshl_b32 s80, s80, 2\n"
"s_add_u32 m0, s65, 0x1c000\n"
"s_nop 0\n"
"buffer_load_dwordx4 v131, s[8:11], s72 offen lds\n"
"s_add_u32 s80, s80, s64\n"
"s_lshl_b32 s80, s80, 5\n"
"v_lshrrev_b32 v236, 5, v253\n"
"v_lshl_add_u32 v236, v236, 4, s80\n"
"v_add_f32_dpp v232, v232, v232 row_shr:2 row_mask:0xf bank_mask:0xf bound_ctrl:1\n"
"v_add_f32_dpp v234, v234, v234 row_shr:2 row_mask:0xf bank_mask:0xf bound_ctrl:1\n"
"buffer_load_dwordx4 v202, s[8:11], s72 offen offset:1024 lds\n"
"v_and_b32 v235, 31, v253\n"
"v_add_f32_dpp v232, v232, v232 row_shr:4 row_mask:0xf bank_mask:0xf bound_ctrl:1\n"
"v_add_f32_dpp v234, v234, v234 row_shr:4 row_mask:0xf bank_mask:0xf bound_ctrl:1\n"
"v_cmp_eq_u32 vcc, 31, v235\n"
"v_add_f32_dpp v232, v232, v232 row_shr:8 row_mask:0xf bank_mask:0xf bound_ctrl:1\n"
"v_add_f32_dpp v234, v234, v234 row_shr:8 row_mask:0xf bank_mask:0xf bound_ctrl:1\n"
"buffer_load_dwordx4 v203, s[8:11], s72 offen offset:2048 lds\n"
"v_mov_b32 v235, 0x40000000\n"
"v_add_f32_dpp v232, v232, v232 row_bcast:15 row_mask:0xa bank_mask:0xf\n"
"v_add_f32_dpp v234, v234, v234 row_bcast:15 row_mask:0xa bank_mask:0xf\n"
"v_cndmask_b32 v236, v235, v236, vcc\n"
"v_mul_f32 v232, 0x3acccccd, v232\n"
"v_mul_f32 v234, 0x3acccccd, v234\n"
"buffer_load_dwordx4 v204, s[8:11], s72 offen offset:3072 lds\n"
"s_add_u32 s72, s72, 0x36000\n"
"v_fma_f32 v234, -v232, v232, v234\n"
"v_max_f32 v234, 0, v234\n"
"v_add_f32 v234, 0x3727c5ac, v234\n"
"v_rsq_f32 v234, v234\n"
"v_mov_b32 v233, s76\n"
"v_mov_b32 v235, s78\n"
"s_mov_b32 s80, 0x50000\n"
"buffer_store_dwordx4 v[232:235], v236, s[24:27], s80 offen sc1\n"
"v_and_b32 v219, s76, v220\n"
"v_and_b32 v220, s78, v220\n"
"v_and_b32 v221, s76, v222\n"
"v_and_b32 v222, s78, v222\n"
"s_waitcnt vmcnt(13)\n"
"s_waitcnt vmcnt(9)\n"
"s_waitcnt lgkmcnt(0)\n"
"s_barrier\n"
"ds_read_b128 a[0:3], v133 offset:32768\n"
"ds_read_b64_tr_b16 a[160:161], v141\n"
"ds_read_b64_tr_b16 a[162:163], v141 offset:512\n"
"ds_read_b64_tr_b16 a[164:165], v142\n"
"ds_read_b64_tr_b16 a[166:167], v142 offset:512\n"
"ds_read_b64_tr_b16 a[168:169], v143\n"
"ds_read_b64_tr_b16 a[170:171], v143 offset:512\n"
"ds_read_b64_tr_b16 a[172:173], v144\n"
"ds_read_b64_tr_b16 a[174:175], v144 offset:512\n"
"ds_read_b128 a[4:7], v134 offset:32768\n"
"ds_read_b64_tr_b16 a[176:177], v141 offset:1024\n"
"ds_read_b64_tr_b16 a[178:179], v141 offset:1536\n"
"ds_read_b64_tr_b16 a[180:181], v142 offset:1024\n"
"ds_read_b64_tr_b16 a[182:183], v142 offset:1536\n"
"ds_read_b64_tr_b16 a[184:185], v143 offset:1024\n"
"ds_read_b64_tr_b16 a[186:187], v143 offset:1536\n"
"ds_read_b64_tr_b16 a[188:189], v144 offset:1024\n"
"ds_read_b64_tr_b16 a[190:191], v144 offset:1536\n"
"s_waitcnt lgkmcnt(9)\n"
"v_mfma_f32_32x32x16_bf16 v[0:15], a[160:163], a[0:3], 0\n"
"s_add_u32 m0, s65, 0xbf00\n"
"ds_read_b128 a[8:11], v135 offset:32768\n"
"ds_read_b64_tr_b16 a[192:193], v141 offset:2048\n"
"ds_read_b64_tr_b16 a[194:195], v141 offset:2560\n"
"buffer_load_dwordx4 v129, s[4:7], s68 offen offset:256 lds\n"
"v_mfma_f32_32x32x16_bf16 v[16:31], a[164:167], a[0:3], 0\n"
"s_add_u32 m0, s65, 0xc300\n"
"ds_read_b64_tr_b16 a[196:197], v142 offset:2048\n"
"ds_read_b64_tr_b16 a[198:199], v142 offset:2560\n"
"buffer_load_dwordx4 v130, s[4:7], s69 offen offset:256 lds\n"
"v_lshlrev_b32 v124, 16, v124\n"
"v_mfma_f32_32x32x16_bf16 v[32:47], a[168:171], a[0:3], 0\n"
"s_add_u32 m0, s65, 0xc700\n"
"ds_read_b64_tr_b16 a[200:201], v143 offset:2048\n"
"ds_read_b64_tr_b16 a[202:203], v143 offset:2560\n"
"buffer_load_dwordx4 v129, s[4:7], s70 offen offset:256 lds\n"
"v_lshlrev_b32 v125, 16, v125\n"
"v_mfma_f32_32x32x16_bf16 v[48:63], a[172:175], a[0:3], 0\n"
"s_waitcnt lgkmcnt(7)\n"
"s_add_u32 m0, s65, 0xcb00\n"
"ds_read_b64_tr_b16 a[204:205], v144 offset:2048\n"
"ds_read_b64_tr_b16 a[206:207], v144 offset:2560\n"
"buffer_load_dwordx4 v130, s[4:7], s71 offen offset:256 lds\n"
"v_lshlrev_b32 v126, 16, v126\n"
"v_mfma_f32_32x32x16_bf16 v[0:15], a[176:179], a[4:7], v[0:15]\n"
"s_add_u32 m0, s65, 0x20000\n"
"ds_read_b128 a[12:15], v136 offset:32768\n"
"ds_read_b64_tr_b16 a[160:161], v141 offset:3072\n"
"ds_read_b64_tr_b16 a[162:163], v141 offset:3584\n"
"buffer_load_dwordx4 v131, s[8:11], s72 offen lds\n"
"v_mfma_f32_32x32x16_bf16 v[16:31], a[180:183], a[4:7], v[16:31]\n"
"ds_read_b64_tr_b16 a[164:165], v142 offset:3072\n"
"ds_read_b64_tr_b16 a[166:167], v142 offset:3584\n"
"buffer_load_dwordx4 v202, s[8:11], s72 offen offset:1024 lds\n"
"v_mul_f32 v126, 0x3fb8aa3b, v126\n"
"v_mfma_f32_32x32x16_bf16 v[32:47], a[184:187], a[4:7], v[32:47]\n"
"ds_read_b64_tr_b16 a[168:169], v143 offset:3072\n"
"ds_read_b64_tr_b16 a[170:171], v143 offset:3584\n"
"buffer_load_dwordx4 v203, s[8:11], s72 offen offset:2048 lds\n"
"v_mfma_f32_32x32x16_bf16 v[48:63], a[188:191], a[4:7], v[48:63]\n"
"s_waitcnt lgkmcnt(7)\n"
"ds_read_b64_tr_b16 a[172:173], v144 offset:3072\n"
"ds_read_b64_tr_b16 a[174:175], v144 offset:3584\n"
"buffer_load_dwordx4 v204, s[8:11], s72 offen offset:3072 lds\n"
"s_add_u32 s72, s72, 0x36000\n"
"s_waitcnt vmcnt(9)\n"
"s_waitcnt lgkmcnt(8)\n"
"s_barrier\n"
"v_mfma_f32_32x32x16_bf16 v[0:15], a[192:195], a[8:11], v[0:15]\n"
"s_add_u32 m0, s65, 0xfe80\n"
"ds_read_b128 a[16:19], v133\n"
"ds_read_b64_tr_b16 a[176:177], v141 offset:16384\n"
"ds_read_b64_tr_b16 a[178:179], v141 offset:16896\n"
"buffer_load_dwordx4 v129, s[4:7], s68 offen offset:384 lds\n"
"v_mfma_f32_32x32x16_bf16 v[16:31], a[196:199], a[8:11], v[16:31]\n"
"ds_read_b64_tr_b16 a[180:181], v142 offset:16384\n"
"ds_read_b64_tr_b16 a[182:183], v142 offset:16896\n"
"v_exp_f32 v126, v126\n"
"s_nop 0\n"
"v_cvt_pk_bf16_f32 v126, v126, v126\n"
"v_and_b32 v126, 0xffff0000, v126\n"
"v_mul_f32 v238, v126, v124\n"
"v_mul_f32 v239, v126, v125\n"
"v_mfma_f32_32x32x16_bf16 v[32:47], a[200:203], a[8:11], v[32:47]\n"
"s_add_u32 m0, s65, 0x10280\n"
"ds_read_b64_tr_b16 a[184:185], v143 offset:16384\n"
"ds_read_b64_tr_b16 a[186:187], v143 offset:16896\n"
"buffer_load_dwordx4 v130, s[4:7], s69 offen offset:384 lds\n"
"v_mul_f32 v238, 0x3fb8aa3b, v238\n"
"v_mfma_f32_32x32x16_bf16 v[48:63], a[204:207], a[8:11], v[48:63]\n"
"s_waitcnt lgkmcnt(7)\n"
"ds_read_b64_tr_b16 a[188:189], v144 offset:16384\n"
"ds_read_b64_tr_b16 a[190:191], v144 offset:16896\n"
"v_mul_f32 v239, 0x3e22f983, v239\n"
"v_mul_f32 v240, 1.0, v238\n"
"v_mul_f32 v241, 1.0, v239\n"
"v_exp_f32 v240, v240\n"
"v_fract_f32 v241, v241\n"
"v_mfma_f32_32x32x16_bf16 v[0:15], a[160:163], a[12:15], v[0:15]\n"
"s_add_u32 m0, s65, 0x10680\n"
"ds_read_b128 a[20:23], v134\n"
"ds_read_b64_tr_b16 a[192:193], v141 offset:17408\n"
"ds_read_b64_tr_b16 a[194:195], v141 offset:17920\n"
"buffer_load_dwordx4 v129, s[4:7], s70 offen offset:384 lds\n"
"v_mfma_f32_32x32x16_bf16 v[16:31], a[164:167], a[12:15], v[16:31]\n"
"ds_read_b64_tr_b16 a[196:197], v142 offset:17408\n"
"ds_read_b64_tr_b16 a[198:199], v142 offset:17920\n"
"v_cos_f32 v190, v241\n"
"v_sin_f32 v191, v241\n"
"v_mul_f32 v190, v190, v240\n"
"v_mul_f32 v191, v191, v240\n"
"v_mfma_f32_32x32x16_bf16 v[32:47], a[168:171], a[12:15], v[32:47]\n"
"s_add_u32 m0, s65, 0x10a80\n"
"ds_read_b64_tr_b16 a[200:201], v143 offset:17408\n"
"ds_read_b64_tr_b16 a[202:203], v143 offset:17920\n"
"buffer_load_dwordx4 v130, s[4:7], s71 offen offset:384 lds\n"
"v_mul_f32 v240, 0x41800000, v238\n"
"v_mfma_f32_32x32x16_bf16 v[48:63], a[172:175], a[12:15], v[48:63]\n"
"s_waitcnt lgkmcnt(7)\n"
"ds_read_b64_tr_b16 a[204:205], v144 offset:17408\n"
"ds_read_b64_tr_b16 a[206:207], v144 offset:17920\n"
"v_mul_f32 v241, 0x41800000, v239\n"
"v_exp_f32 v240, v240\n"
"v_fract_f32 v241, v241\n"
"v_cos_f32 v192, v241\n"
"v_mfma_f32_32x32x16_bf16 v[0:15], a[176:179], a[16:19], v[0:15]\n"
"s_add_u32 m0, s65, 0x24000\n"
"ds_read_b128 a[24:27], v135\n"
"ds_read_b64_tr_b16 a[160:161], v141 offset:18432\n"
"ds_read_b64_tr_b16 a[162:163], v141 offset:18944\n"
"buffer_load_dwordx4 v131, s[8:11], s72 offen lds\n"
"v_mfma_f32_32x32x16_bf16 v[16:31], a[180:183], a[16:19], v[16:31]\n"
"s_lshl_b32 s80, s64, 1\n"
"ds_read_b64_tr_b16 a[164:165], v142 offset:18432\n"
"ds_read_b64_tr_b16 a[166:167], v142 offset:18944\n"
"v_sin_f32 v193, v241\n"
"v_mul_f32 v192, v192, v240\n"
"v_mul_f32 v193, v193, v240\n"
"v_lshrrev_b32 v243, 5, v253\n"
"v_mfma_f32_32x32x16_bf16 v[32:47], a[184:187], a[16:19], v[32:47]\n"
"ds_read_b64_tr_b16 a[168:169], v143 offset:18432\n"
"ds_read_b64_tr_b16 a[170:171], v143 offset:18944\n"
"buffer_load_dwordx4 v202, s[8:11], s72 offen offset:1024 lds\n"
"v_add_u32 v243, s80, v243\n"
"v_mfma_f32_32x32x16_bf16 v[48:63], a[188:191], a[16:19], v[48:63]\n"
"s_waitcnt lgkmcnt(7)\n"
"ds_read_b64_tr_b16 a[172:173], v144 offset:18432\n"
"ds_read_b64_tr_b16 a[174:175], v144 offset:18944\n"
"v_lshlrev_b32 v242, 4, v243\n"
"v_cvt_f32_u32 v242, v242\n"
"v_mul_f32 v240, v242, v238\n"
"v_mul_f32 v241, v242, v239\n"
"v_exp_f32 v240, v240\n"
"v_mfma_f32_32x32x16_bf16 v[0:15], a[192:195], a[20:23], v[0:15]\n"
"ds_read_b128 a[28:31], v136\n"
"ds_read_b64_tr_b16 a[176:177], v141 offset:19456\n"
"ds_read_b64_tr_b16 a[178:179], v141 offset:19968\n"
"buffer_load_dwordx4 v203, s[8:11], s72 offen offset:2048 lds\n"
"v_mfma_f32_32x32x16_bf16 v[16:31], a[196:199], a[20:23], v[16:31]\n"
"ds_read_b64_tr_b16 a[180:181], v142 offset:19456\n"
"ds_read_b64_tr_b16 a[182:183], v142 offset:19968\n"
"v_fract_f32 v241, v241\n"
"v_cos_f32 v194, v241\n"
"v_sin_f32 v195, v241\n"
"v_mul_f32 v194, v194, v240\n"
"v_mfma_f32_32x32x16_bf16 v[32:47], a[200:203], a[20:23], v[32:47]\n"
"ds_read_b64_tr_b16 a[184:185], v143 offset:19456\n"
"ds_read_b64_tr_b16 a[186:187], v143 offset:19968\n"
"buffer_load_dwordx4 v204, s[8:11], s72 offen offset:3072 lds\n"
"s_add_u32 s72, s72, 0x36000\n"
"v_mfma_f32_32x32x16_bf16 v[48:63], a[204:207], a[20:23], v[48:63]\n"
"s_waitcnt lgkmcnt(7)\n"
"ds_read_b64_tr_b16 a[188:189], v144 offset:19456\n"
"ds_read_b64_tr_b16 a[190:191], v144 offset:19968\n"
"v_mul_f32 v195, v195, v240\n"
"s_sub_u32 s80, s66, 1\n"
"v_lshlrev_b32 v242, 1, v243\n"
"v_sub_u32 v242, s80, v242\n"
"v_max_i32 v242, 0, v242\n"
"v_lshlrev_b32 v242, 7, v242\n"
"s_waitcnt vmcnt(8)\n"
"s_waitcnt lgkmcnt(8)\n"
"s_barrier\n"
"v_mfma_f32_32x32x16_bf16 v[0:15], a[160:163], a[24:27], v[0:15]\n"
"s_add_u32 m0, s65, 0x7e00\n"
"ds_read_b128 a[32:35], v133 offset:16384\n"
"ds_read_b64_tr_b16 a[192:193], v141 offset:32768\n"
"ds_read_b64_tr_b16 a[194:195], v141 offset:33280\n"
"buffer_load_dwordx4 v129, s[4:7], s68 offen offset:512 lds\n"
"v_mfma_f32_32x32x16_bf16 v[16:31], a[164:167], a[24:27], v[16:31]\n"
"ds_read_b64_tr_b16 a[196:197], v142 offset:32768\n"
"ds_read_b64_tr_b16 a[198:199], v142 offset:33280\n"
"v_cvt_f32_u32 v242, v242\n"
"v_mul_f32 v240, v242, v238\n"
"v_mul_f32 v241, v242, v239\n"
"v_exp_f32 v240, v240\n"
"v_fract_f32 v241, v241\n"
"v_mfma_f32_32x32x16_bf16 v[32:47], a[168:171], a[24:27], v[32:47]\n"
"s_add_u32 m0, s65, 0x8200\n"
"ds_read_b64_tr_b16 a[200:201], v143 offset:32768\n"
"ds_read_b64_tr_b16 a[202:203], v143 offset:33280\n"
"buffer_load_dwordx4 v130, s[4:7], s69 offen offset:512 lds\n"
"v_mfma_f32_32x32x16_bf16 v[48:63], a[172:175], a[24:27], v[48:63]\n"
"s_waitcnt lgkmcnt(7)\n"
"ds_read_b64_tr_b16 a[204:205], v144 offset:32768\n"
"ds_read_b64_tr_b16 a[206:207], v144 offset:33280\n"
"v_cos_f32 v196, v241\n"
"v_sin_f32 v197, v241\n"
"v_mul_f32 v196, v196, v240\n"
"v_mul_f32 v197, v197, v240\n"
"v_mfma_f32_32x32x16_bf16 v[0:15], a[176:179], a[28:31], v[0:15]\n"
"s_add_u32 m0, s65, 0x8600\n"
"ds_read_b128 a[36:39], v134 offset:16384\n"
"ds_read_b64_tr_b16 a[160:161], v141 offset:33792\n"
"ds_read_b64_tr_b16 a[162:163], v141 offset:34304\n"
"buffer_load_dwordx4 v129, s[4:7], s70 offen offset:512 lds\n"
"v_mfma_f32_32x32x16_bf16 v[16:31], a[180:183], a[28:31], v[16:31]\n"
"s_sub_u32 s80, s66, 2\n"
"ds_read_b64_tr_b16 a[164:165], v142 offset:33792\n"
"ds_read_b64_tr_b16 a[166:167], v142 offset:34304\n"
"v_lshlrev_b32 v242, 1, v243\n"
"v_sub_u32 v242, s80, v242\n"
"v_max_i32 v242, 0, v242\n"
"v_lshlrev_b32 v242, 7, v242\n"
"v_cvt_f32_u32 v242, v242\n"
"v_mfma_f32_32x32x16_bf16 v[32:47], a[184:187], a[28:31], v[32:47]\n"
"s_add_u32 m0, s65, 0x8a00\n"
"ds_read_b64_tr_b16 a[168:169], v143 offset:33792\n"
"ds_read_b64_tr_b16 a[170:171], v143 offset:34304\n"
"buffer_load_dwordx4 v130, s[4:7], s71 offen offset:512 lds\n"
"v_mul_f32 v240, v242, v238\n"
"v_mfma_f32_32x32x16_bf16 v[48:63], a[188:191], a[28:31], v[48:63]\n"
"s_waitcnt lgkmcnt(7)\n"
"ds_read_b64_tr_b16 a[172:173], v144 offset:33792\n"
"ds_read_b64_tr_b16 a[174:175], v144 offset:34304\n"
"v_mul_f32 v241, v242, v239\n"
"v_exp_f32 v240, v240\n"
"v_fract_f32 v241, v241\n"
"v_cos_f32 v198, v241\n"
"v_mfma_f32_32x32x16_bf16 v[0:15], a[192:195], a[32:35], v[0:15]\n"
"s_add_u32 m0, s65, 0x18000\n"
"ds_read_b128 a[40:43], v135 offset:16384\n"
"ds_read_b64_tr_b16 a[176:177], v141 offset:34816\n"
"ds_read_b64_tr_b16 a[178:179], v141 offset:35328\n"
"buffer_load_dwordx4 v131, s[8:11], s72 offen lds\n"
"v_mfma_f32_32x32x16_bf16 v[16:31], a[196:199], a[32:35], v[16:31]\n"
"ds_read_b64_tr_b16 a[180:181], v142 offset:34816\n"
"ds_read_b64_tr_b16 a[182:183], v142 offset:35328\n"
"v_sin_f32 v199, v241\n"
"v_mul_f32 v198, v198, v240\n"
"v_mul_f32 v199, v199, v240\n"
"v_add_f32 v240, -1.0, v190\n"
"v_mul_f32 v241, v124, v124\n"
"v_mfma_f32_32x32x16_bf16 v[32:47], a[200:203], a[32:35], v[32:47]\n"
"ds_read_b64_tr_b16 a[184:185], v143 offset:34816\n"
"ds_read_b64_tr_b16 a[186:187], v143 offset:35328\n"
"buffer_load_dwordx4 v202, s[8:11], s72 offen offset:1024 lds\n"
"v_fmac_f32 v241, v125, v125\n"
"v_mfma_f32_32x32x16_bf16 v[48:63], a[204:207], a[32:35], v[48:63]\n"
"s_waitcnt lgkmcnt(7)\n"
"ds_read_b64_tr_b16 a[188:189], v144 offset:34816\n"
"ds_read_b64_tr_b16 a[190:191], v144 offset:35328\n"
"v_rcp_f32 v241, v241\n"
"v_mul_f32 v200, v240, v124\n"
"v_fmac_f32 v200, v191, v125\n"
"v_mul_f32 v201, v191, v124\n"
"v_add_f32 v241, v241, v241\n"
"v_mfma_f32_32x32x16_bf16 v[0:15], a[160:163], a[36:39], v[0:15]\n"
"ds_read_b128 a[44:47], v136 offset:16384\n"
"ds_read_b64_tr_b16 a[192:193], v141 offset:35840\n"
"ds_read_b64_tr_b16 a[194:195], v141 offset:36352\n"
"buffer_load_dwordx4 v203, s[8:11], s72 offen offset:2048 lds\n"
"v_mfma_f32_32x32x16_bf16 v[16:31], a[164:167], a[36:39], v[16:31]\n"
"s_mul_i32 s80, s64, 0xb0\n"
"ds_read_b64_tr_b16 a[196:197], v142 offset:35840\n"
"ds_read_b64_tr_b16 a[198:199], v142 offset:36352\n"
"v_fma_f32 v201, -v240, v125, v201\n"
"v_mul_f32 v200, v200, v241\n"
"v_mul_f32 v201, v201, v241\n"
"s_add_u32 s80, s80, s2\n"
"s_mul_i32 s81, s80, 0x199a\n"
"v_mfma_f32_32x32x16_bf16 v[32:47], a[168:171], a[36:39], v[32:47]\n"
"ds_read_b64_tr_b16 a[200:201], v143 offset:35840\n"
"ds_read_b64_tr_b16 a[202:203], v143 offset:36352\n"
"buffer_load_dwordx4 v204, s[8:11], s72 offen offset:3072 lds\n"
"s_add_u32 s72, s72, 0x36000\n"
"v_mfma_f32_32x32x16_bf16 v[48:63], a[172:175], a[36:39], v[48:63]\n"
"s_waitcnt lgkmcnt(7)\n"
"ds_read_b64_tr_b16 a[204:205], v144 offset:35840\n"
"ds_read_b64_tr_b16 a[206:207], v144 offset:36352\n"
"s_lshr_b32 s81, s81, 16\n"
"s_mul_i32 s82, s81, 10\n"
"s_sub_u32 s82, s80, s82\n"
"s_mul_i32 s83, s81, 0xa000\n"
"s_lshl_b32 s81, s82, 12\n"
"s_add_u32 s79, s83, s81\n"
"s_waitcnt vmcnt(8)\n"
"s_waitcnt lgkmcnt(8)\n"
"s_barrier\n"
"v_mfma_f32_32x32x16_bf16 v[0:15], a[176:179], a[40:43], v[0:15]\n"
"s_add_u32 m0, s65, 0xbd80\n"
"ds_read_b128 a[48:51], v133 offset:32768\n"
"ds_read_b64_tr_b16 a[160:161], v141 offset:49152\n"
"ds_read_b64_tr_b16 a[162:163], v141 offset:49664\n"
"buffer_load_dwordx4 v129, s[4:7], s68 offen offset:640 lds\n"
"v_mfma_f32_32x32x16_bf16 v[16:31], a[180:183], a[40:43], v[16:31]\n"
"s_add_u32 s79, s79, 0x80000\n"
"ds_read_b64_tr_b16 a[164:165], v142 offset:49152\n"
"ds_read_b64_tr_b16 a[166:167], v142 offset:49664\n"
"s_lshl_b32 s81, s82, 7\n"
"s_add_u32 s83, s83, s81\n"
"s_cmp_lt_u32 s80, 0x21c\n"
"s_cselect_b32 s83, s83, 0x7000000\n"
"s_cselect_b32 s79, s79, 0x7000000\n"
"v_mfma_f32_32x32x16_bf16 v[32:47], a[184:187], a[40:43], v[32:47]\n"
"s_add_u32 m0, s65, 0xc180\n"
"ds_read_b64_tr_b16 a[168:169], v143 offset:49152\n"
"ds_read_b64_tr_b16 a[170:171], v143 offset:49664\n"
"buffer_load_dwordx4 v130, s[4:7], s69 offen offset:640 lds\n"
"v_lshrrev_b32 v238, 3, v253\n"
"v_mfma_f32_32x32x16_bf16 v[48:63], a[188:191], a[40:43], v[48:63]\n"
"s_waitcnt lgkmcnt(7)\n"
"ds_read_b64_tr_b16 a[172:173], v144 offset:49152\n"
"ds_read_b64_tr_b16 a[174:175], v144 offset:49664\n"
"v_mul_u32_u24 v238, 0x500, v238\n"
"v_and_b32 v239, 7, v253\n"
"v_lshl_add_u32 v238, v239, 4, v238\n"
"s_and_b32 s80, s64, 1\n"
"s_lshl_b32 s81, s2, 1\n"
"s_add_u32 s80, s80, s81\n"
"s_mul_i32 s80, s80, 0x980\n"
"v_cmp_gt_u32 vcc, 19, v253\n"
"v_lshl_add_u32 v232, v253, 7, s80\n"
"v_mov_b32 v233, 0x40000000\n"
"v_cndmask_b32 v232, v233, v232, vcc\n"
"v_mfma_f32_32x32x16_bf16 v[0:15], a[192:195], a[44:47], v[0:15]\n"
"s_add_u32 m0, s65, 0xc580\n"
"ds_read_b128 a[52:55], v134 offset:32768\n"
"ds_read_b64_tr_b16 a[176:177], v141 offset:50176\n"
"ds_read_b64_tr_b16 a[178:179], v141 offset:50688\n"
"buffer_load_dwordx4 v129, s[4:7], s70 offen offset:640 lds\n"
"v_mfma_f32_32x32x16_bf16 v[16:31], a[196:199], a[44:47], v[16:31]\n"
"ds_read_b64_tr_b16 a[180:181], v142 offset:50176\n"
"ds_read_b64_tr_b16 a[182:183], v142 offset:50688\n"
"v_mfma_f32_32x32x16_bf16 v[32:47], a[200:203], a[44:47], v[32:47]\n"
"s_add_u32 m0, s65, 0xc980\n"
"ds_read_b64_tr_b16 a[184:185], v143 offset:50176\n"
"ds_read_b64_tr_b16 a[186:187], v143 offset:50688\n"
"buffer_load_dwordx4 v130, s[4:7], s71 offen offset:640 lds\n"
"v_mfma_f32_32x32x16_bf16 v[48:63], a[204:207], a[44:47], v[48:63]\n"
"s_waitcnt lgkmcnt(7)\n"
"ds_read_b64_tr_b16 a[188:189], v144 offset:50176\n"
"ds_read_b64_tr_b16 a[190:191], v144 offset:50688\n"
"v_mfma_f32_32x32x16_bf16 v[0:15], a[160:163], a[48:51], v[0:15]\n"
"s_add_u32 m0, s65, 0x1c000\n"
"ds_read_b128 a[56:59], v135 offset:32768\n"
"ds_read_b64_tr_b16 a[192:193], v141 offset:51200\n"
"ds_read_b64_tr_b16 a[194:195], v141 offset:51712\n"
"buffer_load_dwordx4 v131, s[8:11], s72 offen lds\n"
"v_mfma_f32_32x32x16_bf16 v[16:31], a[164:167], a[48:51], v[16:31]\n"
"ds_read_b64_tr_b16 a[196:197], v142 offset:51200\n"
"ds_read_b64_tr_b16 a[198:199], v142 offset:51712\n"
"v_mfma_f32_32x32x16_bf16 v[32:47], a[168:171], a[48:51], v[32:47]\n"
"ds_read_b64_tr_b16 a[200:201], v143 offset:51200\n"
"ds_read_b64_tr_b16 a[202:203], v143 offset:51712\n"
"buffer_load_dwordx4 v202, s[8:11], s72 offen offset:1024 lds\n"
"v_mfma_f32_32x32x16_bf16 v[48:63], a[172:175], a[48:51], v[48:63]\n"
"s_waitcnt lgkmcnt(7)\n"
"ds_read_b64_tr_b16 a[204:205], v144 offset:51200\n"
"ds_read_b64_tr_b16 a[206:207], v144 offset:51712\n"
"v_mfma_f32_32x32x16_bf16 v[0:15], a[176:179], a[52:55], v[0:15]\n"
"ds_read_b128 a[60:63], v136 offset:32768\n"
"ds_read_b64_tr_b16 a[160:161], v141 offset:52224\n"
"ds_read_b64_tr_b16 a[162:163], v141 offset:52736\n"
"buffer_load_dwordx4 v203, s[8:11], s72 offen offset:2048 lds\n"
"v_mfma_f32_32x32x16_bf16 v[16:31], a[180:183], a[52:55], v[16:31]\n"
"ds_read_b64_tr_b16 a[164:165], v142 offset:52224\n"
"ds_read_b64_tr_b16 a[166:167], v142 offset:52736\n"
"v_mfma_f32_32x32x16_bf16 v[32:47], a[184:187], a[52:55], v[32:47]\n"
"ds_read_b64_tr_b16 a[168:169], v143 offset:52224\n"
"ds_read_b64_tr_b16 a[170:171], v143 offset:52736\n"
"buffer_load_dwordx4 v204, s[8:11], s72 offen offset:3072 lds\n"
"s_add_u32 s72, s72, 0x36000\n"
"v_mfma_f32_32x32x16_bf16 v[48:63], a[188:191], a[52:55], v[48:63]\n"
"s_waitcnt lgkmcnt(7)\n"
"ds_read_b64_tr_b16 a[172:173], v144 offset:52224\n"
"ds_read_b64_tr_b16 a[174:175], v144 offset:52736\n"
"s_waitcnt vmcnt(8)\n"
"s_waitcnt lgkmcnt(8)\n"
"s_barrier\n"
"v_mfma_f32_32x32x16_bf16 v[0:15], a[192:195], a[56:59], v[0:15]\n"
"s_add_u32 m0, s65, 0xfd00\n"
"ds_read_b128 a[64:67], v133\n"
"ds_read_b64_tr_b16 a[176:177], v141\n"
"ds_read_b64_tr_b16 a[178:179], v141 offset:512\n"
"buffer_load_dwordx4 v129, s[4:7], s68 offen offset:768 lds\n"
"v_mfma_f32_32x32x16_bf16 v[16:31], a[196:199], a[56:59], v[16:31]\n"
"ds_read_b64_tr_b16 a[180:181], v142\n"
"ds_read_b64_tr_b16 a[182:183], v142 offset:512\n"
"v_mfma_f32_32x32x16_bf16 v[32:47], a[200:203], a[56:59], v[32:47]\n"
"s_add_u32 m0, s65, 0x10100\n"
"ds_read_b64_tr_b16 a[184:185], v143\n"
"ds_read_b64_tr_b16 a[186:187], v143 offset:512\n"
"buffer_load_dwordx4 v130, s[4:7], s69 offen offset:768 lds\n"
"v_mfma_f32_32x32x16_bf16 v[48:63], a[204:207], a[56:59], v[48:63]\n"
"s_waitcnt lgkmcnt(7)\n"
"ds_read_b64_tr_b16 a[188:189], v144\n"
"ds_read_b64_tr_b16 a[190:191], v144 offset:512\n"
"v_mfma_f32_32x32x16_bf16 v[0:15], a[160:163], a[60:63], v[0:15]\n"
"s_add_u32 m0, s65, 0x10500\n"
"ds_read_b128 a[68:71], v134\n"
"ds_read_b64_tr_b16 a[192:193], v141 offset:1024\n"
"ds_read_b64_tr_b16 a[194:195], v141 offset:1536\n"
"buffer_load_dwordx4 v129, s[4:7], s70 offen offset:768 lds\n"
"v_mfma_f32_32x32x16_bf16 v[16:31], a[164:167], a[60:63], v[16:31]\n"
"ds_read_b64_tr_b16 a[196:197], v142 offset:1024\n"
"ds_read_b64_tr_b16 a[198:199], v142 offset:1536\n"
"v_mfma_f32_32x32x16_bf16 v[32:47], a[168:171], a[60:63], v[32:47]\n"
"s_add_u32 m0, s65, 0x10900\n"
"ds_read_b64_tr_b16 a[200:201], v143 offset:1024\n"
"ds_read_b64_tr_b16 a[202:203], v143 offset:1536\n"
"buffer_load_dwordx4 v130, s[4:7], s71 offen offset:768 lds\n"
"v_mfma_f32_32x32x16_bf16 v[48:63], a[172:175], a[60:63], v[48:63]\n"
"s_waitcnt lgkmcnt(7)\n"
"ds_read_b64_tr_b16 a[204:205], v144 offset:1024\n"
"ds_read_b64_tr_b16 a[206:207], v144 offset:1536\n"
"v_mfma_f32_32x32x16_bf16 v[0:15], a[176:179], a[64:67], v[0:15]\n"
"s_add_u32 m0, s65, 0x20000\n"
"ds_read_b128 a[72:75], v135\n"
"ds_read_b64_tr_b16 a[160:161], v141 offset:2048\n"
"ds_read_b64_tr_b16 a[162:163], v141 offset:2560\n"
"buffer_load_dwordx4 v131, s[8:11], s72 offen lds\n"
"v_mfma_f32_32x32x16_bf16 v[16:31], a[180:183], a[64:67], v[16:31]\n"
"ds_read_b64_tr_b16 a[164:165], v142 offset:2048\n"
"ds_read_b64_tr_b16 a[166:167], v142 offset:2560\n"
"v_mfma_f32_32x32x16_bf16 v[32:47], a[184:187], a[64:67], v[32:47]\n"
"ds_read_b64_tr_b16 a[168:169], v143 offset:2048\n"
"ds_read_b64_tr_b16 a[170:171], v143 offset:2560\n"
"buffer_load_dwordx4 v202, s[8:11], s72 offen offset:1024 lds\n"
"v_mfma_f32_32x32x16_bf16 v[48:63], a[188:191], a[64:67], v[48:63]\n"
"s_waitcnt lgkmcnt(7)\n"
"ds_read_b64_tr_b16 a[172:173], v144 offset:2048\n"
"ds_read_b64_tr_b16 a[174:175], v144 offset:2560\n"
"v_mfma_f32_32x32x16_bf16 v[0:15], a[192:195], a[68:71], v[0:15]\n"
"ds_read_b128 a[76:79], v136\n"
"ds_read_b64_tr_b16 a[176:177], v141 offset:3072\n"
"ds_read_b64_tr_b16 a[178:179], v141 offset:3584\n"
"buffer_load_dwordx4 v203, s[8:11], s72 offen offset:2048 lds\n"
"v_mfma_f32_32x32x16_bf16 v[16:31], a[196:199], a[68:71], v[16:31]\n"
"ds_read_b64_tr_b16 a[180:181], v142 offset:3072\n"
"ds_read_b64_tr_b16 a[182:183], v142 offset:3584\n"
"v_mfma_f32_32x32x16_bf16 v[32:47], a[200:203], a[68:71], v[32:47]\n"
"ds_read_b64_tr_b16 a[184:185], v143 offset:3072\n"
"ds_read_b64_tr_b16 a[186:187], v143 offset:3584\n"
"buffer_load_dwordx4 v204, s[8:11], s72 offen offset:3072 lds\n"
"s_add_u32 s72, s72, 0x36000\n"
"v_mfma_f32_32x32x16_bf16 v[48:63], a[204:207], a[68:71], v[48:63]\n"
"s_waitcnt lgkmcnt(7)\n"
"ds_read_b64_tr_b16 a[188:189], v144 offset:3072\n"
"ds_read_b64_tr_b16 a[190:191], v144 offset:3584\n"
"s_waitcnt vmcnt(8)\n"
"s_waitcnt lgkmcnt(8)\n"
"s_barrier\n"
"v_mfma_f32_32x32x16_bf16 v[0:15], a[160:163], a[72:75], v[0:15]\n"
"s_add_u32 m0, s65, 0x7c80\n"
"ds_read_b128 a[80:83], v133 offset:16384\n"
"ds_read_b64_tr_b16 a[192:193], v141 offset:16384\n"
"ds_read_b64_tr_b16 a[194:195], v141 offset:16896\n"
"buffer_load_dwordx4 v129, s[4:7], s68 offen offset:896 lds\n"
"v_mfma_f32_32x32x16_bf16 v[16:31], a[164:167], a[72:75], v[16:31]\n"
"ds_read_b64_tr_b16 a[196:197], v142 offset:16384\n"
"ds_read_b64_tr_b16 a[198:199], v142 offset:16896\n"
"v_mfma_f32_32x32x16_bf16 v[32:47], a[168:171], a[72:75], v[32:47]\n"
"s_add_u32 m0, s65, 0x8080\n"
"ds_read_b64_tr_b16 a[200:201], v143 offset:16384\n"
"ds_read_b64_tr_b16 a[202:203], v143 offset:16896\n"
"buffer_load_dwordx4 v130, s[4:7], s69 offen offset:896 lds\n"
"v_mfma_f32_32x32x16_bf16 v[48:63], a[172:175], a[72:75], v[48:63]\n"
"s_waitcnt lgkmcnt(7)\n"
"ds_read_b64_tr_b16 a[204:205], v144 offset:16384\n"
"ds_read_b64_tr_b16 a[206:207], v144 offset:16896\n"
"v_mfma_f32_32x32x16_bf16 v[0:15], a[176:179], a[76:79], v[0:15]\n"
"s_add_u32 m0, s65, 0x8480\n"
"ds_read_b128 a[84:87], v134 offset:16384\n"
"ds_read_b64_tr_b16 a[160:161], v141 offset:17408\n"
"ds_read_b64_tr_b16 a[162:163], v141 offset:17920\n"
"buffer_load_dwordx4 v129, s[4:7], s70 offen offset:896 lds\n"
"v_mfma_f32_32x32x16_bf16 v[16:31], a[180:183], a[76:79], v[16:31]\n"
"ds_read_b64_tr_b16 a[164:165], v142 offset:17408\n"
"ds_read_b64_tr_b16 a[166:167], v142 offset:17920\n"
"v_mfma_f32_32x32x16_bf16 v[32:47], a[184:187], a[76:79], v[32:47]\n"
"s_add_u32 m0, s65, 0x8880\n"
"ds_read_b64_tr_b16 a[168:169], v143 offset:17408\n"
"ds_read_b64_tr_b16 a[170:171], v143 offset:17920\n"
"buffer_load_dwordx4 v130, s[4:7], s71 offen offset:896 lds\n"
"v_mfma_f32_32x32x16_bf16 v[48:63], a[188:191], a[76:79], v[48:63]\n"
"s_waitcnt lgkmcnt(7)\n"
"ds_read_b64_tr_b16 a[172:173], v144 offset:17408\n"
"ds_read_b64_tr_b16 a[174:175], v144 offset:17920\n"
"v_mfma_f32_32x32x16_bf16 v[0:15], a[192:195], a[80:83], v[0:15]\n"
"s_add_u32 m0, s65, 0x24000\n"
"ds_read_b128 a[88:91], v135 offset:16384\n"
"ds_read_b64_tr_b16 a[176:177], v141 offset:18432\n"
"ds_read_b64_tr_b16 a[178:179], v141 offset:18944\n"
"buffer_load_dwordx4 v131, s[8:11], s72 offen lds\n"
"s_lshr_b32 s82, s3, 3\n"
"s_lshl_b32 s82, s82, 2\n"
"s_add_u32 s82, s82, s64\n"
"s_mul_i32 s82, s82, 5\n"
"v_min_u32 v252, 4, v253\n"
"v_add_u32 v252, s82, v252\n"
"v_lshlrev_b32 v252, 7, v252\n"
"v_min_u32 v252, L%=_end-L%=_pc-8, v252\n"
"s_getpc_b64 s[80:81]\n"
"L%=_st_ct1:\n"
"s_sub_u32 s80, s80, L%=_st_ct1-L%=_pc\n"
"s_subb_u32 s81, s81, 0\n"
"global_load_dword v252, v252, s[80:81]\n"
"v_mfma_f32_32x32x16_bf16 v[16:31], a[196:199], a[80:83], v[16:31]\n"
"ds_read_b64_tr_b16 a[180:181], v142 offset:18432\n"
"ds_read_b64_tr_b16 a[182:183], v142 offset:18944\n"
"v_mfma_f32_32x32x16_bf16 v[32:47], a[200:203], a[80:83], v[32:47]\n"
"ds_read_b64_tr_b16 a[184:185], v143 offset:18432\n"
"ds_read_b64_tr_b16 a[186:187], v143 offset:18944\n"
"buffer_load_dwordx4 v202, s[8:11], s72 offen offset:1024 lds\n"
"v_mfma_f32_32x32x16_bf16 v[48:63], a[204:207], a[80:83], v[48:63]\n"
"s_waitcnt lgkmcnt(7)\n"
"ds_read_b64_tr_b16 a[188:189], v144 offset:18432\n"
"ds_read_b64_tr_b16 a[190:191], v144 offset:18944\n"
"v_mfma_f32_32x32x16_bf16 v[0:15], a[160:163], a[84:87], v[0:15]\n"
"ds_read_b128 a[92:95], v136 offset:16384\n"
"ds_read_b64_tr_b16 a[192:193], v141 offset:19456\n"
"ds_read_b64_tr_b16 a[194:195], v141 offset:19968\n"
"buffer_load_dwordx4 v203, s[8:11], s72 offen offset:2048 lds\n"
"v_mfma_f32_32x32x16_bf16 v[16:31], a[164:167], a[84:87], v[16:31]\n"
"ds_read_b64_tr_b16 a[196:197], v142 offset:19456\n"
"ds_read_b64_tr_b16 a[198:199], v142 offset:19968\n"
"v_mfma_f32_32x32x16_bf16 v[32:47], a[168:171], a[84:87], v[32:47]\n"
"ds_read_b64_tr_b16 a[200:201], v143 offset:19456\n"
"ds_read_b64_tr_b16 a[202:203], v143 offset:19968\n"
"buffer_load_dwordx4 v204, s[8:11], s72 offen offset:3072 lds\n"
"s_add_u32 s72, s72, 0x36000\n"
"v_mfma_f32_32x32x16_bf16 v[48:63], a[172:175], a[84:87], v[48:63]\n"
"s_waitcnt lgkmcnt(7)\n"
"ds_read_b64_tr_b16 a[204:205], v144 offset:19456\n"
"ds_read_b64_tr_b16 a[206:207], v144 offset:19968\n"
"s_waitcnt vmcnt(9)\n"
"s_waitcnt lgkmcnt(8)\n"
"s_barrier\n"
"v_mfma_f32_32x32x16_bf16 v[0:15], a[176:179], a[88:91], v[0:15]\n"
"s_add_u32 m0, s65, 0xbc00\n"
"ds_read_b128 a[96:99], v133 offset:32768\n"
"ds_read_b64_tr_b16 a[160:161], v141 offset:32768\n"
"ds_read_b64_tr_b16 a[162:163], v141 offset:33280\n"
"buffer_load_dwordx4 v129, s[4:7], s68 offen offset:1024 lds\n"
"v_mfma_f32_32x32x16_bf16 v[16:31], a[180:183], a[88:91], v[16:31]\n"
"ds_read_b64_tr_b16 a[164:165], v142 offset:32768\n"
"ds_read_b64_tr_b16 a[166:167], v142 offset:33280\n"
"v_mfma_f32_32x32x16_bf16 v[32:47], a[184:187], a[88:91], v[32:47]\n"
"s_add_u32 m0, s65, 0xc000\n"
"ds_read_b64_tr_b16 a[168:169], v143 offset:32768\n"
"ds_read_b64_tr_b16 a[170:171], v143 offset:33280\n"
"buffer_load_dwordx4 v130, s[4:7], s69 offen offset:1024 lds\n"
"v_mfma_f32_32x32x16_bf16 v[48:63], a[188:191], a[88:91], v[48:63]\n"
"s_waitcnt lgkmcnt(7)\n"
"ds_read_b64_tr_b16 a[172:173], v144 offset:32768\n"
"ds_read_b64_tr_b16 a[174:175], v144 offset:33280\n"
"v_mfma_f32_32x32x16_bf16 v[0:15], a[192:195], a[92:95], v[0:15]\n"
"s_add_u32 m0, s65, 0xc400\n"
"ds_read_b128 a[100:103], v134 offset:32768\n"
"ds_read_b64_tr_b16 a[176:177], v141 offset:33792\n"
"ds_read_b64_tr_b16 a[178:179], v141 offset:34304\n"
"buffer_load_dwordx4 v129, s[4:7], s70 offen offset:1024 lds\n"
"v_mfma_f32_32x32x16_bf16 v[16:31], a[196:199], a[92:95], v[16:31]\n"
"ds_read_b64_tr_b16 a[180:181], v142 offset:33792\n"
"ds_read_b64_tr_b16 a[182:183], v142 offset:34304\n"
"v_mfma_f32_32x32x16_bf16 v[32:47], a[200:203], a[92:95], v[32:47]\n"
"s_add_u32 m0, s65, 0xc800\n"
"ds_read_b64_tr_b16 a[184:185], v143 offset:33792\n"
"ds_read_b64_tr_b16 a[186:187], v143 offset:34304\n"
"buffer_load_dwordx4 v130, s[4:7], s71 offen offset:1024 lds\n"
"v_mfma_f32_32x32x16_bf16 v[48:63], a[204:207], a[92:95], v[48:63]\n"
"s_waitcnt lgkmcnt(7)\n"
"ds_read_b64_tr_b16 a[188:189], v144 offset:33792\n"
"ds_read_b64_tr_b16 a[190:191], v144 offset:34304\n"
"v_mfma_f32_32x32x16_bf16 v[0:15], a[160:163], a[96:99], v[0:15]\n"
"s_add_u32 m0, s65, 0x18000\n"
"ds_read_b128 a[104:107], v135 offset:32768\n"
"ds_read_b64_tr_b16 a[192:193], v141 offset:34816\n"
"ds_read_b64_tr_b16 a[194:195], v141 offset:35328\n"
"buffer_load_dwordx4 v131, s[8:11], s72 offen lds\n"
"v_mfma_f32_32x32x16_bf16 v[16:31], a[164:167], a[96:99], v[16:31]\n"
"ds_read_b64_tr_b16 a[196:197], v142 offset:34816\n"
"ds_read_b64_tr_b16 a[198:199], v142 offset:35328\n"
"v_mfma_f32_32x32x16_bf16 v[32:47], a[168:171], a[96:99], v[32:47]\n"
"ds_read_b64_tr_b16 a[200:201], v143 offset:34816\n"
"ds_read_b64_tr_b16 a[202:203], v143 offset:35328\n"
"buffer_load_dwordx4 v202, s[8:11], s72 offen offset:1024 lds\n"
"v_mfma_f32_32x32x16_bf16 v[48:63], a[172:175], a[96:99], v[48:63]\n"
"s_waitcnt lgkmcnt(7)\n"
"ds_read_b64_tr_b16 a[204:205], v144 offset:34816\n"
"ds_read_b64_tr_b16 a[206:207], v144 offset:35328\n"
"v_mfma_f32_32x32x16_bf16 v[0:15], a[176:179], a[100:103], v[0:15]\n"
"ds_read_b128 a[108:111], v136 offset:32768\n"
"ds_read_b64_tr_b16 a[160:161], v141 offset:35840\n"
"ds_read_b64_tr_b16 a[162:163], v141 offset:36352\n"
"buffer_load_dwordx4 v203, s[8:11], s72 offen offset:2048 lds\n"
"v_mfma_f32_32x32x16_bf16 v[16:31], a[180:183], a[100:103], v[16:31]\n"
"ds_read_b64_tr_b16 a[164:165], v142 offset:35840\n"
"ds_read_b64_tr_b16 a[166:167], v142 offset:36352\n"
"v_mfma_f32_32x32x16_bf16 v[32:47], a[184:187], a[100:103], v[32:47]\n"
"ds_read_b64_tr_b16 a[168:169], v143 offset:35840\n"
"ds_read_b64_tr_b16 a[170:171], v143 offset:36352\n"
"buffer_load_dwordx4 v204, s[8:11], s72 offen offset:3072 lds\n"
"s_add_u32 s72, s72, 0x36000\n"
"v_mfma_f32_32x32x16_bf16 v[48:63], a[188:191], a[100:103], v[48:63]\n"
"s_waitcnt lgkmcnt(7)\n"
"ds_read_b64_tr_b16 a[172:173], v144 offset:35840\n"
"ds_read_b64_tr_b16 a[174:175], v144 offset:36352\n"
"s_waitcnt vmcnt(8)\n"
"s_waitcnt lgkmcnt(8)\n"
"s_barrier\n"
"v_mfma_f32_32x32x16_bf16 v[0:15], a[192:195], a[104:107], v[0:15]\n"
"s_add_u32 m0, s65, 0xfb80\n"
"ds_read_b128 a[112:115], v133\n"
"ds_read_b64_tr_b16 a[176:177], v141 offset:49152\n"
"ds_read_b64_tr_b16 a[178:179], v141 offset:49664\n"
"buffer_load_dwordx4 v129, s[4:7], s68 offen offset:1152 lds\n"
"v_mfma_f32_32x32x16_bf16 v[16:31], a[196:199], a[104:107], v[16:31]\n"
"ds_read_b64_tr_b16 a[180:181], v142 offset:49152\n"
"ds_read_b64_tr_b16 a[182:183], v142 offset:49664\n"
"v_mfma_f32_32x32x16_bf16 v[32:47], a[200:203], a[104:107], v[32:47]\n"
"s_add_u32 m0, s65, 0xff80\n"
"ds_read_b64_tr_b16 a[184:185], v143 offset:49152\n"
"ds_read_b64_tr_b16 a[186:187], v143 offset:49664\n"
"buffer_load_dwordx4 v130, s[4:7], s69 offen offset:1152 lds\n"
"v_mfma_f32_32x32x16_bf16 v[48:63], a[204:207], a[104:107], v[48:63]\n"
"s_waitcnt lgkmcnt(7)\n"
"ds_read_b64_tr_b16 a[188:189], v144 offset:49152\n"
"ds_read_b64_tr_b16 a[190:191], v144 offset:49664\n"
"v_mfma_f32_32x32x16_bf16 v[0:15], a[160:163], a[108:111], v[0:15]\n"
"s_add_u32 m0, s65, 0x10380\n"
"ds_read_b128 a[116:119], v134\n"
"ds_read_b64_tr_b16 a[192:193], v141 offset:50176\n"
"ds_read_b64_tr_b16 a[194:195], v141 offset:50688\n"
"buffer_load_dwordx4 v129, s[4:7], s70 offen offset:1152 lds\n"
"v_mfma_f32_32x32x16_bf16 v[16:31], a[164:167], a[108:111], v[16:31]\n"
"ds_read_b64_tr_b16 a[196:197], v142 offset:50176\n"
"ds_read_b64_tr_b16 a[198:199], v142 offset:50688\n"
"v_mfma_f32_32x32x16_bf16 v[32:47], a[168:171], a[108:111], v[32:47]\n"
"s_add_u32 m0, s65, 0x10780\n"
"ds_read_b64_tr_b16 a[200:201], v143 offset:50176\n"
"ds_read_b64_tr_b16 a[202:203], v143 offset:50688\n"
"buffer_load_dwordx4 v130, s[4:7], s71 offen offset:1152 lds\n"
"v_mfma_f32_32x32x16_bf16 v[48:63], a[172:175], a[108:111], v[48:63]\n"
"s_waitcnt lgkmcnt(7)\n"
"ds_read_b64_tr_b16 a[204:205], v144 offset:50176\n"
"ds_read_b64_tr_b16 a[206:207], v144 offset:50688\n"
"v_mfma_f32_32x32x16_bf16 v[0:15], a[176:179], a[112:115], v[0:15]\n"
"s_add_u32 m0, s65, 0x1c000\n"
"ds_read_b128 a[120:123], v135\n"
"ds_read_b64_tr_b16 a[160:161], v141 offset:51200\n"
"ds_read_b64_tr_b16 a[162:163], v141 offset:51712\n"
"buffer_load_dwordx4 v131, s[8:11], s72 offen lds\n"
"v_mfma_f32_32x32x16_bf16 v[16:31], a[180:183], a[112:115], v[16:31]\n"
"ds_read_b64_tr_b16 a[164:165], v142 offset:51200\n"
"ds_read_b64_tr_b16 a[166:167], v142 offset:51712\n"
"v_mfma_f32_32x32x16_bf16 v[32:47], a[184:187], a[112:115], v[32:47]\n"
"ds_read_b64_tr_b16 a[168:169], v143 offset:51200\n"
"ds_read_b64_tr_b16 a[170:171], v143 offset:51712\n"
"buffer_load_dwordx4 v202, s[8:11], s72 offen offset:1024 lds\n"
"v_mfma_f32_32x32x16_bf16 v[48:63], a[188:191], a[112:115], v[48:63]\n"
"s_waitcnt lgkmcnt(7)\n"
"ds_read_b64_tr_b16 a[172:173], v144 offset:51200\n"
"ds_read_b64_tr_b16 a[174:175], v144 offset:51712\n"
"v_mfma_f32_32x32x16_bf16 v[0:15], a[192:195], a[116:119], v[0:15]\n"
"ds_read_b128 a[124:127], v136\n"
"ds_read_b64_tr_b16 a[176:177], v141 offset:52224\n"
"ds_read_b64_tr_b16 a[178:179], v141 offset:52736\n"
"buffer_load_dwordx4 v203, s[8:11], s72 offen offset:2048 lds\n"
"v_mfma_f32_32x32x16_bf16 v[16:31], a[196:199], a[116:119], v[16:31]\n"
"ds_read_b64_tr_b16 a[180:181], v142 offset:52224\n"
"ds_read_b64_tr_b16 a[182:183], v142 offset:52736\n"
"v_mfma_f32_32x32x16_bf16 v[32:47], a[200:203], a[116:119], v[32:47]\n"
"ds_read_b64_tr_b16 a[184:185], v143 offset:52224\n"
"ds_read_b64_tr_b16 a[186:187], v143 offset:52736\n"
"buffer_load_dwordx4 v204, s[8:11], s72 offen offset:3072 lds\n"
"s_add_u32 s72, s72, 0x36000\n"
"v_mfma_f32_32x32x16_bf16 v[48:63], a[204:207], a[116:119], v[48:63]\n"
"s_waitcnt lgkmcnt(7)\n"
"ds_read_b64_tr_b16 a[188:189], v144 offset:52224\n"
"ds_read_b64_tr_b16 a[190:191], v144 offset:52736\n"
"s_waitcnt vmcnt(8)\n"
"s_waitcnt lgkmcnt(8)\n"
"s_barrier\n"
"v_mfma_f32_32x32x16_bf16 v[0:15], a[160:163], a[120:123], v[0:15]\n"
"s_add_u32 m0, s65, 0x20000\n"
"ds_read_b128 a[128:131], v133 offset:16384\n"
"ds_read_b64_tr_b16 a[192:193], v141\n"
"ds_read_b64_tr_b16 a[194:195], v141 offset:512\n"
"buffer_load_dwordx4 v132, s[16:19], s73 offen lds\n"
"v_mfma_f32_32x32x16_bf16 v[16:31], a[164:167], a[120:123], v[16:31]\n"
"s_lshl_b32 s81, s95, 10\n"
"ds_read_b64_tr_b16 a[196:197], v142\n"
"ds_read_b64_tr_b16 a[198:199], v142 offset:512\n"
"buffer_load_dwordx4 v[108:111], v247, s[24:27], 0 offen sc1\n"
"buffer_load_dwordx4 v[112:115], v248, s[24:27], s81 offen sc1\n"
"s_lshl_b32 s81, s93, 9\n"
"buffer_load_dwordx4 v[116:119], v249, s[24:27], s81 offen sc1\n"
"s_lshl_b32 s81, s96, 10\n"
"buffer_load_dwordx4 v[120:123], v248, s[24:27], s81 offen sc1\n"
"v_mfma_f32_32x32x16_bf16 v[32:47], a[168:171], a[120:123], v[32:47]\n"
"s_mul_i32 s80, s97, 0x1400\n"
"ds_read_b64_tr_b16 a[200:201], v143\n"
"ds_read_b64_tr_b16 a[202:203], v143 offset:512\n"
"v_add_u32 v130, 0x1000, v128\n"
"s_mul_i32 s81, s64, 0xa000\n"
"s_add_u32 s90, s80, s81\n"
"s_add_u32 s90, s90, 0x80000\n"
"s_bfe_u32 s1, s3, 0x10002\n"
"v_mfma_f32_32x32x16_bf16 v[48:63], a[172:175], a[120:123], v[48:63]\n"
"s_waitcnt lgkmcnt(7)\n"
"s_add_u32 m0, s65, 0x20400\n"
"ds_read_b64_tr_b16 a[204:205], v144\n"
"ds_read_b64_tr_b16 a[206:207], v144 offset:512\n"
"buffer_load_dwordx4 v132, s[20:23], s73 offen lds\n"
"s_lshl_b32 s80, s98, 2\n"
"v_mfma_f32_32x32x16_bf16 v[0:15], a[176:179], a[124:127], v[0:15]\n"
"s_add_u32 s80, s80, s64\n"
"ds_read_b128 a[132:135], v134 offset:16384\n"
"ds_read_b64_tr_b16 a[160:161], v141 offset:1024\n"
"ds_read_b64_tr_b16 a[162:163], v141 offset:1536\n"
"s_lshl_b32 s80, s80, 1\n"
"s_add_u32 s80, s80, s1\n"
"s_mul_i32 s92, s80, 0x17800\n"
"s_add_u32 s92, s92, 0x300000\n"
"v_mfma_f32_32x32x16_bf16 v[16:31], a[180:183], a[124:127], v[16:31]\n"
"s_add_u32 m0, s65, 0x20780\n"
"ds_read_b64_tr_b16 a[164:165], v142 offset:1024\n"
"ds_read_b64_tr_b16 a[166:167], v142 offset:1536\n"
"buffer_load_dwordx4 v132, s[16:19], s73 offen offset:128 lds\n"
"s_mul_i32 s0, s1, 0xfffd1000\n"
"v_mfma_f32_32x32x16_bf16 v[32:47], a[184:187], a[124:127], v[32:47]\n"
"s_add_u32 s0, s0, 0x17800\n"
"ds_read_b64_tr_b16 a[168:169], v143 offset:1024\n"
"ds_read_b64_tr_b16 a[170:171], v143 offset:1536\n"
"v_mfma_f32_32x32x16_bf16 v[48:63], a[188:191], a[124:127], v[48:63]\n"
"s_waitcnt lgkmcnt(7)\n"
"ds_read_b64_tr_b16 a[172:173], v144 offset:1024\n"
"ds_read_b64_tr_b16 a[174:175], v144 offset:1536\n"
"v_mfma_f32_32x32x16_bf16 v[0:15], a[192:195], a[128:131], v[0:15]\n"
"s_add_u32 m0, s65, 0x20b80\n"
"ds_read_b128 a[136:139], v135 offset:16384\n"
"ds_read_b64_tr_b16 a[176:177], v141 offset:2048\n"
"ds_read_b64_tr_b16 a[178:179], v141 offset:2560\n"
"buffer_load_dwordx4 v132, s[20:23], s73 offen offset:128 lds\n"
"v_mfma_f32_32x32x16_bf16 v[16:31], a[196:199], a[128:131], v[16:31]\n"
"ds_read_b64_tr_b16 a[180:181], v142 offset:2048\n"
"ds_read_b64_tr_b16 a[182:183], v142 offset:2560\n"
"v_mfma_f32_32x32x16_bf16 v[32:47], a[200:203], a[128:131], v[32:47]\n"
"ds_read_b64_tr_b16 a[184:185], v143 offset:2048\n"
"ds_read_b64_tr_b16 a[186:187], v143 offset:2560\n"
"v_mfma_f32_32x32x16_bf16 v[48:63], a[204:207], a[128:131], v[48:63]\n"
"s_waitcnt lgkmcnt(7)\n"
"ds_read_b64_tr_b16 a[188:189], v144 offset:2048\n"
"ds_read_b64_tr_b16 a[190:191], v144 offset:2560\n"
"v_mfma_f32_32x32x16_bf16 v[0:15], a[160:163], a[132:135], v[0:15]\n"
"s_add_u32 m0, s65, 0x8000\n"
"ds_read_b128 a[140:143], v136 offset:16384\n"
"ds_read_b64_tr_b16 a[192:193], v141 offset:3072\n"
"ds_read_b64_tr_b16 a[194:195], v141 offset:3584\n"
"s_add_u32 s83, s83, 0\n"
"buffer_load_dwordx4 v238, s[28:31], s83 offen lds\n"
"v_mfma_f32_32x32x16_bf16 v[16:31], a[164:167], a[132:135], v[16:31]\n"
"s_add_u32 s83, s83, 0x2400\n"
"ds_read_b64_tr_b16 a[196:197], v142 offset:3072\n"
"ds_read_b64_tr_b16 a[198:199], v142 offset:3584\n"
"buffer_load_dwordx4 v238, s[28:31], s83 offen offset:1024 lds\n"
"s_add_u32 s83, s83, 0x2400\n"
"buffer_load_dwordx4 v238, s[28:31], s83 offen offset:2048 lds\n"
"v_mfma_f32_32x32x16_bf16 v[32:47], a[168:171], a[132:135], v[32:47]\n"
"s_add_u32 s83, s83, 0x2400\n"
"ds_read_b64_tr_b16 a[200:201], v143 offset:3072\n"
"ds_read_b64_tr_b16 a[202:203], v143 offset:3584\n"
"buffer_load_dwordx4 v238, s[28:31], s83 offen offset:3072 lds\n"
"buffer_load_dword a252, v232, s[32:35], 0 offen\n"
"v_mfma_f32_32x32x16_bf16 v[48:63], a[172:175], a[132:135], v[48:63]\n"
"s_waitcnt lgkmcnt(7)\n"
"ds_read_b64_tr_b16 a[204:205], v144 offset:3072\n"
"ds_read_b64_tr_b16 a[206:207], v144 offset:3584\n"
"s_waitcnt vmcnt(13)\n"
"s_waitcnt lgkmcnt(8)\n"
"s_barrier\n"
"v_mfma_f32_32x32x16_bf16 v[0:15], a[176:179], a[136:139], v[0:15]\n"
"s_add_u32 m0, s65, 0x23f00\n"
"ds_read_b128 a[144:147], v133 offset:32768\n"
"ds_read_b64_tr_b16 a[160:161], v141 offset:16384\n"
"ds_read_b64_tr_b16 a[162:163], v141 offset:16896\n"
"buffer_load_dwordx4 v132, s[16:19], s73 offen offset:256 lds\n"
"v_mfma_f32_32x32x16_bf16 v[16:31], a[180:183], a[136:139], v[16:31]\n"
"ds_read_b64_tr_b16 a[164:165], v142 offset:16384\n"
"ds_read_b64_tr_b16 a[166:167], v142 offset:16896\n"
"s_waitcnt vmcnt(12)\n"
"v_xor_b32 v240, s76, v109\n"
"v_bitop3_b32 v240, v111, s78, v240 bitop3:0xbe\n"
"s_waitcnt vmcnt(11)\n"
"v_bitop3_b32 v240, v113, s76, v240 bitop3:0xbe\n"
"v_bitop3_b32 v240, v115, s76, v240 bitop3:0xbe\n"
"s_waitcnt vmcnt(10)\n"
"v_bitop3_b32 v240, v117, s76, v240 bitop3:0xbe\n"
"v_bitop3_b32 v240, v119, s76, v240 bitop3:0xbe\n"
"s_waitcnt vmcnt(9)\n"
"v_bitop3_b32 v240, v121, s76, v240 bitop3:0xbe\n"
"v_bitop3_b32 v240, v123, s76, v240 bitop3:0xbe\n"
"v_cmp_ne_u32 vcc, 0, v240\n"
"s_nop 0\n"
"s_cbranch_vccnz L%=_stM_slow\n"
"L%=_stM_back:\n"
"v_mfma_f32_32x32x16_bf16 v[32:47], a[184:187], a[136:139], v[32:47]\n"
"ds_read_b64_tr_b16 a[168:169], v143 offset:16384\n"
"ds_read_b64_tr_b16 a[170:171], v143 offset:16896\n"
"ds_write_b32 v250, v110\n"
"ds_read_b128 v[174:177], v251\n"
"ds_read_b128 v[178:181], v251 offset:16\n"
"ds_read_b128 v[182:185], v251 offset:32\n"
"v_mfma_f32_32x32x16_bf16 v[48:63], a[188:191], a[136:139], v[48:63]\n"
"s_waitcnt lgkmcnt(11)\n"
"s_add_u32 m0, s65, 0x24300\n"
"ds_read_b64_tr_b16 a[172:173], v144 offset:16384\n"
"ds_read_b64_tr_b16 a[174:175], v144 offset:16896\n"
"buffer_load_dwordx4 v132, s[20:23], s73 offen offset:256 lds\n"
"ds_read_b128 v[186:189], v251 offset:48\n"
"v_mfma_f32_32x32x16_bf16 v[0:15], a[192:195], a[140:143], v[0:15]\n"
"ds_read_b128 a[148:151], v134 offset:32768\n"
"ds_read_b64_tr_b16 a[176:177], v141 offset:17408\n"
"ds_read_b64_tr_b16 a[178:179], v141 offset:17920\n"
"v_mul_f32 v108, v108, v246\n"
"v_cvt_pk_bf16_f32 v240, v108, v108\n"
"v_mul_f32 v146, 0xbfb8aa3b, v110\n"
"v_lshlrev_b32 v240, 16, v240\n"
"v_sub_f32 v241, v108, v240\n"
"v_mfma_f32_32x32x16_bf16 v[16:31], a[196:199], a[140:143], v[16:31]\n"
"s_add_u32 m0, s65, 0x24680\n"
"ds_read_b64_tr_b16 a[180:181], v142 offset:17408\n"
"ds_read_b64_tr_b16 a[182:183], v142 offset:17920\n"
"buffer_load_dwordx4 v132, s[16:19], s73 offen offset:384 lds\n"
"v_cvt_pk_bf16_f32 v148, v240, v241\n"
"v_mfma_f32_32x32x16_bf16 v[32:47], a[200:203], a[140:143], v[32:47]\n"
"ds_read_b64_tr_b16 a[184:185], v143 offset:17408\n"
"ds_read_b64_tr_b16 a[186:187], v143 offset:17920\n"
"v_mul_f32 v240, v110, v110\n"
"v_mov_b32 v149, v148\n"
"v_rcp_f32 v147, v240\n"
"v_mov_b32 v113, v112\n"
"v_mov_b32 v115, v114\n"
"v_mfma_f32_32x32x16_bf16 v[48:63], a[204:207], a[140:143], v[48:63]\n"
"s_waitcnt lgkmcnt(8)\n"
"ds_read_b64_tr_b16 a[188:189], v144 offset:17408\n"
"ds_read_b64_tr_b16 a[190:191], v144 offset:17920\n"
"v_permlane32_swap_b32 v112, v113\n"
"v_permlane32_swap_b32 v114, v115\n"
"v_cvt_pk_bf16_f32 v153, v112, v112\n"
"v_and_b32 v241, 0xffff0000, v153\n"
"v_sub_f32 v241, v112, v241\n"
"v_cvt_pk_bf16_f32 v152, v241, v241\n"
"v_mfma_f32_32x32x16_bf16 v[0:15], a[160:163], a[144:147], v[0:15]\n"
"s_add_u32 m0, s65, 0x24a80\n"
"ds_read_b128 a[152:155], v135 offset:32768\n"
"ds_read_b64_tr_b16 a[192:193], v141 offset:18432\n"
"ds_read_b64_tr_b16 a[194:195], v141 offset:18944\n"
"buffer_load_dwordx4 v132, s[20:23], s73 offen offset:384 lds\n"
"v_mfma_f32_32x32x16_bf16 v[16:31], a[164:167], a[144:147], v[16:31]\n"
"ds_read_b64_tr_b16 a[196:197], v142 offset:18432\n"
"ds_read_b64_tr_b16 a[198:199], v142 offset:18944\n"
"v_cvt_pk_bf16_f32 v155, v114, v114\n"
"v_and_b32 v241, 0xffff0000, v155\n"
"v_sub_f32 v241, v114, v241\n"
"v_cvt_pk_bf16_f32 v154, v241, v241\n"
"v_cvt_pk_bf16_f32 v157, v113, v113\n"
"v_and_b32 v241, 0xffff0000, v157\n"
"v_mfma_f32_32x32x16_bf16 v[32:47], a[168:171], a[144:147], v[32:47]\n"
"ds_read_b64_tr_b16 a[200:201], v143 offset:18432\n"
"ds_read_b64_tr_b16 a[202:203], v143 offset:18944\n"
"v_sub_f32 v241, v113, v241\n"
"v_cvt_pk_bf16_f32 v156, v241, v241\n"
"v_cvt_pk_bf16_f32 v159, v115, v115\n"
"v_and_b32 v241, 0xffff0000, v159\n"
"v_sub_f32 v241, v115, v241\n"
"v_cvt_pk_bf16_f32 v158, v241, v241\n"
"v_mfma_f32_32x32x16_bf16 v[48:63], a[172:175], a[144:147], v[48:63]\n"
"s_waitcnt lgkmcnt(7)\n"
"ds_read_b64_tr_b16 a[204:205], v144 offset:18432\n"
"ds_read_b64_tr_b16 a[206:207], v144 offset:18944\n"
"v_cvt_pk_bf16_f32 v161, v116, v116\n"
"v_and_b32 v241, 0xffff0000, v161\n"
"v_sub_f32 v241, v116, v241\n"
"v_cvt_pk_bf16_f32 v160, v241, v241\n"
"v_cvt_pk_bf16_f32 v163, v118, v118\n"
"v_and_b32 v241, 0xffff0000, v163\n"
"v_mfma_f32_32x32x16_bf16 v[0:15], a[176:179], a[148:151], v[0:15]\n"
"ds_read_b128 a[156:159], v136 offset:32768\n"
"ds_read_b64_tr_b16 a[160:161], v141 offset:19456\n"
"ds_read_b64_tr_b16 a[162:163], v141 offset:19968\n"
"v_sub_f32 v241, v118, v241\n"
"v_cvt_pk_bf16_f32 v162, v241, v241\n"
"v_mfma_f32_32x32x16_bf16 v[16:31], a[180:183], a[148:151], v[16:31]\n"
"s_and_b32 s80, s64, 1\n"
"ds_read_b64_tr_b16 a[164:165], v142 offset:19456\n"
"ds_read_b64_tr_b16 a[166:167], v142 offset:19968\n"
"ds_read_b64_tr_b16 a[168:169], v143 offset:19456\n"
"v_lshrrev_b32 v232, 3, v253\n"
"s_mul_i32 s81, s80, 40\n"
"s_mul_i32 s82, s97, 0x50\n"
"s_add_u32 s81, s81, s82\n"
"s_lshl_b32 s80, s80, 2\n"
"v_add_u32 v233, s81, v232\n"
"v_lshrrev_b32 v232, 1, v232\n"
"v_xor_b32 v232, s80, v232\n"
"v_mfma_f32_32x32x16_bf16 v[32:47], a[184:187], a[148:151], v[32:47]\n"
"ds_read_b64_tr_b16 a[170:171], v143 offset:19968\n"
"ds_read_b64_tr_b16 a[172:173], v144 offset:19456\n"
"ds_read_b64_tr_b16 a[174:175], v144 offset:19968\n"
"v_mul_u32_u24 v233, 0x500, v233\n"
"v_and_b32 v234, 7, v253\n"
"v_xor_b32 v232, v232, v234\n"
"v_lshl_add_u32 v133, v232, 4, v233\n"
"v_xor_b32 v134, 64, v133\n"
"v_mfma_f32_32x32x16_bf16 v[48:63], a[188:191], a[148:151], v[48:63]\n"
"s_waitcnt lgkmcnt(9)\n"
"v_add_u32 v134, 0x2400, v134\n"
"v_xor_b32 v135, 0, v133\n"
"v_add_u32 v135, 0x4800, v135\n"
"v_xor_b32 v136, 64, v133\n"
"v_add_u32 v136, 0x6c00, v136\n"
"v_xor_b32 v129, 0, v133\n"
"v_add_u32 v129, 0xa000, v129\n"
"s_waitcnt vmcnt(9)\n"
"s_waitcnt lgkmcnt(0)\n"
"s_barrier\n"
"v_mfma_f32_32x32x16_bf16 v[0:15], a[192:195], a[152:155], v[0:15]\n"
"s_add_u32 m0, s65, 0x17e00\n"
"ds_read_b128 a[176:179], v137 offset:32768\n"
"buffer_load_dwordx4 v132, s[16:19], s73 offen offset:512 lds\n"
"v_mfma_f32_32x32x16_bf16 v[16:31], a[196:199], a[152:155], v[16:31]\n"
"ds_read_b128 a[180:183], v137 offset:33792\n"
"v_mfma_f32_32x32x16_bf16 v[32:47], a[200:203], a[152:155], v[32:47]\n"
"ds_read_b128 a[184:187], v138 offset:32768\n"
"v_mfma_f32_32x32x16_bf16 v[48:63], a[204:207], a[152:155], v[48:63]\n"
"s_add_u32 m0, s65, 0x18200\n"
"ds_read_b128 a[188:191], v138 offset:33792\n"
"buffer_load_dwordx4 v132, s[20:23], s73 offen offset:512 lds\n"
"v_mfma_f32_32x32x16_bf16 v[0:15], a[160:163], a[156:159], v[0:15]\n"
"ds_read_b128 a[192:195], v139 offset:32768\n"
"v_mfma_f32_32x32x16_bf16 v[16:31], a[164:167], a[156:159], v[16:31]\n"
"s_add_u32 m0, s65, 0x18580\n"
"ds_read_b128 a[196:199], v139 offset:33792\n"
"buffer_load_dwordx4 v132, s[16:19], s73 offen offset:640 lds\n"
"v_mfma_f32_32x32x16_bf16 v[32:47], a[168:171], a[156:159], v[32:47]\n"
"ds_read_b128 a[200:203], v140 offset:32768\n"
"v_mfma_f32_32x32x16_bf16 v[48:63], a[172:175], a[156:159], v[48:63]\n"
"ds_read_b128 a[204:207], v140 offset:33792\n"
"v_mfma_f32_32x32x16_bf16 v[0:15], v[152:155], v[148:151], v[0:15]\n"
"v_mov_b32 v121, v120\n"
"v_mov_b32 v123, v122\n"
"s_nop 0\n"
"v_permlane32_swap_b32 v120, v121\n"
"v_permlane32_swap_b32 v122, v123\n"
"v_cvt_pk_bf16_f32 v165, v120, v120\n"
"v_and_b32 v232, 0xffff0000, v165\n"
"v_sub_f32 v232, v120, v232\n"
"v_mfma_f32_32x32x16_bf16 v[16:31], v[154:157], v[148:151], v[16:31]\n"
"v_cvt_pk_bf16_f32 v164, v232, v232\n"
"v_cvt_pk_bf16_f32 v167, v122, v122\n"
"v_and_b32 v232, 0xffff0000, v167\n"
"v_sub_f32 v232, v122, v232\n"
"v_cvt_pk_bf16_f32 v166, v232, v232\n"
"v_cvt_pk_bf16_f32 v169, v121, v121\n"
"v_and_b32 v232, 0xffff0000, v169\n"
"v_mfma_f32_32x32x16_bf16 v[32:47], v[156:159], v[148:151], v[32:47]\n"
"v_sub_f32 v232, v121, v232\n"
"v_cvt_pk_bf16_f32 v168, v232, v232\n"
"v_cvt_pk_bf16_f32 v171, v123, v123\n"
"v_and_b32 v232, 0xffff0000, v171\n"
"v_sub_f32 v232, v123, v232\n"
"v_cvt_pk_bf16_f32 v170, v232, v232\n"
"v_mul_f32 v232, v0, v146\n"
"v_mfma_f32_32x32x16_bf16 v[48:63], v[158:161], v[148:151], v[48:63]\n"
"s_waitcnt lgkmcnt(4)\n"
"v_mul_f32 v233, v1, v146\n"
"v_mul_f32 v234, v2, v146\n"
"v_mul_f32 v235, v3, v146\n"
"v_exp_f32 v232, v232\n"
"v_exp_f32 v233, v233\n"
"v_mfma_f32_32x32x16_bf16 v[64:79], a[0:3], a[176:179], 0\n"
"s_add_u32 m0, s65, 0x18980\n"
"ds_read_b128 a[160:163], v137 offset:34816\n"
"buffer_load_dwordx4 v132, s[20:23], s73 offen offset:640 lds\n"
"v_exp_f32 v234, v234\n"
"v_mfma_f32_32x32x16_bf16 v[80:95], a[0:3], a[180:183], 0\n"
"ds_read_b128 a[164:167], v137 offset:35840\n"
"v_exp_f32 v235, v235\n"
"v_mul_f32 v8, v0, v8\n"
"v_mul_f32 v9, v1, v9\n"
"v_mul_f32 v10, v2, v10\n"
"v_mul_f32 v11, v3, v11\n"
"v_mfma_f32_32x32x16_bf16 v[64:79], a[4:7], a[184:187], v[64:79]\n"
"ds_read_b128 a[168:171], v138 offset:34816\n"
"v_fma_f32 v232, v232, v147, v147\n"
"v_fma_f32 v233, v233, v147, v147\n"
"v_fma_f32 v234, v234, v147, v147\n"
"v_fma_f32 v235, v235, v147, v147\n"
"v_rcp_f32 v232, v232\n"
"v_mfma_f32_32x32x16_bf16 v[80:95], a[4:7], a[188:191], v[80:95]\n"
"s_waitcnt lgkmcnt(3)\n"
"s_add_u32 m0, s65, 0x1bd00\n"
"ds_read_b128 a[172:175], v138 offset:35840\n"
"buffer_load_dwordx4 v132, s[16:19], s73 offen offset:768 lds\n"
"v_rcp_f32 v233, v233\n"
"v_mfma_f32_32x32x16_bf16 v[64:79], a[8:11], a[192:195], v[64:79]\n"
"ds_read_b128 a[176:179], v139 offset:34816\n"
"ds_read_b128 a[180:183], v139 offset:35840\n"
"v_rcp_f32 v234, v234\n"
"v_rcp_f32 v235, v235\n"
"v_mul_f32 v8, v8, v232\n"
"v_mfma_f32_32x32x16_bf16 v[80:95], a[8:11], a[196:199], v[80:95]\n"
"s_add_u32 m0, s65, 0x1c100\n"
"ds_read_b128 a[184:187], v140 offset:34816\n"
"buffer_load_dwordx4 v132, s[20:23], s73 offen offset:768 lds\n"
"v_mul_f32 v9, v9, v233\n"
"v_mul_f32 v10, v10, v234\n"
"v_mfma_f32_32x32x16_bf16 v[64:79], a[12:15], a[200:203], v[64:79]\n"
"ds_read_b128 a[188:191], v140 offset:35840\n"
"v_mul_f32 v11, v11, v235\n"
"v_cvt_pk_bf16_f32 v0, v8, v9\n"
"v_cvt_pk_bf16_f32 v1, v10, v11\n"
"v_mul_f32 v232, v4, v146\n"
"v_mul_f32 v233, v5, v146\n"
"v_mul_f32 v234, v6, v146\n"
"v_mfma_f32_32x32x16_bf16 v[80:95], a[12:15], a[204:207], v[80:95]\n"
"s_waitcnt lgkmcnt(4)\n"
"v_mul_f32 v235, v7, v146\n"
"v_exp_f32 v232, v232\n"
"v_exp_f32 v233, v233\n"
"v_exp_f32 v234, v234\n"
"s_waitcnt vmcnt(6)\n"
"s_waitcnt lgkmcnt(0)\n"
"s_barrier\n"
"v_mfma_f32_32x32x16_bf16 v[64:79], a[16:19], a[160:163], v[64:79]\n"
"s_add_u32 m0, s65, 0x1c480\n"
"ds_read_b128 a[192:195], v137 offset:49152\n"
"buffer_load_dwordx4 v132, s[16:19], s73 offen offset:896 lds\n"
"v_mfma_f32_32x32x16_bf16 v[80:95], a[16:19], a[164:167], v[80:95]\n"
"ds_read_b128 a[196:199], v137 offset:50176\n"
"v_exp_f32 v235, v235\n"
"v_mul_f32 v12, v4, v12\n"
"v_mul_f32 v13, v5, v13\n"
"v_mul_f32 v14, v6, v14\n"
"v_mul_f32 v15, v7, v15\n"
"v_mfma_f32_32x32x16_bf16 v[64:79], a[20:23], a[168:171], v[64:79]\n"
"ds_read_b128 a[200:203], v138 offset:49152\n"
"v_fma_f32 v232, v232, v147, v147\n"
"v_fma_f32 v233, v233, v147, v147\n"
"v_fma_f32 v234, v234, v147, v147\n"
"v_fma_f32 v235, v235, v147, v147\n"
"v_rcp_f32 v232, v232\n"
"v_mfma_f32_32x32x16_bf16 v[80:95], a[20:23], a[172:175], v[80:95]\n"
"s_add_u32 m0, s65, 0x1c880\n"
"ds_read_b128 a[204:207], v138 offset:50176\n"
"buffer_load_dwordx4 v132, s[20:23], s73 offen offset:896 lds\n"
"v_rcp_f32 v233, v233\n"
"v_mfma_f32_32x32x16_bf16 v[64:79], a[24:27], a[176:179], v[64:79]\n"
"ds_read_b128 a[160:163], v139 offset:49152\n"
"v_rcp_f32 v234, v234\n"
"v_rcp_f32 v235, v235\n"
"v_mul_f32 v12, v12, v232\n"
"v_mul_f32 v13, v13, v233\n"
"v_mfma_f32_32x32x16_bf16 v[80:95], a[24:27], a[180:183], v[80:95]\n"
"s_add_u32 m0, s65, 0x1fc00\n"
"ds_read_b128 a[164:167], v139 offset:50176\n"
"buffer_load_dwordx4 v132, s[16:19], s73 offen offset:1024 lds\n"
"v_mul_f32 v14, v14, v234\n"
"v_mul_f32 v15, v15, v235\n"
"v_mfma_f32_32x32x16_bf16 v[64:79], a[28:31], a[184:187], v[64:79]\n"
"ds_read_b128 a[168:171], v140 offset:49152\n"
"v_lshrrev_b32 v238, 5, v253\n"
"v_lshlrev_b32 v238, 4, v238\n"
"v_bfe_u32 v237, v253, 4, 1\n"
"v_lshl_add_u32 v238, v237, 2, v238\n"
"v_bfe_u32 v237, v253, 2, 2\n"
"v_add_u32 v238, v238, v237\n"
"v_mfma_f32_32x32x16_bf16 v[80:95], a[28:31], a[188:191], v[80:95]\n"
"s_waitcnt lgkmcnt(3)\n"
"ds_read_b128 a[172:175], v140 offset:50176\n"
"v_lshlrev_b32 v238, 7, v238\n"
"v_and_b32 v237, 3, v253\n"
"v_lshl_add_u32 v238, v237, 3, v238\n"
"v_add_u32 v238, s65, v238\n"
"ds_read_b64_tr_b16 v[96:97], v238 offset:32768\n"
"ds_read_b64_tr_b16 v[98:99], v238 offset:33792\n"
"v_mfma_f32_32x32x16_bf16 v[64:79], a[32:35], a[192:195], v[64:79]\n"
"s_add_u32 m0, s65, 0x20000\n"
"ds_read_b128 a[176:179], v137 offset:51200\n"
"buffer_load_dwordx4 v132, s[20:23], s73 offen offset:1024 lds\n"
"ds_read_b64_tr_b16 v[100:101], v238 offset:32800\n"
"ds_read_b64_tr_b16 v[102:103], v238 offset:33824\n"
"v_mfma_f32_32x32x16_bf16 v[80:95], a[32:35], a[196:199], v[80:95]\n"
"ds_read_b128 a[180:183], v137 offset:52224\n"
"ds_read_b64_tr_b16 v[104:105], v238 offset:32832\n"
"ds_read_b64_tr_b16 v[106:107], v238 offset:33856\n"
"ds_read_b64_tr_b16 v[108:109], v238 offset:32864\n"
"ds_read_b64_tr_b16 v[110:111], v238 offset:33888\n"
"s_waitcnt lgkmcnt(8)\n"
"buffer_store_dwordx4 v[96:99], v128, s[24:27], s79 offen sc1\n"
"v_mfma_f32_32x32x16_bf16 v[64:79], a[36:39], a[200:203], v[64:79]\n"
"ds_read_b128 a[184:187], v138 offset:51200\n"
"s_waitcnt lgkmcnt(6)\n"
"buffer_store_dwordx4 v[100:103], v128, s[24:27], s79 offen offset:1024 sc1\n"
"s_waitcnt lgkmcnt(3)\n"
"buffer_store_dwordx4 v[104:107], v128, s[24:27], s79 offen offset:2048 sc1\n"
"v_mfma_f32_32x32x16_bf16 v[80:95], a[36:39], a[204:207], v[80:95]\n"
"s_add_u32 m0, s65, 0x20380\n"
"ds_read_b128 a[188:191], v138 offset:52224\n"
"buffer_load_dwordx4 v132, s[16:19], s73 offen offset:1152 lds\n"
"s_waitcnt lgkmcnt(2)\n"
"buffer_store_dwordx4 v[108:111], v128, s[24:27], s79 offen offset:3072 sc1\n"
"v_mfma_f32_32x32x16_bf16 v[64:79], a[40:43], a[160:163], v[64:79]\n"
"s_lshr_b32 s82, s3, 3\n"
"ds_read_b128 a[192:195], v139 offset:51200\n"
"ds_read_b128 a[196:199], v139 offset:52224\n"
"s_lshl_b32 s82, s82, 2\n"
"s_add_u32 s82, s82, s64\n"
"s_mul_i32 s82, s82, 5\n"
"v_min_u32 v252, 4, v253\n"
"v_add_u32 v252, s82, v252\n"
"v_lshlrev_b32 v252, 7, v252\n"
"v_min_u32 v252, L%=_end-L%=_pc-8, v252\n"
"s_getpc_b64 s[80:81]\n"
"L%=_st_ct4:\n"
"s_sub_u32 s80, s80, L%=_st_ct4-L%=_pc\n"
"s_subb_u32 s81, s81, 0\n"
"global_load_dword v252, v252, s[80:81]\n"
"v_mfma_f32_32x32x16_bf16 v[80:95], a[40:43], a[164:167], v[80:95]\n"
"s_add_u32 m0, s65, 0x20780\n"
"ds_read_b128 a[200:203], v140 offset:51200\n"
"buffer_load_dwordx4 v132, s[20:23], s73 offen offset:1152 lds\n"
"v_cvt_pk_bf16_f32 v2, v12, v13\n"
"v_cvt_pk_bf16_f32 v3, v14, v15\n"
"v_mfma_f32_32x32x16_bf16 v[64:79], a[44:47], a[168:171], v[64:79]\n"
"ds_read_b128 a[204:207], v140 offset:52224\n"
"buffer_store_dwordx4 v[0:3], v145, s[24:27], s75 offen sc1\n"
"v_mfma_f32_32x32x16_bf16 v[80:95], a[44:47], a[172:175], v[80:95]\n"
"s_waitcnt lgkmcnt(4)\n"
"v_mul_f32 v232, v16, v146\n"
"v_mul_f32 v233, v17, v146\n"
"v_mul_f32 v234, v18, v146\n"
"v_mul_f32 v235, v19, v146\n"
"v_exp_f32 v232, v232\n"
"s_waitcnt vmcnt(14)\n"
"s_waitcnt lgkmcnt(0)\n"
"s_barrier\n"
"v_mfma_f32_32x32x16_bf16 v[64:79], a[48:51], a[176:179], v[64:79]\n"
"s_lshl_b32 s72, s96, 7\n"
"s_add_u32 m0, s65, 0x24000\n"
"ds_read_b128 a[160:163], v137\n"
"buffer_load_dwordx4 v131, s[8:11], s72 offen lds\n"
"v_mfma_f32_32x32x16_bf16 v[80:95], a[48:51], a[180:183], v[80:95]\n"
"ds_read_b128 a[164:167], v137 offset:1024\n"
"v_exp_f32 v233, v233\n"
"v_exp_f32 v234, v234\n"
"v_exp_f32 v235, v235\n"
"v_mfma_f32_32x32x16_bf16 v[64:79], a[52:55], a[184:187], v[64:79]\n"
"ds_read_b128 a[168:171], v138\n"
"v_mul_f32 v24, v16, v24\n"
"v_mul_f32 v25, v17, v25\n"
"v_mul_f32 v26, v18, v26\n"
"v_mul_f32 v27, v19, v27\n"
"v_fma_f32 v232, v232, v147, v147\n"
"v_fma_f32 v233, v233, v147, v147\n"
"v_mfma_f32_32x32x16_bf16 v[80:95], a[52:55], a[188:191], v[80:95]\n"
"ds_read_b128 a[172:175], v138 offset:1024\n"
"buffer_load_dwordx4 v202, s[8:11], s72 offen offset:1024 lds\n"
"v_fma_f32 v234, v234, v147, v147\n"
"v_fma_f32 v235, v235, v147, v147\n"
"v_mfma_f32_32x32x16_bf16 v[64:79], a[56:59], a[192:195], v[64:79]\n"
"ds_read_b128 a[176:179], v139\n"
"v_rcp_f32 v232, v232\n"
"v_rcp_f32 v233, v233\n"
"v_rcp_f32 v234, v234\n"
"v_mfma_f32_32x32x16_bf16 v[80:95], a[56:59], a[196:199], v[80:95]\n"
"ds_read_b128 a[180:183], v139 offset:1024\n"
"buffer_load_dwordx4 v203, s[8:11], s72 offen offset:2048 lds\n"
"v_rcp_f32 v235, v235\n"
"v_mfma_f32_32x32x16_bf16 v[64:79], a[60:63], a[200:203], v[64:79]\n"
"ds_read_b128 a[184:187], v140\n"
"v_mul_f32 v24, v24, v232\n"
"v_mul_f32 v25, v25, v233\n"
"v_mul_f32 v26, v26, v234\n"
"v_mul_f32 v27, v27, v235\n"
"v_cvt_pk_bf16_f32 v16, v24, v25\n"
"v_cvt_pk_bf16_f32 v17, v26, v27\n"
"v_mfma_f32_32x32x16_bf16 v[80:95], a[60:63], a[204:207], v[80:95]\n"
"s_waitcnt lgkmcnt(3)\n"
"ds_read_b128 a[188:191], v140 offset:1024\n"
"v_mul_f32 v232, v20, v146\n"
"v_mul_f32 v233, v21, v146\n"
"v_mul_f32 v234, v22, v146\n"
"v_mul_f32 v235, v23, v146\n"
"v_exp_f32 v232, v232\n"
"v_mfma_f32_32x32x16_bf16 v[64:79], a[64:67], a[160:163], v[64:79]\n"
"ds_read_b128 a[192:195], v137 offset:2048\n"
"buffer_load_dwordx4 v204, s[8:11], s72 offen offset:3072 lds\n"
"s_add_u32 s72, s72, 0x36000\n"
"v_mfma_f32_32x32x16_bf16 v[80:95], a[64:67], a[164:167], v[80:95]\n"
"ds_read_b128 a[196:199], v137 offset:3072\n"
"v_exp_f32 v233, v233\n"
"v_exp_f32 v234, v234\n"
"v_exp_f32 v235, v235\n"
"v_mfma_f32_32x32x16_bf16 v[64:79], a[68:71], a[168:171], v[64:79]\n"
"ds_read_b128 a[200:203], v138 offset:2048\n"
"v_mul_f32 v28, v20, v28\n"
"v_mul_f32 v29, v21, v29\n"
"v_mul_f32 v30, v22, v30\n"
"v_mul_f32 v31, v23, v31\n"
"v_fma_f32 v232, v232, v147, v147\n"
"v_fma_f32 v233, v233, v147, v147\n"
"v_mfma_f32_32x32x16_bf16 v[80:95], a[68:71], a[172:175], v[80:95]\n"
"s_waitcnt lgkmcnt(3)\n"
"ds_read_b128 a[204:207], v138 offset:3072\n"
"v_fma_f32 v234, v234, v147, v147\n"
"v_fma_f32 v235, v235, v147, v147\n"
"v_rcp_f32 v232, v232\n"
"v_rcp_f32 v233, v233\n"
"v_mfma_f32_32x32x16_bf16 v[64:79], a[72:75], a[176:179], v[64:79]\n"
"ds_read_b128 a[160:163], v139 offset:2048\n"
"ds_read_b128 a[164:167], v139 offset:3072\n"
"v_rcp_f32 v234, v234\n"
"v_rcp_f32 v235, v235\n"
"v_mul_f32 v28, v28, v232\n"
"v_mfma_f32_32x32x16_bf16 v[80:95], a[72:75], a[180:183], v[80:95]\n"
"ds_read_b128 a[168:171], v140 offset:2048\n"
"v_mul_f32 v29, v29, v233\n"
"v_mul_f32 v30, v30, v234\n"
"v_mul_f32 v31, v31, v235\n"
"v_cvt_pk_bf16_f32 v18, v28, v29\n"
"v_cvt_pk_bf16_f32 v19, v30, v31\n"
"buffer_store_dwordx4 v[16:19], v145, s[24:27], s75 offen offset:512 sc1\n"
"v_mfma_f32_32x32x16_bf16 v[64:79], a[76:79], a[184:187], v[64:79]\n"
"ds_read_b128 a[172:175], v140 offset:3072\n"
"v_mul_f32 v232, v32, v146\n"
"v_mul_f32 v233, v33, v146\n"
"v_mul_f32 v234, v34, v146\n"
"v_mul_f32 v235, v35, v146\n"
"v_exp_f32 v232, v232\n"
"v_mfma_f32_32x32x16_bf16 v[80:95], a[76:79], a[188:191], v[80:95]\n"
"s_waitcnt lgkmcnt(4)\n"
"v_exp_f32 v233, v233\n"
"v_exp_f32 v234, v234\n"
"v_exp_f32 v235, v235\n"
"v_mul_f32 v40, v32, v40\n"
"s_waitcnt vmcnt(15)\n"
"s_waitcnt lgkmcnt(0)\n"
"s_barrier\n"
"v_mfma_f32_32x32x16_bf16 v[64:79], a[80:83], a[192:195], v[64:79]\n"
"s_add_u32 m0, s65, 0x18000\n"
"ds_read_b128 a[176:179], v137 offset:16384\n"
"buffer_load_dwordx4 v131, s[8:11], s72 offen lds\n"
"v_mfma_f32_32x32x16_bf16 v[80:95], a[80:83], a[196:199], v[80:95]\n"
"ds_read_b128 a[180:183], v137 offset:17408\n"
"v_mul_f32 v41, v33, v41\n"
"v_mul_f32 v42, v34, v42\n"
"v_mul_f32 v43, v35, v43\n"
"v_fma_f32 v232, v232, v147, v147\n"
"v_fma_f32 v233, v233, v147, v147\n"
"v_fma_f32 v234, v234, v147, v147\n"
"v_mfma_f32_32x32x16_bf16 v[64:79], a[84:87], a[200:203], v[64:79]\n"
"ds_read_b128 a[184:187], v138 offset:16384\n"
"v_fma_f32 v235, v235, v147, v147\n"
"v_rcp_f32 v232, v232\n"
"v_rcp_f32 v233, v233\n"
"v_mfma_f32_32x32x16_bf16 v[80:95], a[84:87], a[204:207], v[80:95]\n"
"ds_read_b128 a[188:191], v138 offset:17408\n"
"buffer_load_dwordx4 v202, s[8:11], s72 offen offset:1024 lds\n"
"v_rcp_f32 v234, v234\n"
"v_mfma_f32_32x32x16_bf16 v[64:79], a[88:91], a[160:163], v[64:79]\n"
"ds_read_b128 a[192:195], v139 offset:16384\n"
"v_rcp_f32 v235, v235\n"
"v_mul_f32 v40, v40, v232\n"
"v_mul_f32 v41, v41, v233\n"
"v_mul_f32 v42, v42, v234\n"
"v_mul_f32 v43, v43, v235\n"
"v_mfma_f32_32x32x16_bf16 v[80:95], a[88:91], a[164:167], v[80:95]\n"
"ds_read_b128 a[196:199], v139 offset:17408\n"
"buffer_load_dwordx4 v203, s[8:11], s72 offen offset:2048 lds\n"
"v_cvt_pk_bf16_f32 v32, v40, v41\n"
"v_cvt_pk_bf16_f32 v33, v42, v43\n"
"v_mfma_f32_32x32x16_bf16 v[64:79], a[92:95], a[168:171], v[64:79]\n"
"ds_read_b128 a[200:203], v140 offset:16384\n"
"v_mul_f32 v232, v36, v146\n"
"v_mul_f32 v233, v37, v146\n"
"v_mul_f32 v234, v38, v146\n"
"v_mul_f32 v235, v39, v146\n"
"v_exp_f32 v232, v232\n"
"v_mfma_f32_32x32x16_bf16 v[80:95], a[92:95], a[172:175], v[80:95]\n"
"s_waitcnt lgkmcnt(3)\n"
"ds_read_b128 a[204:207], v140 offset:17408\n"
"v_exp_f32 v233, v233\n"
"v_exp_f32 v234, v234\n"
"v_exp_f32 v235, v235\n"
"v_mfma_f32_32x32x16_bf16 v[64:79], a[96:99], a[176:179], v[64:79]\n"
"ds_read_b128 a[160:163], v137 offset:18432\n"
"buffer_load_dwordx4 v204, s[8:11], s72 offen offset:3072 lds\n"
"s_add_u32 s72, s72, 0x36000\n"
"v_mul_f32 v44, v36, v44\n"
"v_mfma_f32_32x32x16_bf16 v[80:95], a[96:99], a[180:183], v[80:95]\n"
"ds_read_b128 a[164:167], v137 offset:19456\n"
"v_mul_f32 v45, v37, v45\n"
"v_mul_f32 v46, v38, v46\n"
"v_mul_f32 v47, v39, v47\n"
"v_fma_f32 v232, v232, v147, v147\n"
"v_fma_f32 v233, v233, v147, v147\n"
"v_fma_f32 v234, v234, v147, v147\n"
"v_mfma_f32_32x32x16_bf16 v[64:79], a[100:103], a[184:187], v[64:79]\n"
"ds_read_b128 a[168:171], v138 offset:18432\n"
"v_fma_f32 v235, v235, v147, v147\n"
"v_rcp_f32 v232, v232\n"
"v_rcp_f32 v233, v233\n"
"v_mfma_f32_32x32x16_bf16 v[80:95], a[100:103], a[188:191], v[80:95]\n"
"s_waitcnt lgkmcnt(3)\n"
"ds_read_b128 a[172:175], v138 offset:19456\n"
"v_rcp_f32 v234, v234\n"
"v_rcp_f32 v235, v235\n"
"v_mul_f32 v44, v44, v232\n"
"v_mul_f32 v45, v45, v233\n"
"v_mfma_f32_32x32x16_bf16 v[64:79], a[104:107], a[192:195], v[64:79]\n"
"ds_read_b128 a[176:179], v139 offset:18432\n"
"ds_read_b128 a[180:183], v139 offset:19456\n"
"v_mul_f32 v46, v46, v234\n"
"v_mul_f32 v47, v47, v235\n"
"v_cvt_pk_bf16_f32 v34, v44, v45\n"
"v_cvt_pk_bf16_f32 v35, v46, v47\n"
"buffer_store_dwordx4 v[32:35], v145, s[24:27], s75 offen offset:1024 sc1\n"
"v_mfma_f32_32x32x16_bf16 v[80:95], a[104:107], a[196:199], v[80:95]\n"
"ds_read_b128 a[184:187], v140 offset:18432\n"
"v_mul_f32 v232, v48, v146\n"
"v_mul_f32 v233, v49, v146\n"
"v_mul_f32 v234, v50, v146\n"
"v_mul_f32 v235, v51, v146\n"
"v_exp_f32 v232, v232\n"
"v_mfma_f32_32x32x16_bf16 v[64:79], a[108:111], a[200:203], v[64:79]\n"
"ds_read_b128 a[188:191], v140 offset:19456\n"
"s_waitcnt vmcnt(13)\n"
"v_exp_f32 v233, v233\n"
"v_exp_f32 v234, v234\n"
"v_exp_f32 v235, v235\n"
"v_mfma_f32_32x32x16_bf16 v[80:95], a[108:111], a[204:207], v[80:95]\n"
"s_waitcnt lgkmcnt(4)\n"
"v_mul_f32 v56, v48, v56\n"
"v_mul_f32 v57, v49, v57\n"
"v_mul_f32 v58, v50, v58\n"
"v_mul_f32 v59, v51, v59\n"
"v_fma_f32 v232, v232, v147, v147\n"
"v_fma_f32 v233, v233, v147, v147\n"
"v_fma_f32 v234, v234, v147, v147\n"
"s_waitcnt vmcnt(11)\n"
"s_waitcnt lgkmcnt(0)\n"
"s_barrier\n"
"v_mfma_f32_32x32x16_bf16 v[64:79], a[112:115], a[160:163], v[64:79]\n"
"s_add_u32 m0, s65, 0x1c000\n"
"ds_read_b128 a[192:195], v137 offset:32768\n"
"buffer_load_dwordx4 v131, s[8:11], s72 offen lds\n"
"v_mfma_f32_32x32x16_bf16 v[80:95], a[112:115], a[164:167], v[80:95]\n"
"s_lshl_b32 s80, s2, 3\n"
"ds_read_b128 a[196:199], v137 offset:33792\n"
"s_add_u32 s80, s80, 0x1000\n"
"s_cmp_eq_u32 s64, 0\n"
"s_cselect_b32 s80, s80, 0x7000000\n"
"v_mov_b32 v230, s76\n"
"buffer_store_dwordx2 v[230:231], v229, s[24:27], s80 offen sc1\n"
"v_mfma_f32_32x32x16_bf16 v[64:79], a[116:119], a[168:171], v[64:79]\n"
"ds_read_b128 a[200:203], v138 offset:32768\n"
"buffer_load_dwordx4 v202, s[8:11], s72 offen offset:1024 lds\n"
"v_fma_f32 v235, v235, v147, v147\n"
"v_mfma_f32_32x32x16_bf16 v[80:95], a[116:119], a[172:175], v[80:95]\n"
"ds_read_b128 a[204:207], v138 offset:33792\n"
"v_rcp_f32 v232, v232\n"
"v_rcp_f32 v233, v233\n"
"v_rcp_f32 v234, v234\n"
"v_mfma_f32_32x32x16_bf16 v[64:79], a[120:123], a[176:179], v[64:79]\n"
"ds_read_b128 a[160:163], v139 offset:32768\n"
"buffer_load_dwordx4 v203, s[8:11], s72 offen offset:2048 lds\n"
"v_rcp_f32 v235, v235\n"
"v_mfma_f32_32x32x16_bf16 v[80:95], a[120:123], a[180:183], v[80:95]\n"
"ds_read_b128 a[164:167], v139 offset:33792\n"
"v_mul_f32 v56, v56, v232\n"
"v_mul_f32 v57, v57, v233\n"
"v_mul_f32 v58, v58, v234\n"
"v_mul_f32 v59, v59, v235\n"
"v_cvt_pk_bf16_f32 v48, v56, v57\n"
"v_cvt_pk_bf16_f32 v49, v58, v59\n"
"v_mfma_f32_32x32x16_bf16 v[64:79], a[124:127], a[184:187], v[64:79]\n"
"ds_read_b128 a[168:171], v140 offset:32768\n"
"v_mul_f32 v232, v52, v146\n"
"v_mul_f32 v233, v53, v146\n"
"v_mul_f32 v234, v54, v146\n"
"v_mul_f32 v235, v55, v146\n"
"v_exp_f32 v232, v232\n"
"v_mfma_f32_32x32x16_bf16 v[80:95], a[124:127], a[188:191], v[80:95]\n"
"ds_read_b128 a[172:175], v140 offset:33792\n"
"buffer_load_dwordx4 v204, s[8:11], s72 offen offset:3072 lds\n"
"s_add_u32 s72, s72, 0x36000\n"
"v_mfma_f32_32x32x16_bf16 v[64:79], v[148:151], v[160:163], v[64:79]\n"
"v_exp_f32 v233, v233\n"
"v_exp_f32 v234, v234\n"
"v_exp_f32 v235, v235\n"
"v_mul_f32 v60, v52, v60\n"
"v_mfma_f32_32x32x16_bf16 v[80:95], v[148:151], v[162:165], v[80:95]\n"
"s_waitcnt lgkmcnt(4)\n"
"v_mul_f32 v61, v53, v61\n"
"v_mul_f32 v62, v54, v62\n"
"v_mul_f32 v63, v55, v63\n"
"v_fma_f32 v232, v232, v147, v147\n"
"v_fma_f32 v233, v233, v147, v147\n"
"v_fma_f32 v234, v234, v147, v147\n"
"v_fma_f32 v235, v235, v147, v147\n"
"v_mfma_f32_32x32x16_bf16 v[64:79], a[128:131], a[192:195], v[64:79]\n"
"ds_read_b128 a[176:179], v137 offset:34816\n"
"v_rcp_f32 v232, v232\n"
"v_rcp_f32 v233, v233\n"
"v_rcp_f32 v234, v234\n"
"v_mfma_f32_32x32x16_bf16 v[80:95], a[128:131], a[196:199], v[80:95]\n"
"s_lshl_b32 s99, s1, 7\n"
"s_add_u32 s99, s99, 0\n"
"s_add_u32 m0, s88, 0\n"
"ds_read_b128 a[180:183], v137 offset:35840\n"
"buffer_load_dwordx4 v133, s[32:35], s99 offen lds\n"
"v_mfma_f32_32x32x16_bf16 v[64:79], a[132:135], a[200:203], v[64:79]\n"
"ds_read_b128 a[184:187], v138 offset:34816\n"
"v_rcp_f32 v235, v235\n"
"v_mul_f32 v60, v60, v232\n"
"v_mul_f32 v61, v61, v233\n"
"v_mul_f32 v62, v62, v234\n"
"v_mul_f32 v63, v63, v235\n"
"v_mfma_f32_32x32x16_bf16 v[80:95], a[132:135], a[204:207], v[80:95]\n"
"s_waitcnt lgkmcnt(3)\n"
"ds_read_b128 a[188:191], v138 offset:35840\n"
"buffer_load_dwordx4 v134, s[32:35], s99 offen offset:1024 lds\n"
"v_cvt_pk_bf16_f32 v50, v60, v61\n"
"v_cvt_pk_bf16_f32 v51, v62, v63\n"
"v_mfma_f32_32x32x16_bf16 v[64:79], a[136:139], a[160:163], v[64:79]\n"
"ds_read_b128 a[192:195], v139 offset:34816\n"
"ds_read_b128 a[196:199], v139 offset:35840\n"
"buffer_store_dwordx4 v[48:51], v145, s[24:27], s75 offen offset:1536 sc1\n"
"v_mfma_f32_32x32x16_bf16 v[80:95], a[136:139], a[164:167], v[80:95]\n"
"ds_read_b128 a[200:203], v140 offset:34816\n"
"buffer_load_dwordx4 v135, s[32:35], s99 offen offset:2048 lds\n"
"v_mfma_f32_32x32x16_bf16 v[64:79], a[140:143], a[168:171], v[64:79]\n"
"ds_read_b128 a[204:207], v140 offset:35840\n"
"v_mfma_f32_32x32x16_bf16 v[80:95], a[140:143], a[172:175], v[80:95]\n"
"s_waitcnt lgkmcnt(4)\n"
"s_waitcnt vmcnt(15)\n"
"s_waitcnt lgkmcnt(0)\n"
"s_barrier\n"
"v_mfma_f32_32x32x16_bf16 v[64:79], a[144:147], a[176:179], v[64:79]\n"
"s_add_u32 m0, s65, 0x20000\n"
"ds_read_b64_tr_b16 a[160:161], v141 offset:49152\n"
"ds_read_b64_tr_b16 a[162:163], v141 offset:49664\n"
"buffer_load_dwordx4 v131, s[8:11], s72 offen lds\n"
"v_mfma_f32_32x32x16_bf16 v[80:95], a[144:147], a[180:183], v[80:95]\n"
"ds_read_b64_tr_b16 a[164:165], v142 offset:49152\n"
"ds_read_b64_tr_b16 a[166:167], v142 offset:49664\n"
"v_mfma_f32_32x32x16_bf16 v[64:79], a[148:151], a[184:187], v[64:79]\n"
"ds_read_b64_tr_b16 a[168:169], v143 offset:49152\n"
"ds_read_b64_tr_b16 a[170:171], v143 offset:49664\n"
"buffer_load_dwordx4 v202, s[8:11], s72 offen offset:1024 lds\n"
"v_mfma_f32_32x32x16_bf16 v[80:95], a[148:151], a[188:191], v[80:95]\n"
"ds_read_b64_tr_b16 a[172:173], v144 offset:49152\n"
"ds_read_b64_tr_b16 a[174:175], v144 offset:49664\n"
"v_mfma_f32_32x32x16_bf16 v[64:79], a[152:155], a[192:195], v[64:79]\n"
"ds_read_b64_tr_b16 a[176:177], v141 offset:50176\n"
"ds_read_b64_tr_b16 a[178:179], v141 offset:50688\n"
"buffer_load_dwordx4 v203, s[8:11], s72 offen offset:2048 lds\n"
"v_mfma_f32_32x32x16_bf16 v[80:95], a[152:155], a[196:199], v[80:95]\n"
"ds_read_b64_tr_b16 a[180:181], v142 offset:50176\n"
"ds_read_b64_tr_b16 a[182:183], v142 offset:50688\n"
"v_mfma_f32_32x32x16_bf16 v[64:79], a[156:159], a[200:203], v[64:79]\n"
"ds_read_b64_tr_b16 a[184:185], v143 offset:50176\n"
"ds_read_b64_tr_b16 a[186:187], v143 offset:50688\n"
"v_mfma_f32_32x32x16_bf16 v[80:95], a[156:159], a[204:207], v[80:95]\n"
"ds_read_b64_tr_b16 a[188:189], v144 offset:50176\n"
"ds_read_b64_tr_b16 a[190:191], v144 offset:50688\n"
"buffer_load_dwordx4 v204, s[8:11], s72 offen offset:3072 lds\n"
"s_add_u32 s72, s72, 0x36000\n"
"v_mfma_f32_32x32x16_bf16 v[0:15], v[164:167], v[148:151], 0\n"
"v_mfma_f32_32x32x16_bf16 v[16:31], v[166:169], v[148:151], 0\n"
"s_nop 2\n"
"v_mul_f32 v64, v64, v174\n"
"s_nop 1\n"
"v_mul_f32 v80, v80, v174\n"
"v_mul_f32 v65, v65, v175\n"
"v_mul_f32 v81, v81, v175\n"
"v_fma_f32 v238, v190, v64, v65\n"
"v_fma_f32 v239, v190, v80, v81\n"
"v_fma_f32 v238, -v191, v80, v238\n"
"v_fmac_f32 v239, v191, v64\n"
"v_mul_f32 v66, v66, v176\n"
"v_mul_f32 v82, v82, v176\n"
"v_fma_f32 v236, v190, v238, v66\n"
"v_fma_f32 v237, v190, v239, v82\n"
"v_mfma_f32_32x32x16_bf16 v[32:47], v[168:171], v[148:151], 0\n"
"v_fma_f32 v236, -v191, v239, v236\n"
"v_fmac_f32 v237, v191, v238\n"
"v_mul_f32 v67, v67, v177\n"
"v_mul_f32 v83, v83, v177\n"
"v_fma_f32 v238, v190, v236, v67\n"
"v_fma_f32 v239, v190, v237, v83\n"
"v_fma_f32 v238, -v191, v237, v238\n"
"v_fmac_f32 v239, v191, v236\n"
"v_mul_f32 v68, v68, v178\n"
"v_mul_f32 v84, v84, v178\n"
"v_fma_f32 v236, v190, v238, v68\n"
"v_fma_f32 v237, v190, v239, v84\n"
"v_mfma_f32_32x32x16_bf16 v[48:63], v[170:173], v[148:151], 0\n"
"s_waitcnt lgkmcnt(8)\n"
"v_fma_f32 v236, -v191, v239, v236\n"
"v_fmac_f32 v237, v191, v238\n"
"v_mul_f32 v69, v69, v179\n"
"v_mul_f32 v85, v85, v179\n"
"v_fma_f32 v238, v190, v236, v69\n"
"v_fma_f32 v239, v190, v237, v85\n"
"v_fma_f32 v238, -v191, v237, v238\n"
"v_fmac_f32 v239, v191, v236\n"
"v_mul_f32 v70, v70, v180\n"
"v_mul_f32 v86, v86, v180\n"
"v_fma_f32 v236, v190, v238, v70\n"
"v_fma_f32 v237, v190, v239, v86\n"
"v_mfma_f32_32x32x16_bf16 v[0:15], a[160:163], a[0:3], v[0:15]\n"
"ds_read_b64_tr_b16 a[192:193], v141 offset:51200\n"
"ds_read_b64_tr_b16 a[194:195], v141 offset:51712\n"
"v_fma_f32 v236, -v191, v239, v236\n"
"v_fmac_f32 v237, v191, v238\n"
"v_mul_f32 v71, v71, v181\n"
"v_mul_f32 v87, v87, v181\n"
"v_fma_f32 v238, v190, v236, v71\n"
"v_fma_f32 v239, v190, v237, v87\n"
"v_fma_f32 v238, -v191, v237, v238\n"
"v_fmac_f32 v239, v191, v236\n"
"v_mul_f32 v72, v72, v182\n"
"v_mul_f32 v88, v88, v182\n"
"v_mfma_f32_32x32x16_bf16 v[16:31], a[164:167], a[0:3], v[16:31]\n"
"s_add_u32 m0, s88, 0\n"
"ds_read_b64_tr_b16 a[196:197], v142 offset:51200\n"
"ds_read_b64_tr_b16 a[198:199], v142 offset:51712\n"
"buffer_load_dwordx4 v136, s[32:35], s99 offen offset:3072 lds\n"
"v_fma_f32 v236, v190, v238, v72\n"
"v_fma_f32 v237, v190, v239, v88\n"
"v_fma_f32 v236, -v191, v239, v236\n"
"v_fmac_f32 v237, v191, v238\n"
"v_mul_f32 v73, v73, v183\n"
"v_mfma_f32_32x32x16_bf16 v[32:47], a[168:171], a[0:3], v[32:47]\n"
"ds_read_b64_tr_b16 a[200:201], v143 offset:51200\n"
"ds_read_b64_tr_b16 a[202:203], v143 offset:51712\n"
"v_mul_f32 v89, v89, v183\n"
"v_fma_f32 v238, v190, v236, v73\n"
"v_fma_f32 v239, v190, v237, v89\n"
"v_fma_f32 v238, -v191, v237, v238\n"
"v_fmac_f32 v239, v191, v236\n"
"v_mul_f32 v74, v74, v184\n"
"v_mul_f32 v90, v90, v184\n"
"v_fma_f32 v236, v190, v238, v74\n"
"v_fma_f32 v237, v190, v239, v90\n"
"v_fma_f32 v236, -v191, v239, v236\n"
"v_mfma_f32_32x32x16_bf16 v[48:63], a[172:175], a[0:3], v[48:63]\n"
"s_waitcnt lgkmcnt(6)\n"
"s_add_u32 m0, s88, 0x1000\n"
"ds_read_b64_tr_b16 a[204:205], v144 offset:51200\n"
"ds_read_b64_tr_b16 a[206:207], v144 offset:51712\n"
"buffer_load_dwordx4 v129, s[32:35], s99 offen lds\n"
"v_fmac_f32 v237, v191, v238\n"
"v_mul_f32 v75, v75, v185\n"
"v_mul_f32 v91, v91, v185\n"
"v_fma_f32 v238, v190, v236, v75\n"
"v_fma_f32 v239, v190, v237, v91\n"
"v_mfma_f32_32x32x16_bf16 v[0:15], a[176:179], a[4:7], v[0:15]\n"
"ds_read_b64_tr_b16 a[160:161], v141 offset:52224\n"
"ds_read_b64_tr_b16 a[162:163], v141 offset:52736\n"
"ds_read_b64_tr_b16 a[164:165], v142 offset:52224\n"
"v_fma_f32 v238, -v191, v237, v238\n"
"v_fmac_f32 v239, v191, v236\n"
"v_mul_f32 v76, v76, v186\n"
"v_mul_f32 v92, v92, v186\n"
"v_fma_f32 v236, v190, v238, v76\n"
"v_fma_f32 v237, v190, v239, v92\n"
"v_fma_f32 v236, -v191, v239, v236\n"
"v_fmac_f32 v237, v191, v238\n"
"v_mul_f32 v77, v77, v187\n"
"v_mfma_f32_32x32x16_bf16 v[16:31], a[180:183], a[4:7], v[16:31]\n"
"s_lshl_b32 s99, s1, 7\n"
"s_add_u32 s99, s99, 0x100\n"
"s_add_u32 m0, s88, 0x5000\n"
"ds_read_b64_tr_b16 a[166:167], v142 offset:52736\n"
"ds_read_b64_tr_b16 a[168:169], v143 offset:52224\n"
"ds_read_b64_tr_b16 a[170:171], v143 offset:52736\n"
"buffer_load_dwordx4 v133, s[32:35], s99 offen lds\n"
"v_mul_f32 v93, v93, v187\n"
"v_fma_f32 v238, v190, v236, v77\n"
"v_mfma_f32_32x32x16_bf16 v[32:47], a[184:187], a[4:7], v[32:47]\n"
"ds_read_b64_tr_b16 a[172:173], v144 offset:52224\n"
"ds_read_b64_tr_b16 a[174:175], v144 offset:52736\n"
"v_fma_f32 v239, v190, v237, v93\n"
"v_fma_f32 v238, -v191, v237, v238\n"
"v_fmac_f32 v239, v191, v236\n"
"v_mul_f32 v78, v78, v188\n"
"v_mul_f32 v94, v94, v188\n"
"v_fma_f32 v236, v190, v238, v78\n"
"v_fma_f32 v237, v190, v239, v94\n"
"v_fma_f32 v236, -v191, v239, v236\n"
"v_fmac_f32 v237, v191, v238\n"
"v_mul_f32 v79, v79, v189\n"
"v_mfma_f32_32x32x16_bf16 v[48:63], a[188:191], a[4:7], v[48:63]\n"
"s_waitcnt lgkmcnt(8)\n"
"v_mul_f32 v95, v95, v189\n"
"v_fma_f32 v238, v190, v236, v79\n"
"v_fma_f32 v239, v190, v237, v95\n"
"v_fma_f32 v238, -v191, v237, v238\n"
"v_fmac_f32 v239, v191, v236\n"
"ds_write_b64 v214, v[238:239] offset:1792\n"
"s_waitcnt vmcnt(17)\n"
"s_waitcnt lgkmcnt(0)\n"
"s_barrier\n"
"v_mfma_f32_32x32x16_bf16 v[0:15], a[192:195], a[8:11], v[0:15]\n"
"s_add_u32 m0, s65, 0x24000\n"
"ds_read_b64_tr_b16 a[176:177], v141\n"
"ds_read_b64_tr_b16 a[178:179], v141 offset:512\n"
"buffer_load_dwordx4 v131, s[8:11], s72 offen lds\n"
"v_mfma_f32_32x32x16_bf16 v[16:31], a[196:199], a[8:11], v[16:31]\n"
"ds_read_b64_tr_b16 a[180:181], v142\n"
"ds_read_b64_tr_b16 a[182:183], v142 offset:512\n"
"ds_read_b64 v[96:97], v214\n"
"ds_read_b64 v[98:99], v214 offset:256\n"
"ds_read_b64 v[100:101], v214 offset:512\n"
"ds_read_b64 v[102:103], v214 offset:768\n"
"ds_read_b64 v[104:105], v214 offset:1024\n"
"ds_read_b64 v[106:107], v214 offset:1280\n"
"ds_read_b64 v[108:109], v214 offset:1536\n"
"s_waitcnt lgkmcnt(5)\n"
"v_fmac_f32 v98, v192, v96\n"
"v_fmac_f32 v99, v192, v97\n"
"v_fma_f32 v98, -v193, v97, v98\n"
"v_mfma_f32_32x32x16_bf16 v[32:47], a[200:203], a[8:11], v[32:47]\n"
"ds_read_b64_tr_b16 a[184:185], v143\n"
"ds_read_b64_tr_b16 a[186:187], v143 offset:512\n"
"buffer_load_dwordx4 v202, s[8:11], s72 offen offset:1024 lds\n"
"v_fmac_f32 v99, v193, v96\n"
"s_waitcnt lgkmcnt(6)\n"
"v_fmac_f32 v100, v192, v98\n"
"v_fmac_f32 v101, v192, v99\n"
"v_fma_f32 v100, -v193, v99, v100\n"
"v_fmac_f32 v101, v193, v98\n"
"v_mfma_f32_32x32x16_bf16 v[48:63], a[204:207], a[8:11], v[48:63]\n"
"ds_read_b64_tr_b16 a[188:189], v144\n"
"ds_read_b64_tr_b16 a[190:191], v144 offset:512\n"
"s_waitcnt lgkmcnt(7)\n"
"v_fmac_f32 v102, v192, v100\n"
"v_fmac_f32 v103, v192, v101\n"
"v_fma_f32 v102, -v193, v101, v102\n"
"v_fmac_f32 v103, v193, v100\n"
"s_waitcnt lgkmcnt(6)\n"
"v_fmac_f32 v104, v192, v102\n"
"v_fmac_f32 v105, v192, v103\n"
"v_fma_f32 v104, -v193, v103, v104\n"
"v_fmac_f32 v105, v193, v102\n"
"s_waitcnt lgkmcnt(5)\n"
"v_fmac_f32 v106, v192, v104\n"
"v_fmac_f32 v107, v192, v105\n"
"v_mfma_f32_32x32x16_bf16 v[0:15], a[160:163], a[12:15], v[0:15]\n"
"ds_read_b64_tr_b16 a[192:193], v141 offset:1024\n"
"ds_read_b64_tr_b16 a[194:195], v141 offset:1536\n"
"buffer_load_dwordx4 v203, s[8:11], s72 offen offset:2048 lds\n"
"v_fma_f32 v106, -v193, v105, v106\n"
"v_fmac_f32 v107, v193, v104\n"
"s_waitcnt lgkmcnt(6)\n"
"v_fmac_f32 v108, v192, v106\n"
"v_fmac_f32 v109, v192, v107\n"
"v_fma_f32 v108, -v193, v107, v108\n"
"v_mfma_f32_32x32x16_bf16 v[16:31], a[164:167], a[12:15], v[16:31]\n"
"s_lshl_b32 s80, s93, 9\n"
"ds_read_b64_tr_b16 a[196:197], v142 offset:1024\n"
"ds_read_b64_tr_b16 a[198:199], v142 offset:1536\n"
"v_fmac_f32 v109, v193, v106\n"
"v_mov_b32 v240, v108\n"
"v_mov_b32 v241, v109\n"
"v_fma_f32 v96, v192, v108, v238\n"
"v_fma_f32 v98, v192, v109, v239\n"
"v_fma_f32 v96, -v193, v109, v96\n"
"v_fmac_f32 v98, v193, v108\n"
"v_mov_b32 v97, s76\n"
"v_mov_b32 v99, s78\n"
"s_add_u32 s80, s80, 0x20000\n"
"buffer_store_dwordx4 v[96:99], v216, s[24:27], s80 offen sc1\n"
"v_mfma_f32_32x32x16_bf16 v[32:47], a[168:171], a[12:15], v[32:47]\n"
"ds_read_b64_tr_b16 a[200:201], v143 offset:1024\n"
"ds_read_b64_tr_b16 a[202:203], v143 offset:1536\n"
"v_mul_f32 v242, v200, v64\n"
"v_fma_f32 v242, -v201, v80, v242\n"
"v_mul_f32 v80, v200, v80\n"
"v_fmac_f32 v80, v201, v64\n"
"v_mul_f32 v64, v200, v65\n"
"v_fma_f32 v64, -v201, v81, v64\n"
"v_mul_f32 v81, v200, v81\n"
"v_fmac_f32 v81, v201, v65\n"
"v_mul_f32 v65, v200, v66\n"
"v_fma_f32 v65, -v201, v82, v65\n"
"v_mfma_f32_32x32x16_bf16 v[48:63], a[172:175], a[12:15], v[48:63]\n"
"s_waitcnt lgkmcnt(6)\n"
"ds_read_b64_tr_b16 a[204:205], v144 offset:1024\n"
"ds_read_b64_tr_b16 a[206:207], v144 offset:1536\n"
"buffer_load_dwordx4 v204, s[8:11], s72 offen offset:3072 lds\n"
"s_add_u32 s72, s72, 0x36000\n"
"v_mul_f32 v82, v200, v82\n"
"v_fmac_f32 v82, v201, v66\n"
"v_mul_f32 v66, v200, v67\n"
"v_fma_f32 v66, -v201, v83, v66\n"
"v_mfma_f32_32x32x16_bf16 v[0:15], a[176:179], a[16:19], v[0:15]\n"
"ds_read_b64_tr_b16 a[160:161], v141 offset:2048\n"
"ds_read_b64_tr_b16 a[162:163], v141 offset:2560\n"
"v_mul_f32 v83, v200, v83\n"
"v_fmac_f32 v83, v201, v67\n"
"v_mul_f32 v67, v200, v68\n"
"v_fma_f32 v67, -v201, v84, v67\n"
"v_mul_f32 v84, v200, v84\n"
"v_fmac_f32 v84, v201, v68\n"
"v_mul_f32 v68, v200, v69\n"
"v_fma_f32 v68, -v201, v85, v68\n"
"v_mul_f32 v85, v200, v85\n"
"v_fmac_f32 v85, v201, v69\n"
"v_mfma_f32_32x32x16_bf16 v[16:31], a[180:183], a[16:19], v[16:31]\n"
"s_add_u32 m0, s88, 0x5000\n"
"ds_read_b64_tr_b16 a[164:165], v142 offset:2048\n"
"ds_read_b64_tr_b16 a[166:167], v142 offset:2560\n"
"buffer_load_dwordx4 v134, s[32:35], s99 offen offset:1024 lds\n"
"v_mul_f32 v69, v200, v70\n"
"v_fma_f32 v69, -v201, v86, v69\n"
"v_mul_f32 v86, v200, v86\n"
"v_fmac_f32 v86, v201, v70\n"
"v_mul_f32 v70, v200, v71\n"
"v_mfma_f32_32x32x16_bf16 v[32:47], a[184:187], a[16:19], v[32:47]\n"
"ds_read_b64_tr_b16 a[168:169], v143 offset:2048\n"
"ds_read_b64_tr_b16 a[170:171], v143 offset:2560\n"
"v_fma_f32 v70, -v201, v87, v70\n"
"v_mul_f32 v87, v200, v87\n"
"v_fmac_f32 v87, v201, v71\n"
"v_mul_f32 v71, v200, v72\n"
"v_fma_f32 v71, -v201, v88, v71\n"
"v_mul_f32 v88, v200, v88\n"
"v_fmac_f32 v88, v201, v72\n"
"v_mul_f32 v72, v200, v73\n"
"v_fma_f32 v72, -v201, v89, v72\n"
"v_mul_f32 v89, v200, v89\n"
"v_mfma_f32_32x32x16_bf16 v[48:63], a[188:191], a[16:19], v[48:63]\n"
"s_waitcnt lgkmcnt(6)\n"
"ds_read_b64_tr_b16 a[172:173], v144 offset:2048\n"
"ds_read_b64_tr_b16 a[174:175], v144 offset:2560\n"
"buffer_load_dwordx4 v135, s[32:35], s99 offen offset:2048 lds\n"
"v_fmac_f32 v89, v201, v73\n"
"v_mul_f32 v73, v200, v74\n"
"v_fma_f32 v73, -v201, v90, v73\n"
"v_mul_f32 v90, v200, v90\n"
"v_fmac_f32 v90, v201, v74\n"
"v_mfma_f32_32x32x16_bf16 v[0:15], a[192:195], a[20:23], v[0:15]\n"
"ds_read_b64_tr_b16 a[176:177], v141 offset:3072\n"
"ds_read_b64_tr_b16 a[178:179], v141 offset:3584\n"
"ds_read_b64_tr_b16 a[180:181], v142 offset:3072\n"
"v_mul_f32 v74, v200, v75\n"
"v_fma_f32 v74, -v201, v91, v74\n"
"v_mul_f32 v91, v200, v91\n"
"v_fmac_f32 v91, v201, v75\n"
"v_mul_f32 v75, v200, v76\n"
"v_fma_f32 v75, -v201, v92, v75\n"
"v_mul_f32 v92, v200, v92\n"
"v_fmac_f32 v92, v201, v76\n"
"v_mul_f32 v76, v200, v77\n"
"v_mfma_f32_32x32x16_bf16 v[16:31], a[196:199], a[20:23], v[16:31]\n"
"ds_read_b64_tr_b16 a[182:183], v142 offset:3584\n"
"ds_read_b64_tr_b16 a[184:185], v143 offset:3072\n"
"ds_read_b64_tr_b16 a[186:187], v143 offset:3584\n"
"buffer_load_dwordx4 v136, s[32:35], s99 offen offset:3072 lds\n"
"v_fma_f32 v76, -v201, v93, v76\n"
"v_mul_f32 v93, v200, v93\n"
"v_fmac_f32 v93, v201, v77\n"
"v_mul_f32 v77, v200, v78\n"
"v_mfma_f32_32x32x16_bf16 v[32:47], a[200:203], a[20:23], v[32:47]\n"
"ds_read_b64_tr_b16 a[188:189], v144 offset:3072\n"
"ds_read_b64_tr_b16 a[190:191], v144 offset:3584\n"
"v_fma_f32 v77, -v201, v94, v77\n"
"v_mul_f32 v94, v200, v94\n"
"v_fmac_f32 v94, v201, v78\n"
"v_mul_f32 v78, v200, v79\n"
"v_fma_f32 v78, -v201, v95, v78\n"
"v_mul_f32 v95, v200, v95\n"
"v_fmac_f32 v95, v201, v79\n"
"v_add_u32 v137, 0x10000, v128\n"
"v_add_u32 v138, 0x20000, v128\n"
"s_waitcnt vmcnt(16)\n"
"s_add_u32 s80, s76, 1\n"
"v_mov_b32 v230, s80\n"
"s_lshl_b32 s80, s66, 2\n"
"s_add_u32 s80, s80, s64\n"
"s_lshl_b32 s80, s80, 4\n"
"s_add_u32 s80, s80, s67\n"
"s_lshl_b32 s80, s80, 3\n"
"s_add_u32 s80, s80, 0x60000\n"
"buffer_store_dwordx2 v[230:231], v229, s[24:27], s80 offen sc1\n"
"v_mfma_f32_32x32x16_bf16 v[48:63], a[204:207], a[20:23], v[48:63]\n"
"s_waitcnt lgkmcnt(8)\n"
"s_lshl_b32 s80, s66, 2\n"
"s_add_u32 s80, s80, s64\n"
"s_mul_i32 s80, s80, 0x2f000\n"
"s_lshl_b32 s81, s96, 11\n"
"s_add_u32 s80, s80, s81\n"
"s_add_u32 s75, s80, 0x300000\n"
"s_waitcnt lgkmcnt(0)\n"
"s_barrier\n"
"v_mfma_f32_32x32x16_bf16 v[0:15], a[160:163], a[24:27], v[0:15]\n"
"s_add_u32 m0, s65, 0x18000\n"
"ds_read_b64_tr_b16 a[192:193], v141 offset:16384\n"
"ds_read_b64_tr_b16 a[194:195], v141 offset:16896\n"
"buffer_load_dwordx4 v131, s[8:11], s72 offen lds\n"
"v_mfma_f32_32x32x16_bf16 v[16:31], a[164:167], a[24:27], v[16:31]\n"
"ds_read_b64_tr_b16 a[196:197], v142 offset:16384\n"
"ds_read_b64_tr_b16 a[198:199], v142 offset:16896\n"
"v_mfma_f32_32x32x16_bf16 v[32:47], a[168:171], a[24:27], v[32:47]\n"
"ds_read_b64_tr_b16 a[200:201], v143 offset:16384\n"
"ds_read_b64_tr_b16 a[202:203], v143 offset:16896\n"
"v_mfma_f32_32x32x16_bf16 v[48:63], a[172:175], a[24:27], v[48:63]\n"
"ds_read_b64_tr_b16 a[204:205], v144 offset:16384\n"
"ds_read_b64_tr_b16 a[206:207], v144 offset:16896\n"
"buffer_load_dwordx4 v202, s[8:11], s72 offen offset:1024 lds\n"
"v_mfma_f32_32x32x16_bf16 v[0:15], a[176:179], a[28:31], v[0:15]\n"
"ds_read_b64_tr_b16 a[160:161], v141 offset:17408\n"
"ds_read_b64_tr_b16 a[162:163], v141 offset:17920\n"
"v_mfma_f32_32x32x16_bf16 v[16:31], a[180:183], a[28:31], v[16:31]\n"
"ds_read_b64_tr_b16 a[164:165], v142 offset:17408\n"
"ds_read_b64_tr_b16 a[166:167], v142 offset:17920\n"
"buffer_load_dwordx4 v203, s[8:11], s72 offen offset:2048 lds\n"
"v_mfma_f32_32x32x16_bf16 v[32:47], a[184:187], a[28:31], v[32:47]\n"
"ds_read_b64_tr_b16 a[168:169], v143 offset:17408\n"
"ds_read_b64_tr_b16 a[170:171], v143 offset:17920\n"
"v_mfma_f32_32x32x16_bf16 v[48:63], a[188:191], a[28:31], v[48:63]\n"
"s_waitcnt lgkmcnt(6)\n"
"ds_read_b64_tr_b16 a[172:173], v144 offset:17408\n"
"ds_read_b64_tr_b16 a[174:175], v144 offset:17920\n"
"v_mfma_f32_32x32x16_bf16 v[0:15], a[192:195], a[32:35], v[0:15]\n"
"ds_read_b64_tr_b16 a[176:177], v141 offset:18432\n"
"ds_read_b64_tr_b16 a[178:179], v141 offset:18944\n"
"buffer_load_dwordx4 v204, s[8:11], s72 offen offset:3072 lds\n"
"s_add_u32 s72, s72, 0x36000\n"
"v_mfma_f32_32x32x16_bf16 v[16:31], a[196:199], a[32:35], v[16:31]\n"
"ds_read_b64_tr_b16 a[180:181], v142 offset:18432\n"
"ds_read_b64_tr_b16 a[182:183], v142 offset:18944\n"
"v_mfma_f32_32x32x16_bf16 v[32:47], a[200:203], a[32:35], v[32:47]\n"
"ds_read_b64_tr_b16 a[184:185], v143 offset:18432\n"
"ds_read_b64_tr_b16 a[186:187], v143 offset:18944\n"
"v_mfma_f32_32x32x16_bf16 v[48:63], a[204:207], a[32:35], v[48:63]\n"
"s_waitcnt lgkmcnt(6)\n"
"s_add_u32 m0, s88, 0x6000\n"
"ds_read_b64_tr_b16 a[188:189], v144 offset:18432\n"
"ds_read_b64_tr_b16 a[190:191], v144 offset:18944\n"
"buffer_load_dwordx4 v129, s[32:35], s99 offen lds\n"
"v_mfma_f32_32x32x16_bf16 v[0:15], a[160:163], a[36:39], v[0:15]\n"
"ds_read_b64_tr_b16 a[192:193], v141 offset:19456\n"
"ds_read_b64_tr_b16 a[194:195], v141 offset:19968\n"
"ds_read_b64_tr_b16 a[196:197], v142 offset:19456\n"
"v_mfma_f32_32x32x16_bf16 v[16:31], a[164:167], a[36:39], v[16:31]\n"
"ds_read_b64_tr_b16 a[198:199], v142 offset:19968\n"
"ds_read_b64_tr_b16 a[200:201], v143 offset:19456\n"
"ds_read_b64_tr_b16 a[202:203], v143 offset:19968\n"
"v_mfma_f32_32x32x16_bf16 v[32:47], a[168:171], a[36:39], v[32:47]\n"
"ds_read_b64_tr_b16 a[204:205], v144 offset:19456\n"
"ds_read_b64_tr_b16 a[206:207], v144 offset:19968\n"
"v_mfma_f32_32x32x16_bf16 v[48:63], a[172:175], a[36:39], v[48:63]\n"
"s_waitcnt lgkmcnt(8)\n"
"s_waitcnt vmcnt(17)\n"
"s_waitcnt lgkmcnt(0)\n"
"s_barrier\n"
"v_mfma_f32_32x32x16_bf16 v[0:15], a[176:179], a[40:43], v[0:15]\n"
"s_add_u32 m0, s65, 0x1c000\n"
"ds_read_b64_tr_b16 a[160:161], v141 offset:32768\n"
"ds_read_b64_tr_b16 a[162:163], v141 offset:33280\n"
"buffer_load_dwordx4 v131, s[8:11], s72 offen lds\n"
"v_mfma_f32_32x32x16_bf16 v[16:31], a[180:183], a[40:43], v[16:31]\n"
"s_mov_b32 s87, 0x1000\n"
"ds_read_b64_tr_b16 a[164:165], v142 offset:32768\n"
"ds_read_b64_tr_b16 a[166:167], v142 offset:33280\n"
"buffer_load_dwordx4 v[112:115], v128, s[24:27], s87 offen sc1\n"
"buffer_load_dwordx4 v[116:119], v128, s[24:27], s87 offen offset:1024 sc1\n"
"v_mfma_f32_32x32x16_bf16 v[32:47], a[184:187], a[40:43], v[32:47]\n"
"ds_read_b64_tr_b16 a[168:169], v143 offset:32768\n"
"ds_read_b64_tr_b16 a[170:171], v143 offset:33280\n"
"v_mfma_f32_32x32x16_bf16 v[48:63], a[188:191], a[40:43], v[48:63]\n"
"ds_read_b64_tr_b16 a[172:173], v144 offset:32768\n"
"ds_read_b64_tr_b16 a[174:175], v144 offset:33280\n"
"buffer_load_dwordx4 v202, s[8:11], s72 offen offset:1024 lds\n"
"v_mfma_f32_32x32x16_bf16 v[0:15], a[192:195], a[44:47], v[0:15]\n"
"s_lshl_b32 s68, s93, 9\n"
"ds_read_b64_tr_b16 a[176:177], v141 offset:33792\n"
"ds_read_b64_tr_b16 a[178:179], v141 offset:34304\n"
"s_add_u32 s68, s68, 0x20000\n"
"buffer_load_dwordx4 v[96:99], v217, s[24:27], s68 offen sc1\n"
"v_mfma_f32_32x32x16_bf16 v[16:31], a[196:199], a[44:47], v[16:31]\n"
"ds_read_b64_tr_b16 a[180:181], v142 offset:33792\n"
"ds_read_b64_tr_b16 a[182:183], v142 offset:34304\n"
"buffer_load_dwordx4 v203, s[8:11], s72 offen offset:2048 lds\n"
"buffer_load_dwordx4 v[100:103], v218, s[24:27], s68 offen sc1\n"
"v_mfma_f32_32x32x16_bf16 v[32:47], a[200:203], a[44:47], v[32:47]\n"
"ds_read_b64_tr_b16 a[184:185], v143 offset:33792\n"
"ds_read_b64_tr_b16 a[186:187], v143 offset:34304\n"
"v_mfma_f32_32x32x16_bf16 v[48:63], a[204:207], a[44:47], v[48:63]\n"
"s_waitcnt lgkmcnt(6)\n"
"ds_read_b64_tr_b16 a[188:189], v144 offset:33792\n"
"ds_read_b64_tr_b16 a[190:191], v144 offset:34304\n"
"v_mfma_f32_32x32x16_bf16 v[0:15], a[160:163], a[48:51], v[0:15]\n"
"ds_read_b64_tr_b16 a[192:193], v141 offset:34816\n"
"ds_read_b64_tr_b16 a[194:195], v141 offset:35328\n"
"buffer_load_dwordx4 v204, s[8:11], s72 offen offset:3072 lds\n"
"s_add_u32 s72, s72, 0x36000\n"
"v_mfma_f32_32x32x16_bf16 v[16:31], a[164:167], a[48:51], v[16:31]\n"
"ds_read_b64_tr_b16 a[196:197], v142 offset:34816\n"
"ds_read_b64_tr_b16 a[198:199], v142 offset:35328\n"
"v_mfma_f32_32x32x16_bf16 v[32:47], a[168:171], a[48:51], v[32:47]\n"
"ds_read_b64_tr_b16 a[200:201], v143 offset:34816\n"
"ds_read_b64_tr_b16 a[202:203], v143 offset:35328\n"
"v_mfma_f32_32x32x16_bf16 v[48:63], a[172:175], a[48:51], v[48:63]\n"
"s_waitcnt lgkmcnt(6)\n"
"ds_read_b64_tr_b16 a[204:205], v144 offset:34816\n"
"ds_read_b64_tr_b16 a[206:207], v144 offset:35328\n"
"v_mfma_f32_32x32x16_bf16 v[0:15], a[176:179], a[52:55], v[0:15]\n"
"ds_read_b64_tr_b16 a[160:161], v141 offset:35840\n"
"ds_read_b64_tr_b16 a[162:163], v141 offset:36352\n"
"ds_read_b64_tr_b16 a[164:165], v142 offset:35840\n"
"v_mfma_f32_32x32x16_bf16 v[16:31], a[180:183], a[52:55], v[16:31]\n"
"ds_read_b64_tr_b16 a[166:167], v142 offset:36352\n"
"ds_read_b64_tr_b16 a[168:169], v143 offset:35840\n"
"ds_read_b64_tr_b16 a[170:171], v143 offset:36352\n"
"v_mfma_f32_32x32x16_bf16 v[32:47], a[184:187], a[52:55], v[32:47]\n"
"ds_read_b64_tr_b16 a[172:173], v144 offset:35840\n"
"ds_read_b64_tr_b16 a[174:175], v144 offset:36352\n"
"v_mfma_f32_32x32x16_bf16 v[48:63], a[188:191], a[52:55], v[48:63]\n"
"s_waitcnt lgkmcnt(8)\n"
"s_waitcnt vmcnt(17)\n"
"s_waitcnt lgkmcnt(0)\n"
"s_barrier\n"
"v_mfma_f32_32x32x16_bf16 v[0:15], a[192:195], a[56:59], v[0:15]\n"
"s_add_u32 m0, s65, 0x20000\n"
"ds_read_b64_tr_b16 a[176:177], v141 offset:49152\n"
"ds_read_b64_tr_b16 a[178:179], v141 offset:49664\n"
"buffer_load_dwordx4 v131, s[8:11], s72 offen lds\n"
"v_mfma_f32_32x32x16_bf16 v[16:31], a[196:199], a[56:59], v[16:31]\n"
"ds_read_b64_tr_b16 a[180:181], v142 offset:49152\n"
"ds_read_b64_tr_b16 a[182:183], v142 offset:49664\n"
"v_mfma_f32_32x32x16_bf16 v[32:47], a[200:203], a[56:59], v[32:47]\n"
"ds_read_b64_tr_b16 a[184:185], v143 offset:49152\n"
"ds_read_b64_tr_b16 a[186:187], v143 offset:49664\n"
"v_mfma_f32_32x32x16_bf16 v[48:63], a[204:207], a[56:59], v[48:63]\n"
"ds_read_b64_tr_b16 a[188:189], v144 offset:49152\n"
"ds_read_b64_tr_b16 a[190:191], v144 offset:49664\n"
"buffer_load_dwordx4 v202, s[8:11], s72 offen offset:1024 lds\n"
"v_mfma_f32_32x32x16_bf16 v[0:15], a[160:163], a[60:63], v[0:15]\n"
"ds_read_b64_tr_b16 a[192:193], v141 offset:50176\n"
"ds_read_b64_tr_b16 a[194:195], v141 offset:50688\n"
"v_mfma_f32_32x32x16_bf16 v[16:31], a[164:167], a[60:63], v[16:31]\n"
"ds_read_b64_tr_b16 a[196:197], v142 offset:50176\n"
"ds_read_b64_tr_b16 a[198:199], v142 offset:50688\n"
"buffer_load_dwordx4 v203, s[8:11], s72 offen offset:2048 lds\n"
"v_mfma_f32_32x32x16_bf16 v[32:47], a[168:171], a[60:63], v[32:47]\n"
"ds_read_b64_tr_b16 a[200:201], v143 offset:50176\n"
"ds_read_b64_tr_b16 a[202:203], v143 offset:50688\n"
"v_mfma_f32_32x32x16_bf16 v[48:63], a[172:175], a[60:63], v[48:63]\n"
"s_waitcnt lgkmcnt(6)\n"
"ds_read_b64_tr_b16 a[204:205], v144 offset:50176\n"
"ds_read_b64_tr_b16 a[206:207], v144 offset:50688\n"
"v_mfma_f32_32x32x16_bf16 v[0:15], a[176:179], a[64:67], v[0:15]\n"
"ds_read_b64_tr_b16 a[160:161], v141 offset:51200\n"
"ds_read_b64_tr_b16 a[162:163], v141 offset:51712\n"
"buffer_load_dwordx4 v204, s[8:11], s72 offen offset:3072 lds\n"
"s_add_u32 s72, s72, 0x36000\n"
"s_waitcnt vmcnt(7)\n"
"v_xor_b32 v104, v97, v219\n"
"v_bitop3_b32 v104, v99, v220, v104 bitop3:0xbe\n"
"s_waitcnt vmcnt(5)\n"
"v_bitop3_b32 v104, v101, v221, v104 bitop3:0xbe\n"
"v_bitop3_b32 v104, v103, v222, v104 bitop3:0xbe\n"
"v_cmp_ne_u32 vcc, 0, v104\n"
"s_nop 0\n"
"s_cbranch_vccnz L%=_tM_slow\n"
"L%=_tM_back:\n"
"v_mfma_f32_32x32x16_bf16 v[16:31], a[180:183], a[64:67], v[16:31]\n"
"ds_read_b64_tr_b16 a[164:165], v142 offset:51200\n"
"ds_read_b64_tr_b16 a[166:167], v142 offset:51712\n"
"v_mul_f32 v236, v196, v96\n"
"v_mul_f32 v237, v196, v98\n"
"v_fma_f32 v236, -v197, v98, v236\n"
"v_fmac_f32 v237, v197, v96\n"
"v_fmac_f32 v236, v198, v100\n"
"v_fmac_f32 v237, v198, v102\n"
"v_mfma_f32_32x32x16_bf16 v[32:47], a[184:187], a[64:67], v[32:47]\n"
"ds_read_b64_tr_b16 a[168:169], v143 offset:51200\n"
"ds_read_b64_tr_b16 a[170:171], v143 offset:51712\n"
"v_fma_f32 v236, -v199, v102, v236\n"
"v_fmac_f32 v237, v199, v100\n"
"ds_write_b64 v214, v[236:237] offset:3840\n"
"v_mfma_f32_32x32x16_bf16 v[48:63], a[188:191], a[64:67], v[48:63]\n"
"s_waitcnt lgkmcnt(7)\n"
"ds_read_b64_tr_b16 a[172:173], v144 offset:51200\n"
"ds_read_b64_tr_b16 a[174:175], v144 offset:51712\n"
"v_mfma_f32_32x32x16_bf16 v[0:15], a[192:195], a[68:71], v[0:15]\n"
"ds_read_b64_tr_b16 a[176:177], v141 offset:52224\n"
"ds_read_b64_tr_b16 a[178:179], v141 offset:52736\n"
"ds_read_b64_tr_b16 a[180:181], v142 offset:52224\n"
"v_cmp_ne_u64 s[80:81], s[76:77], v[112:113]\n"
"v_cmp_ne_u64 s[82:83], s[76:77], v[114:115]\n"
"v_cmp_ne_u64 s[84:85], s[76:77], v[116:117]\n"
"v_cmp_ne_u64 vcc, s[76:77], v[118:119]\n"
"s_or_b64 s[80:81], s[80:81], s[82:83]\n"
"s_or_b64 s[84:85], s[84:85], vcc\n"
"s_and_b32 s84, s84, 0xffffff\n"
"s_mov_b32 s85, 0\n"
"s_or_b64 s[80:81], s[80:81], s[84:85]\n"
"s_cbranch_scc1 L%=_f0M_slow\n"
"L%=_f0M_back:\n"
"v_mfma_f32_32x32x16_bf16 v[16:31], a[196:199], a[68:71], v[16:31]\n"
"ds_read_b64_tr_b16 a[182:183], v142 offset:52736\n"
"ds_read_b64_tr_b16 a[184:185], v143 offset:52224\n"
"ds_read_b64_tr_b16 a[186:187], v143 offset:52736\n"
"v_mfma_f32_32x32x16_bf16 v[32:47], a[200:203], a[68:71], v[32:47]\n"
"ds_read_b64_tr_b16 a[188:189], v144 offset:52224\n"
"ds_read_b64_tr_b16 a[190:191], v144 offset:52736\n"
"v_mfma_f32_32x32x16_bf16 v[48:63], a[204:207], a[68:71], v[48:63]\n"
"s_waitcnt lgkmcnt(8)\n"
"s_waitcnt lgkmcnt(0)\n"
"s_barrier\n"
"v_mfma_f32_32x32x16_bf16 v[0:15], a[160:163], a[72:75], v[0:15]\n"
"s_add_u32 m0, s65, 0x24000\n"
"ds_read_b64_tr_b16 a[192:193], v141\n"
"ds_read_b64_tr_b16 a[194:195], v141 offset:512\n"
"buffer_load_dwordx4 v131, s[8:11], s72 offen lds\n"
"v_mfma_f32_32x32x16_bf16 v[16:31], a[164:167], a[72:75], v[16:31]\n"
"ds_read_b64_tr_b16 a[196:197], v142\n"
"ds_read_b64_tr_b16 a[198:199], v142 offset:512\n"
"ds_read_b64 v[96:97], v215 offset:3840\n"
"ds_read_b64 v[98:99], v215 offset:4096\n"
"ds_read_b64 v[100:101], v215 offset:4352\n"
"ds_read_b64 v[102:103], v215 offset:4608\n"
"ds_read_b64 v[104:105], v215 offset:4864\n"
"ds_read_b64 v[106:107], v215 offset:5120\n"
"v_mfma_f32_32x32x16_bf16 v[32:47], a[168:171], a[72:75], v[32:47]\n"
"s_lshl_b32 s80, s98, 2\n"
"ds_read_b64_tr_b16 a[200:201], v143\n"
"ds_read_b64_tr_b16 a[202:203], v143 offset:512\n"
"buffer_load_dwordx4 v202, s[8:11], s72 offen offset:1024 lds\n"
"ds_read_b64 v[108:109], v215 offset:5376\n"
"s_add_u32 s80, s80, s64\n"
"s_lshl_b32 s80, s80, 7\n"
"s_add_u32 s80, s80, 0x60000\n"
"v_and_b32 v120, 15, v253\n"
"v_lshl_add_u32 v120, v120, 3, s80\n"
"v_mfma_f32_32x32x16_bf16 v[48:63], a[172:175], a[72:75], v[48:63]\n"
"ds_read_b64_tr_b16 a[204:205], v144\n"
"ds_read_b64_tr_b16 a[206:207], v144 offset:512\n"
"ds_read_b64 v[110:111], v215 offset:5632\n"
"s_waitcnt lgkmcnt(10)\n"
"v_add_f32 v96, v96, v98\n"
"v_add_f32 v97, v97, v99\n"
"s_waitcnt lgkmcnt(8)\n"
"v_add_f32 v100, v100, v102\n"
"v_add_f32 v101, v101, v103\n"
"s_waitcnt lgkmcnt(6)\n"
"v_add_f32 v104, v104, v106\n"
"v_mfma_f32_32x32x16_bf16 v[0:15], a[176:179], a[76:79], v[0:15]\n"
"ds_read_b64_tr_b16 a[160:161], v141 offset:1024\n"
"ds_read_b64_tr_b16 a[162:163], v141 offset:1536\n"
"buffer_load_dwordx4 v203, s[8:11], s72 offen offset:2048 lds\n"
"v_add_f32 v105, v105, v107\n"
"buffer_load_dwordx2 v[122:123], v120, s[24:27], 0 offen sc1\n"
"v_mfma_f32_32x32x16_bf16 v[16:31], a[180:183], a[76:79], v[16:31]\n"
"ds_read_b64_tr_b16 a[164:165], v142 offset:1024\n"
"ds_read_b64_tr_b16 a[166:167], v142 offset:1536\n"
"s_waitcnt lgkmcnt(4)\n"
"v_add_f32 v108, v108, v110\n"
"v_add_f32 v109, v109, v111\n"
"v_add_f32 v96, v96, v100\n"
"v_add_f32 v97, v97, v101\n"
"v_add_f32 v104, v104, v108\n"
"v_add_f32 v105, v105, v109\n"
"v_mfma_f32_32x32x16_bf16 v[32:47], a[184:187], a[76:79], v[32:47]\n"
"ds_read_b64_tr_b16 a[168:169], v143 offset:1024\n"
"ds_read_b64_tr_b16 a[170:171], v143 offset:1536\n"
"buffer_load_dwordx4 v204, s[8:11], s72 offen offset:3072 lds\n"
"s_add_u32 s72, s72, 0x36000\n"
"s_lshr_b32 s82, s3, 3\n"
"s_lshl_b32 s82, s82, 2\n"
"s_add_u32 s82, s82, s64\n"
"s_mul_i32 s82, s82, 5\n"
"v_min_u32 v252, 4, v253\n"
"v_add_u32 v252, s82, v252\n"
"v_lshlrev_b32 v252, 7, v252\n"
"v_min_u32 v252, L%=_end-L%=_pc-8, v252\n"
"s_getpc_b64 s[80:81]\n"
"L%=_st_ct2:\n"
"s_sub_u32 s80, s80, L%=_st_ct2-L%=_pc\n"
"s_subb_u32 s81, s81, 0\n"
"global_load_dword v252, v252, s[80:81]\n"
"v_mfma_f32_32x32x16_bf16 v[48:63], a[188:191], a[76:79], v[48:63]\n"
"ds_read_b64_tr_b16 a[172:173], v144 offset:1024\n"
"ds_read_b64_tr_b16 a[174:175], v144 offset:1536\n"
"v_add_f32 v96, v96, v104\n"
"v_add_f32 v97, v97, v105\n"
"v_fmac_f32 v240, v194, v96\n"
"v_fmac_f32 v241, v194, v97\n"
"v_fma_f32 v240, -v195, v97, v240\n"
"v_fmac_f32 v241, v195, v96\n"
"v_mfma_f32_32x32x16_bf16 v[0:15], a[192:195], a[80:83], v[0:15]\n"
"s_mul_i32 s99, s1, 0x6e000\n"
"s_add_u32 s99, s99, s90\n"
"s_add_u32 s99, s99, 0\n"
"s_add_u32 m0, s88, 0xa000\n"
"ds_read_b64_tr_b16 a[176:177], v141 offset:2048\n"
"ds_read_b64_tr_b16 a[178:179], v141 offset:2560\n"
"buffer_load_dwordx4 v128, s[24:27], s99 offen sc1 lds\n"
"v_mfma_f32_32x32x16_bf16 v[16:31], a[196:199], a[80:83], v[16:31]\n"
"s_lshr_b32 s80, s93, 1\n"
"ds_read_b64_tr_b16 a[180:181], v142 offset:2048\n"
"ds_read_b64_tr_b16 a[182:183], v142 offset:2560\n"
"v_mul_f32 v236, v200, v240\n"
"v_mul_f32 v237, v200, v241\n"
"v_fma_f32 v236, -v201, v241, v236\n"
"v_fmac_f32 v237, v201, v240\n"
"s_lshl_b32 s80, s80, 2\n"
"s_and_b32 s81, s93, 1\n"
"s_add_u32 s80, s80, s81\n"
"s_add_u32 s80, s80, 54\n"
"s_lshl_b32 s80, s80, 10\n"
"s_lshl_b32 s81, s66, 2\n"
"s_add_u32 s81, s81, s64\n"
"s_mul_i32 s81, s81, 0x2f000\n"
"s_add_u32 s80, s80, s81\n"
"s_add_u32 s70, s80, 0x300000\n"
"v_mfma_f32_32x32x16_bf16 v[32:47], a[200:203], a[80:83], v[32:47]\n"
"ds_read_b64_tr_b16 a[184:185], v143 offset:2048\n"
"ds_read_b64_tr_b16 a[186:187], v143 offset:2560\n"
"buffer_load_dwordx4 v128, s[24:27], s99 offen offset:1024 sc1 lds\n"
"v_fmac_f32 v242, v190, v236\n"
"v_mfma_f32_32x32x16_bf16 v[48:63], a[204:207], a[80:83], v[48:63]\n"
"s_waitcnt lgkmcnt(6)\n"
"ds_read_b64_tr_b16 a[188:189], v144 offset:2048\n"
"ds_read_b64_tr_b16 a[190:191], v144 offset:2560\n"
"v_fmac_f32 v80, v190, v237\n"
"v_fma_f32 v242, -v191, v237, v242\n"
"v_fmac_f32 v80, v191, v236\n"
"v_fmac_f32 v64, v190, v242\n"
"v_fmac_f32 v81, v190, v80\n"
"v_fma_f32 v64, -v191, v80, v64\n"
"v_mfma_f32_32x32x16_bf16 v[0:15], a[160:163], a[84:87], v[0:15]\n"
"ds_read_b64_tr_b16 a[192:193], v141 offset:3072\n"
"ds_read_b64_tr_b16 a[194:195], v141 offset:3584\n"
"ds_read_b64_tr_b16 a[196:197], v142 offset:3072\n"
"buffer_load_dwordx4 v128, s[24:27], s99 offen offset:2048 sc1 lds\n"
"v_mfma_f32_32x32x16_bf16 v[16:31], a[164:167], a[84:87], v[16:31]\n"
"ds_read_b64_tr_b16 a[198:199], v142 offset:3584\n"
"ds_read_b64_tr_b16 a[200:201], v143 offset:3072\n"
"ds_read_b64_tr_b16 a[202:203], v143 offset:3584\n"
"v_fmac_f32 v81, v191, v242\n"
"v_fmac_f32 v65, v190, v64\n"
"v_fmac_f32 v82, v190, v81\n"
"v_fma_f32 v65, -v191, v81, v65\n"
"v_fmac_f32 v82, v191, v64\n"
"v_mfma_f32_32x32x16_bf16 v[32:47], a[168:171], a[84:87], v[32:47]\n"
"ds_read_b64_tr_b16 a[204:205], v144 offset:3072\n"
"ds_read_b64_tr_b16 a[206:207], v144 offset:3584\n"
"buffer_load_dwordx4 v128, s[24:27], s99 offen offset:3072 sc1 lds\n"
"v_fmac_f32 v66, v190, v65\n"
"v_mfma_f32_32x32x16_bf16 v[48:63], a[172:175], a[84:87], v[48:63]\n"
"s_waitcnt lgkmcnt(8)\n"
"v_fmac_f32 v83, v190, v82\n"
"v_fma_f32 v66, -v191, v82, v66\n"
"v_fmac_f32 v83, v191, v65\n"
"v_fmac_f32 v67, v190, v66\n"
"v_fmac_f32 v84, v190, v83\n"
"v_fma_f32 v67, -v191, v83, v67\n"
"v_fmac_f32 v84, v191, v66\n"
"v_fmac_f32 v68, v190, v67\n"
"s_waitcnt vmcnt(14)\n"
"s_waitcnt lgkmcnt(0)\n"
"s_barrier\n"
"v_mfma_f32_32x32x16_bf16 v[0:15], a[176:179], a[88:91], v[0:15]\n"
"s_add_u32 m0, s65, 0x18000\n"
"ds_read_b64_tr_b16 a[160:161], v141 offset:16384\n"
"ds_read_b64_tr_b16 a[162:163], v141 offset:16896\n"
"buffer_load_dwordx4 v131, s[8:11], s72 offen lds\n"
"v_mfma_f32_32x32x16_bf16 v[16:31], a[180:183], a[88:91], v[16:31]\n"
"ds_read_b64_tr_b16 a[164:165], v142 offset:16384\n"
"ds_read_b64_tr_b16 a[166:167], v142 offset:16896\n"
"v_fmac_f32 v85, v190, v84\n"
"v_fma_f32 v68, -v191, v84, v68\n"
"v_fmac_f32 v85, v191, v67\n"
"v_fmac_f32 v69, v190, v68\n"
"v_fmac_f32 v86, v190, v85\n"
"v_fma_f32 v69, -v191, v85, v69\n"
"v_mfma_f32_32x32x16_bf16 v[32:47], a[184:187], a[88:91], v[32:47]\n"
"ds_read_b64_tr_b16 a[168:169], v143 offset:16384\n"
"ds_read_b64_tr_b16 a[170:171], v143 offset:16896\n"
"buffer_load_dwordx4 v202, s[8:11], s72 offen offset:1024 lds\n"
"v_fmac_f32 v86, v191, v68\n"
"v_mfma_f32_32x32x16_bf16 v[48:63], a[188:191], a[88:91], v[48:63]\n"
"ds_read_b64_tr_b16 a[172:173], v144 offset:16384\n"
"ds_read_b64_tr_b16 a[174:175], v144 offset:16896\n"
"v_fmac_f32 v70, v190, v69\n"
"v_fmac_f32 v87, v190, v86\n"
"v_fma_f32 v70, -v191, v86, v70\n"
"v_fmac_f32 v87, v191, v69\n"
"v_cvt_pk_bf16_f32 v96, v242, v64\n"
"v_cvt_pk_bf16_f32 v97, v65, v66\n"
"v_mfma_f32_32x32x16_bf16 v[0:15], a[192:195], a[92:95], v[0:15]\n"
"ds_read_b64_tr_b16 a[176:177], v141 offset:17408\n"
"ds_read_b64_tr_b16 a[178:179], v141 offset:17920\n"
"buffer_load_dwordx4 v203, s[8:11], s72 offen offset:2048 lds\n"
"v_cvt_pk_bf16_f32 v98, v67, v68\n"
"v_mfma_f32_32x32x16_bf16 v[16:31], a[196:199], a[92:95], v[16:31]\n"
"ds_read_b64_tr_b16 a[180:181], v142 offset:17408\n"
"ds_read_b64_tr_b16 a[182:183], v142 offset:17920\n"
"v_cvt_pk_bf16_f32 v99, v69, v70\n"
"ds_write_b128 v227, v[96:99]\n"
"v_mfma_f32_32x32x16_bf16 v[32:47], a[200:203], a[92:95], v[32:47]\n"
"ds_read_b64_tr_b16 a[184:185], v143 offset:17408\n"
"ds_read_b64_tr_b16 a[186:187], v143 offset:17920\n"
"buffer_load_dwordx4 v204, s[8:11], s72 offen offset:3072 lds\n"
"s_add_u32 s72, s72, 0x36000\n"
"v_mfma_f32_32x32x16_bf16 v[48:63], a[204:207], a[92:95], v[48:63]\n"
"s_waitcnt lgkmcnt(7)\n"
"ds_read_b64_tr_b16 a[188:189], v144 offset:17408\n"
"ds_read_b64_tr_b16 a[190:191], v144 offset:17920\n"
"ds_read_b64_tr_b16 v[100:101], v228\n"
"ds_read_b64_tr_b16 v[102:103], v228 offset:128\n"
"v_cvt_pk_bf16_f32 v104, -v80, -v81\n"
"v_cvt_pk_bf16_f32 v105, -v82, -v83\n"
"v_cvt_pk_bf16_f32 v106, -v84, -v85\n"
"v_cvt_pk_bf16_f32 v107, -v86, -v87\n"
"v_mfma_f32_32x32x16_bf16 v[0:15], a[160:163], a[96:99], v[0:15]\n"
"s_add_u32 m0, s88, 0xb000\n"
"ds_read_b64_tr_b16 a[192:193], v141 offset:18432\n"
"ds_read_b64_tr_b16 a[194:195], v141 offset:18944\n"
"buffer_load_dwordx4 v130, s[24:27], s99 offen sc1 lds\n"
"ds_write_b128 v227, v[104:107]\n"
"v_mfma_f32_32x32x16_bf16 v[16:31], a[164:167], a[96:99], v[16:31]\n"
"ds_read_b64_tr_b16 a[196:197], v142 offset:18432\n"
"ds_read_b64_tr_b16 a[198:199], v142 offset:18944\n"
"ds_read_b64_tr_b16 v[108:109], v228\n"
"ds_read_b64_tr_b16 v[110:111], v228 offset:128\n"
"s_waitcnt lgkmcnt(7)\n"
"buffer_store_dwordx4 v[100:103], v128, s[24:27], s70 offen sc1\n"
"v_mfma_f32_32x32x16_bf16 v[32:47], a[168:171], a[96:99], v[32:47]\n"
"s_mul_i32 s99, s1, 0x6e000\n"
"s_add_u32 s99, s99, s90\n"
"s_add_u32 s99, s99, 0x28000\n"
"s_add_u32 m0, s88, 0xf000\n"
"ds_read_b64_tr_b16 a[200:201], v143 offset:18432\n"
"ds_read_b64_tr_b16 a[202:203], v143 offset:18944\n"
"buffer_load_dwordx4 v128, s[24:27], s99 offen sc1 lds\n"
"v_mfma_f32_32x32x16_bf16 v[48:63], a[172:175], a[96:99], v[48:63]\n"
"ds_read_b64_tr_b16 a[204:205], v144 offset:18432\n"
"ds_read_b64_tr_b16 a[206:207], v144 offset:18944\n"
"s_waitcnt lgkmcnt(4)\n"
"buffer_store_dwordx4 v[108:111], v128, s[24:27], s70 offen offset:2048 sc1\n"
"v_mfma_f32_32x32x16_bf16 v[0:15], a[176:179], a[100:103], v[0:15]\n"
"s_add_u32 s80, s76, 1\n"
"ds_read_b64_tr_b16 a[160:161], v141 offset:19456\n"
"ds_read_b64_tr_b16 a[162:163], v141 offset:19968\n"
"ds_read_b64_tr_b16 a[164:165], v142 offset:19456\n"
"buffer_load_dwordx4 v128, s[24:27], s99 offen offset:1024 sc1 lds\n"
"s_waitcnt vmcnt(15)\n"
"v_subrev_u32 v121, s80, v122\n"
"v_cmp_lt_u32 vcc, 2, v121\n"
"v_cmp_ne_u32 s[82:83], s77, v123\n"
"s_nop 0\n"
"s_or_b32 s82, s82, vcc_lo\n"
"s_and_b32 s82, s82, 0x7ff\n"
"s_cbranch_scc1 L%=_f2aM_slow\n"
"L%=_f2aM_back:\n"
"v_mfma_f32_32x32x16_bf16 v[16:31], a[180:183], a[100:103], v[16:31]\n"
"s_add_u32 s70, s70, 0x17800\n"
"ds_read_b64_tr_b16 a[166:167], v142 offset:19968\n"
"ds_read_b64_tr_b16 a[168:169], v143 offset:19456\n"
"ds_read_b64_tr_b16 a[170:171], v143 offset:19968\n"
"v_fmac_f32 v71, v190, v70\n"
"v_fmac_f32 v88, v190, v87\n"
"v_fma_f32 v71, -v191, v87, v71\n"
"v_fmac_f32 v88, v191, v70\n"
"v_mfma_f32_32x32x16_bf16 v[32:47], a[184:187], a[100:103], v[32:47]\n"
"s_add_u32 m0, s88, 0xf000\n"
"ds_read_b64_tr_b16 a[172:173], v144 offset:19456\n"
"ds_read_b64_tr_b16 a[174:175], v144 offset:19968\n"
"buffer_load_dwordx4 v128, s[24:27], s99 offen offset:2048 sc1 lds\n"
"v_fmac_f32 v72, v190, v71\n"
"v_mfma_f32_32x32x16_bf16 v[48:63], a[188:191], a[100:103], v[48:63]\n"
"s_waitcnt lgkmcnt(8)\n"
"v_fmac_f32 v89, v190, v88\n"
"v_fma_f32 v72, -v191, v88, v72\n"
"v_fmac_f32 v89, v191, v71\n"
"v_fmac_f32 v73, v190, v72\n"
"v_fmac_f32 v90, v190, v89\n"
"v_fma_f32 v73, -v191, v89, v73\n"
"v_fmac_f32 v90, v191, v72\n"
"v_fmac_f32 v74, v190, v73\n"
"s_waitcnt lgkmcnt(0)\n"
"s_barrier\n"
"v_mfma_f32_32x32x16_bf16 v[0:15], a[192:195], a[104:107], v[0:15]\n"
"ds_read_b64_tr_b16 a[176:177], v141 offset:32768\n"
"ds_read_b64_tr_b16 a[178:179], v141 offset:33280\n"
"buffer_load_dwordx4 v128, s[24:27], s99 offen offset:3072 sc1 lds\n"
"v_mfma_f32_32x32x16_bf16 v[16:31], a[196:199], a[104:107], v[16:31]\n"
"ds_read_b64_tr_b16 a[180:181], v142 offset:32768\n"
"ds_read_b64_tr_b16 a[182:183], v142 offset:33280\n"
"v_fmac_f32 v91, v190, v90\n"
"v_fma_f32 v74, -v191, v90, v74\n"
"v_fmac_f32 v91, v191, v73\n"
"v_fmac_f32 v75, v190, v74\n"
"v_fmac_f32 v92, v190, v91\n"
"v_fma_f32 v75, -v191, v91, v75\n"
"v_mfma_f32_32x32x16_bf16 v[32:47], a[200:203], a[104:107], v[32:47]\n"
"s_add_u32 m0, s88, 0x10000\n"
"ds_read_b64_tr_b16 a[184:185], v143 offset:32768\n"
"ds_read_b64_tr_b16 a[186:187], v143 offset:33280\n"
"buffer_load_dwordx4 v130, s[24:27], s99 offen sc1 lds\n"
"v_fmac_f32 v92, v191, v74\n"
"v_mfma_f32_32x32x16_bf16 v[48:63], a[204:207], a[104:107], v[48:63]\n"
"ds_read_b64_tr_b16 a[188:189], v144 offset:32768\n"
"ds_read_b64_tr_b16 a[190:191], v144 offset:33280\n"
"v_fmac_f32 v76, v190, v75\n"
"v_fmac_f32 v93, v190, v92\n"
"v_fma_f32 v76, -v191, v92, v76\n"
"v_fmac_f32 v93, v191, v75\n"
"v_fmac_f32 v77, v190, v76\n"
"v_fmac_f32 v94, v190, v93\n"
"v_mfma_f32_32x32x16_bf16 v[0:15], a[160:163], a[108:111], v[0:15]\n"
"s_mul_i32 s89, s1, 0x2c00\n"
"s_add_u32 s89, s89, s92\n"
"s_add_u32 s89, s89, 0\n"
"s_add_u32 s74, s89, s0\n"
"ds_read_b64_tr_b16 a[192:193], v141 offset:33792\n"
"ds_read_b64_tr_b16 a[194:195], v141 offset:34304\n"
"buffer_load_dwordx4 a[208:211], v128, s[24:27], s89 offen sc1\n"
"v_mfma_f32_32x32x16_bf16 v[16:31], a[164:167], a[108:111], v[16:31]\n"
"ds_read_b64_tr_b16 a[196:197], v142 offset:33792\n"
"ds_read_b64_tr_b16 a[198:199], v142 offset:34304\n"
"v_fma_f32 v77, -v191, v93, v77\n"
"v_fmac_f32 v94, v191, v76\n"
"v_fmac_f32 v78, v190, v77\n"
"v_fmac_f32 v95, v190, v94\n"
"v_fma_f32 v78, -v191, v94, v78\n"
"v_fmac_f32 v95, v191, v77\n"
"v_mfma_f32_32x32x16_bf16 v[32:47], a[168:171], a[108:111], v[32:47]\n"
"ds_read_b64_tr_b16 a[200:201], v143 offset:33792\n"
"ds_read_b64_tr_b16 a[202:203], v143 offset:34304\n"
"buffer_load_dwordx4 a[212:215], v128, s[24:27], s74 offen sc1\n"
"v_cvt_pk_bf16_f32 v96, v71, v72\n"
"v_cvt_pk_bf16_f32 v97, v73, v74\n"
"v_mfma_f32_32x32x16_bf16 v[48:63], a[172:175], a[108:111], v[48:63]\n"
"s_waitcnt lgkmcnt(6)\n"
"ds_read_b64_tr_b16 a[204:205], v144 offset:33792\n"
"ds_read_b64_tr_b16 a[206:207], v144 offset:34304\n"
"v_cvt_pk_bf16_f32 v98, v75, v76\n"
"v_cvt_pk_bf16_f32 v99, v77, v78\n"
"ds_write_b128 v227, v[96:99]\n"
"v_mfma_f32_32x32x16_bf16 v[0:15], a[176:179], a[112:115], v[0:15]\n"
"ds_read_b64_tr_b16 a[160:161], v141 offset:34816\n"
"ds_read_b64_tr_b16 a[162:163], v141 offset:35328\n"
"buffer_load_dwordx4 a[216:219], v128, s[24:27], s89 offen offset:1024 sc1\n"
"ds_read_b64_tr_b16 v[100:101], v228\n"
"v_mfma_f32_32x32x16_bf16 v[16:31], a[180:183], a[112:115], v[16:31]\n"
"ds_read_b64_tr_b16 a[164:165], v142 offset:34816\n"
"ds_read_b64_tr_b16 a[166:167], v142 offset:35328\n"
"ds_read_b64_tr_b16 v[102:103], v228 offset:128\n"
"v_cvt_pk_bf16_f32 v104, -v88, -v89\n"
"v_cvt_pk_bf16_f32 v105, -v90, -v91\n"
"v_cvt_pk_bf16_f32 v106, -v92, -v93\n"
"v_cvt_pk_bf16_f32 v107, -v94, -v95\n"
"v_mfma_f32_32x32x16_bf16 v[32:47], a[184:187], a[112:115], v[32:47]\n"
"ds_read_b64_tr_b16 a[168:169], v143 offset:34816\n"
"ds_read_b64_tr_b16 a[170:171], v143 offset:35328\n"
"buffer_load_dwordx4 a[220:223], v128, s[24:27], s74 offen offset:1024 sc1\n"
"ds_write_b128 v227, v[104:107]\n"
"v_mfma_f32_32x32x16_bf16 v[48:63], a[188:191], a[112:115], v[48:63]\n"
"s_waitcnt lgkmcnt(10)\n"
"ds_read_b64_tr_b16 a[172:173], v144 offset:34816\n"
"ds_read_b64_tr_b16 a[174:175], v144 offset:35328\n"
"ds_read_b64_tr_b16 v[108:109], v228\n"
"ds_read_b64_tr_b16 v[110:111], v228 offset:128\n"
"s_waitcnt lgkmcnt(7)\n"
"buffer_store_dwordx4 v[100:103], v128, s[24:27], s70 offen sc1\n"
"v_mfma_f32_32x32x16_bf16 v[0:15], a[192:195], a[116:119], v[0:15]\n"
"ds_read_b64_tr_b16 a[176:177], v141 offset:35840\n"
"ds_read_b64_tr_b16 a[178:179], v141 offset:36352\n"
"ds_read_b64_tr_b16 a[180:181], v142 offset:35840\n"
"buffer_load_dwordx4 a[224:227], v128, s[24:27], s89 offen offset:2048 sc1\n"
"s_waitcnt lgkmcnt(3)\n"
"buffer_store_dwordx4 v[108:111], v128, s[24:27], s70 offen offset:2048 sc1\n"
"v_mfma_f32_32x32x16_bf16 v[16:31], a[196:199], a[116:119], v[16:31]\n"
"ds_read_b64_tr_b16 a[182:183], v142 offset:36352\n"
"ds_read_b64_tr_b16 a[184:185], v143 offset:35840\n"
"ds_read_b64_tr_b16 a[186:187], v143 offset:36352\n"
"v_mfma_f32_32x32x16_bf16 v[32:47], a[200:203], a[116:119], v[32:47]\n"
"ds_read_b64_tr_b16 a[188:189], v144 offset:35840\n"
"ds_read_b64_tr_b16 a[190:191], v144 offset:36352\n"
"buffer_load_dwordx4 a[228:231], v128, s[24:27], s74 offen offset:2048 sc1\n"
"v_mfma_f32_32x32x16_bf16 v[48:63], a[204:207], a[116:119], v[48:63]\n"
"s_waitcnt vmcnt(25)\n"
"s_waitcnt lgkmcnt(0)\n"
"s_barrier\n"
"v_mfma_f32_32x32x16_bf16 v[0:15], a[160:163], a[120:123], v[0:15]\n"
"s_mul_i32 s99, s1, 0x6e000\n"
"s_add_u32 s99, s99, s90\n"
"s_add_u32 s99, s99, 0x50000\n"
"s_mul_i32 s79, s1, 0xfffc4000\n"
"s_add_u32 s79, s79, 0x14a000\n"
"s_cmp_ge_u32 s64, 3\n"
"s_cselect_b32 s79, s79, 0\n"
"s_add_u32 s99, s99, s79\n"
"s_add_u32 m0, s88, 0x1e000\n"
"ds_read_b64_tr_b16 a[192:193], v141 offset:49152\n"
"ds_read_b64_tr_b16 a[194:195], v141 offset:49664\n"
"buffer_load_dwordx4 v128, s[24:27], s99 offen sc1 lds\n"
"v_mfma_f32_32x32x16_bf16 v[16:31], a[164:167], a[120:123], v[16:31]\n"
"ds_read_b64_tr_b16 a[196:197], v142 offset:49152\n"
"ds_read_b64_tr_b16 a[198:199], v142 offset:49664\n"
"v_mfma_f32_32x32x16_bf16 v[32:47], a[168:171], a[120:123], v[32:47]\n"
"ds_read_b64_tr_b16 a[200:201], v143 offset:49152\n"
"ds_read_b64_tr_b16 a[202:203], v143 offset:49664\n"
"buffer_load_dwordx4 v128, s[24:27], s99 offen offset:1024 sc1 lds\n"
"v_mfma_f32_32x32x16_bf16 v[48:63], a[172:175], a[120:123], v[48:63]\n"
"ds_read_b64_tr_b16 a[204:205], v144 offset:49152\n"
"ds_read_b64_tr_b16 a[206:207], v144 offset:49664\n"
"v_mfma_f32_32x32x16_bf16 v[0:15], a[176:179], a[124:127], v[0:15]\n"
"ds_read_b64_tr_b16 a[160:161], v141 offset:50176\n"
"ds_read_b64_tr_b16 a[162:163], v141 offset:50688\n"
"buffer_load_dwordx4 v128, s[24:27], s99 offen offset:2048 sc1 lds\n"
"v_mfma_f32_32x32x16_bf16 v[16:31], a[180:183], a[124:127], v[16:31]\n"
"ds_read_b64_tr_b16 a[164:165], v142 offset:50176\n"
"ds_read_b64_tr_b16 a[166:167], v142 offset:50688\n"
"v_mfma_f32_32x32x16_bf16 v[32:47], a[184:187], a[124:127], v[32:47]\n"
"ds_read_b64_tr_b16 a[168:169], v143 offset:50176\n"
"ds_read_b64_tr_b16 a[170:171], v143 offset:50688\n"
"buffer_load_dwordx4 v128, s[24:27], s99 offen offset:3072 sc1 lds\n"
"v_mfma_f32_32x32x16_bf16 v[48:63], a[188:191], a[124:127], v[48:63]\n"
"s_waitcnt lgkmcnt(6)\n"
"ds_read_b64_tr_b16 a[172:173], v144 offset:50176\n"
"ds_read_b64_tr_b16 a[174:175], v144 offset:50688\n"
"v_mfma_f32_32x32x16_bf16 v[0:15], a[192:195], a[128:131], v[0:15]\n"
"s_add_u32 m0, s88, 0x1f000\n"
"ds_read_b64_tr_b16 a[176:177], v141 offset:51200\n"
"ds_read_b64_tr_b16 a[178:179], v141 offset:51712\n"
"buffer_load_dwordx4 v130, s[24:27], s99 offen sc1 lds\n"
"s_lshr_b32 s82, s3, 3\n"
"s_lshl_b32 s82, s82, 2\n"
"s_add_u32 s82, s82, s64\n"
"s_mul_i32 s82, s82, 5\n"
"v_min_u32 v251, 4, v253\n"
"v_add_u32 v251, s82, v251\n"
"v_lshlrev_b32 v251, 7, v251\n"
"v_min_u32 v251, L%=_end-L%=_pc-8, v251\n"
"s_getpc_b64 s[80:81]\n"
"L%=_st_ct3:\n"
"s_sub_u32 s80, s80, L%=_st_ct3-L%=_pc\n"
"s_subb_u32 s81, s81, 0\n"
"global_load_dword v251, v251, s[80:81]\n"
"v_mfma_f32_32x32x16_bf16 v[16:31], a[196:199], a[128:131], v[16:31]\n"
"ds_read_b64_tr_b16 a[180:181], v142 offset:51200\n"
"ds_read_b64_tr_b16 a[182:183], v142 offset:51712\n"
"v_mfma_f32_32x32x16_bf16 v[32:47], a[200:203], a[128:131], v[32:47]\n"
"ds_read_b64_tr_b16 a[184:185], v143 offset:51200\n"
"ds_read_b64_tr_b16 a[186:187], v143 offset:51712\n"
"buffer_load_dwordx4 a[232:235], v128, s[24:27], s89 offen offset:3072 sc1\n"
"v_mfma_f32_32x32x16_bf16 v[48:63], a[204:207], a[128:131], v[48:63]\n"
"s_waitcnt lgkmcnt(6)\n"
"ds_read_b64_tr_b16 a[188:189], v144 offset:51200\n"
"ds_read_b64_tr_b16 a[190:191], v144 offset:51712\n"
"v_mfma_f32_32x32x16_bf16 v[0:15], a[160:163], a[132:135], v[0:15]\n"
"ds_read_b64_tr_b16 a[192:193], v141 offset:52224\n"
"ds_read_b64_tr_b16 a[194:195], v141 offset:52736\n"
"ds_read_b64_tr_b16 a[196:197], v142 offset:52224\n"
"buffer_load_dwordx4 a[236:239], v128, s[24:27], s74 offen offset:3072 sc1\n"
"v_mfma_f32_32x32x16_bf16 v[16:31], a[164:167], a[132:135], v[16:31]\n"
"ds_read_b64_tr_b16 a[198:199], v142 offset:52736\n"
"ds_read_b64_tr_b16 a[200:201], v143 offset:52224\n"
"ds_read_b64_tr_b16 a[202:203], v143 offset:52736\n"
"v_mfma_f32_32x32x16_bf16 v[32:47], a[168:171], a[132:135], v[32:47]\n"
"s_mul_i32 s89, s1, 0x2c00\n"
"s_add_u32 s89, s89, s92\n"
"s_add_u32 s89, s89, 0x1000\n"
"s_add_u32 s74, s89, s0\n"
"ds_read_b64_tr_b16 a[204:205], v144 offset:52224\n"
"ds_read_b64_tr_b16 a[206:207], v144 offset:52736\n"
"buffer_load_dwordx4 a[240:243], v128, s[24:27], s89 offen sc1\n"
"v_mfma_f32_32x32x16_bf16 v[48:63], a[172:175], a[132:135], v[48:63]\n"
"s_waitcnt lgkmcnt(8)\n"
"s_waitcnt vmcnt(25)\n"
"s_waitcnt lgkmcnt(0)\n"
"s_barrier\n"
"v_mfma_f32_32x32x16_bf16 v[0:15], a[176:179], a[136:139], v[0:15]\n"
"s_mul_i32 s99, s1, 0x32000\n"
"s_add_u32 s99, s99, s90\n"
"s_add_u32 s99, s99, 0x1c2000\n"
"s_add_u32 m0, s88, 0x23000\n"
"ds_read_b64_tr_b16 a[160:161], v141\n"
"ds_read_b64_tr_b16 a[162:163], v141 offset:512\n"
"buffer_load_dwordx4 v128, s[24:27], s99 offen sc1 lds\n"
"v_mfma_f32_32x32x16_bf16 v[16:31], a[180:183], a[136:139], v[16:31]\n"
"ds_read_b64_tr_b16 a[164:165], v142\n"
"ds_read_b64_tr_b16 a[166:167], v142 offset:512\n"
"v_mfma_f32_32x32x16_bf16 v[32:47], a[184:187], a[136:139], v[32:47]\n"
"ds_read_b64_tr_b16 a[168:169], v143\n"
"ds_read_b64_tr_b16 a[170:171], v143 offset:512\n"
"buffer_load_dwordx4 v128, s[24:27], s99 offen offset:1024 sc1 lds\n"
"v_mfma_f32_32x32x16_bf16 v[48:63], a[188:191], a[136:139], v[48:63]\n"
"ds_read_b64_tr_b16 a[172:173], v144\n"
"ds_read_b64_tr_b16 a[174:175], v144 offset:512\n"
"v_mfma_f32_32x32x16_bf16 v[0:15], a[192:195], a[140:143], v[0:15]\n"
"ds_read_b64_tr_b16 a[176:177], v141 offset:1024\n"
"ds_read_b64_tr_b16 a[178:179], v141 offset:1536\n"
"buffer_load_dwordx4 v128, s[24:27], s99 offen offset:2048 sc1 lds\n"
"s_waitcnt vmcnt(13)\n"
"s_add_u32 s80, s76, 2\n"
"v_mov_b32 v230, s80\n"
"s_lshl_b32 s80, s66, 2\n"
"s_add_u32 s80, s80, s64\n"
"s_lshl_b32 s80, s80, 4\n"
"s_add_u32 s80, s80, s67\n"
"s_lshl_b32 s80, s80, 3\n"
"s_add_u32 s80, s80, 0x62000\n"
"buffer_store_dwordx2 v[230:231], v229, s[24:27], s80 offen sc1\n"
"v_mfma_f32_32x32x16_bf16 v[16:31], a[196:199], a[140:143], v[16:31]\n"
"ds_read_b64_tr_b16 a[180:181], v142 offset:1024\n"
"ds_read_b64_tr_b16 a[182:183], v142 offset:1536\n"
"v_mfma_f32_32x32x16_bf16 v[32:47], a[200:203], a[140:143], v[32:47]\n"
"ds_read_b64_tr_b16 a[184:185], v143 offset:1024\n"
"ds_read_b64_tr_b16 a[186:187], v143 offset:1536\n"
"buffer_load_dwordx4 v128, s[24:27], s99 offen offset:3072 sc1 lds\n"
"v_mfma_f32_32x32x16_bf16 v[48:63], a[204:207], a[140:143], v[48:63]\n"
"s_waitcnt lgkmcnt(6)\n"
"ds_read_b64_tr_b16 a[188:189], v144 offset:1024\n"
"ds_read_b64_tr_b16 a[190:191], v144 offset:1536\n"
"v_mfma_f32_32x32x16_bf16 v[0:15], a[160:163], a[144:147], v[0:15]\n"
"s_add_u32 m0, s88, 0x24000\n"
"ds_read_b64_tr_b16 a[192:193], v141 offset:2048\n"
"ds_read_b64_tr_b16 a[194:195], v141 offset:2560\n"
"buffer_load_dwordx4 v130, s[24:27], s99 offen sc1 lds\n"
"v_mfma_f32_32x32x16_bf16 v[16:31], a[164:167], a[144:147], v[16:31]\n"
"ds_read_b64_tr_b16 a[196:197], v142 offset:2048\n"
"ds_read_b64_tr_b16 a[198:199], v142 offset:2560\n"
"v_mfma_f32_32x32x16_bf16 v[32:47], a[168:171], a[144:147], v[32:47]\n"
"ds_read_b64_tr_b16 a[200:201], v143 offset:2048\n"
"ds_read_b64_tr_b16 a[202:203], v143 offset:2560\n"
"buffer_load_dwordx4 a[244:247], v128, s[24:27], s74 offen sc1\n"
"v_mfma_f32_32x32x16_bf16 v[48:63], a[172:175], a[144:147], v[48:63]\n"
"s_waitcnt lgkmcnt(6)\n"
"ds_read_b64_tr_b16 a[204:205], v144 offset:2048\n"
"ds_read_b64_tr_b16 a[206:207], v144 offset:2560\n"
"v_mfma_f32_32x32x16_bf16 v[0:15], a[176:179], a[148:151], v[0:15]\n"
"ds_read_b64_tr_b16 a[160:161], v141 offset:3072\n"
"ds_read_b64_tr_b16 a[162:163], v141 offset:3584\n"
"ds_read_b64_tr_b16 a[164:165], v142 offset:3072\n"
"buffer_load_dwordx4 a[248:251], v128, s[24:27], s89 offen offset:1024 sc1\n"
"v_mfma_f32_32x32x16_bf16 v[16:31], a[180:183], a[148:151], v[16:31]\n"
"ds_read_b64_tr_b16 a[166:167], v142 offset:3584\n"
"ds_read_b64_tr_b16 a[168:169], v143 offset:3072\n"
"ds_read_b64_tr_b16 a[170:171], v143 offset:3584\n"
"v_mfma_f32_32x32x16_bf16 v[32:47], a[184:187], a[148:151], v[32:47]\n"
"ds_read_b64_tr_b16 a[172:173], v144 offset:3072\n"
"ds_read_b64_tr_b16 a[174:175], v144 offset:3584\n"
"buffer_load_dwordx4 a[252:255], v128, s[24:27], s74 offen offset:1024 sc1\n"
"v_mfma_f32_32x32x16_bf16 v[48:63], a[188:191], a[148:151], v[48:63]\n"
"s_waitcnt lgkmcnt(8)\n"
"s_waitcnt lgkmcnt(0)\n"
"s_barrier\n"
"v_mfma_f32_32x32x16_bf16 v[0:15], a[192:195], a[152:155], v[0:15]\n"
"ds_read_b128 a[92:95], v128 offset:40960\n"
"ds_read_b128 a[96:99], v128 offset:41984\n"
"buffer_load_dwordx4 a[0:3], v128, s[24:27], s89 offen offset:2048 sc1\n"
"v_mfma_f32_32x32x16_bf16 v[16:31], a[196:199], a[152:155], v[16:31]\n"
"ds_read_b128 a[100:103], v128 offset:43008\n"
"buffer_load_dwordx4 a[4:7], v128, s[24:27], s74 offen offset:2048 sc1\n"
"v_mfma_f32_32x32x16_bf16 v[32:47], a[200:203], a[152:155], v[32:47]\n"
"ds_read_b128 a[104:107], v128 offset:44032\n"
"buffer_load_dwordx4 a[8:11], v128, s[24:27], s89 offen offset:3072 sc1\n"
"v_mfma_f32_32x32x16_bf16 v[48:63], a[204:207], a[152:155], v[48:63]\n"
"ds_read_b128 a[108:111], v128 offset:45056\n"
"buffer_load_dwordx4 a[12:15], v128, s[24:27], s74 offen offset:3072 sc1\n"
"v_mfma_f32_32x32x16_bf16 v[0:15], a[160:163], a[156:159], v[0:15]\n"
"ds_read_b128 a[112:115], v128 offset:46080\n"
"ds_read_b128 a[116:119], v128 offset:47104\n"
"v_mfma_f32_32x32x16_bf16 v[16:31], a[164:167], a[156:159], v[16:31]\n"
"ds_read_b128 a[120:123], v128 offset:48128\n"
"v_mfma_f32_32x32x16_bf16 v[32:47], a[168:171], a[156:159], v[32:47]\n"
"ds_read_b128 a[124:127], v128 offset:49152\n"
"v_mfma_f32_32x32x16_bf16 v[48:63], a[172:175], a[156:159], v[48:63]\n"
"ds_read_b128 a[128:131], v128 offset:50176\n"
"L%=_P2M:\n"
"s_waitcnt lgkmcnt(5)\n"
"v_mfma_f32_16x16x32_bf16 v[64:67], a[208:211], a[92:95], 0\n"
"ds_read_b128 a[132:135], v128 offset:51200\n"
"s_nop 0\n"
"v_mul_f32 v232, v0, v146\n"
"v_mul_f32 v233, v1, v146\n"
"v_mul_f32 v234, v2, v146\n"
"v_mul_f32 v235, v3, v146\n"
"v_exp_f32 v232, v232\n"
"v_exp_f32 v233, v233\n"
"v_exp_f32 v234, v234\n"
"v_exp_f32 v235, v235\n"
"v_mfma_f32_16x16x32_bf16 v[84:87], a[212:215], a[92:95], 0\n"
"v_mul_f32 v8, v0, v8\n"
"v_mul_f32 v9, v1, v9\n"
"v_mul_f32 v10, v2, v10\n"
"v_mul_f32 v11, v3, v11\n"
"v_fma_f32 v232, v232, v147, v147\n"
"v_fma_f32 v233, v233, v147, v147\n"
"v_fma_f32 v234, v234, v147, v147\n"
"v_fma_f32 v235, v235, v147, v147\n"
"v_rcp_f32 v232, v232\n"
"v_rcp_f32 v233, v233\n"
"v_mfma_f32_16x16x32_bf16 v[68:71], a[208:211], a[96:99], 0\n"
"ds_read_b128 a[136:139], v128 offset:52224\n"
"v_rcp_f32 v234, v234\n"
"v_rcp_f32 v235, v235\n"
"v_mul_f32 v8, v8, v232\n"
"v_mul_f32 v9, v9, v233\n"
"v_mul_f32 v10, v10, v234\n"
"v_mul_f32 v11, v11, v235\n"
"v_cvt_pk_bf16_f32 v0, v8, v9\n"
"v_cvt_pk_bf16_f32 v1, v10, v11\n"
"v_mul_f32 v232, v4, v146\n"
"v_mul_f32 v233, v5, v146\n"
"v_mfma_f32_16x16x32_bf16 v[88:91], a[212:215], a[96:99], 0\n"
"v_mul_f32 v234, v6, v146\n"
"v_mul_f32 v235, v7, v146\n"
"v_exp_f32 v232, v232\n"
"v_exp_f32 v233, v233\n"
"v_exp_f32 v234, v234\n"
"v_exp_f32 v235, v235\n"
"v_mul_f32 v12, v4, v12\n"
"v_mul_f32 v13, v5, v13\n"
"v_mul_f32 v14, v6, v14\n"
"v_mfma_f32_16x16x32_bf16 v[72:75], a[208:211], a[100:103], 0\n"
"ds_read_b128 a[140:143], v128 offset:53248\n"
"v_mul_f32 v15, v7, v15\n"
"v_fma_f32 v232, v232, v147, v147\n"
"v_fma_f32 v233, v233, v147, v147\n"
"v_fma_f32 v234, v234, v147, v147\n"
"v_fma_f32 v235, v235, v147, v147\n"
"v_rcp_f32 v232, v232\n"
"v_rcp_f32 v233, v233\n"
"v_rcp_f32 v234, v234\n"
"v_mfma_f32_16x16x32_bf16 v[92:95], a[212:215], a[100:103], 0\n"
"v_rcp_f32 v235, v235\n"
"v_mul_f32 v12, v12, v232\n"
"v_mul_f32 v13, v13, v233\n"
"v_mul_f32 v14, v14, v234\n"
"v_mul_f32 v15, v15, v235\n"
"v_cvt_pk_bf16_f32 v2, v12, v13\n"
"v_cvt_pk_bf16_f32 v3, v14, v15\n"
"buffer_store_dwordx4 v[0:3], v145, s[24:27], s75 offen sc1\n"
"v_mfma_f32_16x16x32_bf16 v[76:79], a[208:211], a[104:107], 0\n"
"ds_read_b128 a[144:147], v128 offset:54272\n"
"v_mul_f32 v232, v16, v146\n"
"v_mul_f32 v233, v17, v146\n"
"v_mul_f32 v234, v18, v146\n"
"v_mul_f32 v235, v19, v146\n"
"v_exp_f32 v232, v232\n"
"v_exp_f32 v233, v233\n"
"v_exp_f32 v234, v234\n"
"v_exp_f32 v235, v235\n"
"v_mfma_f32_16x16x32_bf16 v[96:99], a[212:215], a[104:107], 0\n"
"v_mul_f32 v24, v16, v24\n"
"v_mul_f32 v25, v17, v25\n"
"v_mul_f32 v26, v18, v26\n"
"v_mul_f32 v27, v19, v27\n"
"v_fma_f32 v232, v232, v147, v147\n"
"v_fma_f32 v233, v233, v147, v147\n"
"v_fma_f32 v234, v234, v147, v147\n"
"v_fma_f32 v235, v235, v147, v147\n"
"v_rcp_f32 v232, v232\n"
"v_rcp_f32 v233, v233\n"
"v_mfma_f32_16x16x32_bf16 v[80:83], a[208:211], a[108:111], 0\n"
"ds_read_b128 a[148:151], v128 offset:55296\n"
"v_rcp_f32 v234, v234\n"
"v_rcp_f32 v235, v235\n"
"v_mul_f32 v24, v24, v232\n"
"v_mul_f32 v25, v25, v233\n"
"v_mul_f32 v26, v26, v234\n"
"v_mul_f32 v27, v27, v235\n"
"v_cvt_pk_bf16_f32 v16, v24, v25\n"
"v_cvt_pk_bf16_f32 v17, v26, v27\n"
"v_mul_f32 v232, v20, v146\n"
"v_mul_f32 v233, v21, v146\n"
"v_mfma_f32_16x16x32_bf16 v[100:103], a[212:215], a[108:111], 0\n"
"s_waitcnt lgkmcnt(5)\n"
"v_mul_f32 v234, v22, v146\n"
"v_mul_f32 v235, v23, v146\n"
"v_exp_f32 v232, v232\n"
"v_exp_f32 v233, v233\n"
"v_exp_f32 v234, v234\n"
"v_exp_f32 v235, v235\n"
"v_mul_f32 v28, v20, v28\n"
"v_mul_f32 v29, v21, v29\n"
"v_mul_f32 v30, v22, v30\n"
"v_mfma_f32_16x16x32_bf16 v[64:67], a[216:219], a[112:115], v[64:67]\n"
"ds_read_b128 a[92:95], v128 offset:56320\n"
"v_mul_f32 v31, v23, v31\n"
"v_fma_f32 v232, v232, v147, v147\n"
"v_fma_f32 v233, v233, v147, v147\n"
"v_fma_f32 v234, v234, v147, v147\n"
"v_fma_f32 v235, v235, v147, v147\n"
"v_rcp_f32 v232, v232\n"
"v_rcp_f32 v233, v233\n"
"v_rcp_f32 v234, v234\n"
"v_mfma_f32_16x16x32_bf16 v[84:87], a[220:223], a[112:115], v[84:87]\n"
"v_rcp_f32 v235, v235\n"
"v_mul_f32 v28, v28, v232\n"
"v_mul_f32 v29, v29, v233\n"
"v_mul_f32 v30, v30, v234\n"
"v_mul_f32 v31, v31, v235\n"
"v_cvt_pk_bf16_f32 v18, v28, v29\n"
"v_cvt_pk_bf16_f32 v19, v30, v31\n"
"buffer_store_dwordx4 v[16:19], v145, s[24:27], s75 offen offset:512 sc1\n"
"v_mfma_f32_16x16x32_bf16 v[68:71], a[216:219], a[116:119], v[68:71]\n"
"ds_read_b128 a[96:99], v128 offset:57344\n"
"v_mul_f32 v232, v32, v146\n"
"v_mul_f32 v233, v33, v146\n"
"v_mul_f32 v234, v34, v146\n"
"v_mul_f32 v235, v35, v146\n"
"v_exp_f32 v232, v232\n"
"v_exp_f32 v233, v233\n"
"v_exp_f32 v234, v234\n"
"v_exp_f32 v235, v235\n"
"v_mfma_f32_16x16x32_bf16 v[88:91], a[220:223], a[116:119], v[88:91]\n"
"v_mul_f32 v40, v32, v40\n"
"v_mul_f32 v41, v33, v41\n"
"v_mul_f32 v42, v34, v42\n"
"v_mul_f32 v43, v35, v43\n"
"v_fma_f32 v232, v232, v147, v147\n"
"v_fma_f32 v233, v233, v147, v147\n"
"v_fma_f32 v234, v234, v147, v147\n"
"v_fma_f32 v235, v235, v147, v147\n"
"v_rcp_f32 v232, v232\n"
"v_rcp_f32 v233, v233\n"
"v_mfma_f32_16x16x32_bf16 v[72:75], a[216:219], a[120:123], v[72:75]\n"
"ds_read_b128 a[100:103], v128 offset:58368\n"
"v_rcp_f32 v234, v234\n"
"v_rcp_f32 v235, v235\n"
"v_mul_f32 v40, v40, v232\n"
"v_mul_f32 v41, v41, v233\n"
"v_mul_f32 v42, v42, v234\n"
"v_mul_f32 v43, v43, v235\n"
"v_cvt_pk_bf16_f32 v32, v40, v41\n"
"v_cvt_pk_bf16_f32 v33, v42, v43\n"
"v_mul_f32 v232, v36, v146\n"
"v_mul_f32 v233, v37, v146\n"
"v_mfma_f32_16x16x32_bf16 v[92:95], a[220:223], a[120:123], v[92:95]\n"
"v_mul_f32 v234, v38, v146\n"
"v_mul_f32 v235, v39, v146\n"
"v_exp_f32 v232, v232\n"
"v_exp_f32 v233, v233\n"
"v_exp_f32 v234, v234\n"
"v_exp_f32 v235, v235\n"
"v_mul_f32 v44, v36, v44\n"
"v_mul_f32 v45, v37, v45\n"
"v_mul_f32 v46, v38, v46\n"
"v_mfma_f32_16x16x32_bf16 v[76:79], a[216:219], a[124:127], v[76:79]\n"
"ds_read_b128 a[104:107], v128 offset:59392\n"
"v_mul_f32 v47, v39, v47\n"
"v_fma_f32 v232, v232, v147, v147\n"
"v_fma_f32 v233, v233, v147, v147\n"
"v_fma_f32 v234, v234, v147, v147\n"
"v_fma_f32 v235, v235, v147, v147\n"
"v_rcp_f32 v232, v232\n"
"v_rcp_f32 v233, v233\n"
"v_rcp_f32 v234, v234\n"
"v_mfma_f32_16x16x32_bf16 v[96:99], a[220:223], a[124:127], v[96:99]\n"
"v_rcp_f32 v235, v235\n"
"v_mul_f32 v44, v44, v232\n"
"v_mul_f32 v45, v45, v233\n"
"v_mul_f32 v46, v46, v234\n"
"v_mul_f32 v47, v47, v235\n"
"v_cvt_pk_bf16_f32 v34, v44, v45\n"
"v_cvt_pk_bf16_f32 v35, v46, v47\n"
"buffer_store_dwordx4 v[32:35], v145, s[24:27], s75 offen offset:1024 sc1\n"
"v_mfma_f32_16x16x32_bf16 v[80:83], a[216:219], a[128:131], v[80:83]\n"
"ds_read_b128 a[108:111], v128 offset:60416\n"
"v_mul_f32 v232, v48, v146\n"
"v_mul_f32 v233, v49, v146\n"
"v_mul_f32 v234, v50, v146\n"
"v_mul_f32 v235, v51, v146\n"
"v_exp_f32 v232, v232\n"
"v_exp_f32 v233, v233\n"
"v_exp_f32 v234, v234\n"
"v_exp_f32 v235, v235\n"
"v_mfma_f32_16x16x32_bf16 v[100:103], a[220:223], a[128:131], v[100:103]\n"
"s_waitcnt lgkmcnt(5)\n"
"v_mul_f32 v56, v48, v56\n"
"v_mul_f32 v57, v49, v57\n"
"v_mul_f32 v58, v50, v58\n"
"v_mul_f32 v59, v51, v59\n"
"v_fma_f32 v232, v232, v147, v147\n"
"v_fma_f32 v233, v233, v147, v147\n"
"v_fma_f32 v234, v234, v147, v147\n"
"v_fma_f32 v235, v235, v147, v147\n"
"v_rcp_f32 v232, v232\n"
"v_rcp_f32 v233, v233\n"
"s_barrier\n"
"v_mfma_f32_16x16x32_bf16 v[64:67], a[224:227], a[132:135], v[64:67]\n"
"s_lshl_b32 s80, s98, 2\n"
"ds_read_b128 a[112:115], v128 offset:61440\n"
"s_add_u32 s80, s80, s64\n"
"s_lshl_b32 s80, s80, 7\n"
"s_add_u32 s80, s80, 0x62000\n"
"v_and_b32 v140, 15, v253\n"
"v_lshl_add_u32 v140, v140, 3, s80\n"
"v_bfe_u32 v141, v253, 4, 1\n"
"v_lshlrev_b32 v141, 13, v141\n"
"v_sub_u32 v140, v140, v141\n"
"s_waitcnt vmcnt(25)\n"
"v_mfma_f32_16x16x32_bf16 v[84:87], a[228:231], a[132:135], v[84:87]\n"
"s_mul_i32 s89, s1, 0x2c00\n"
"s_add_u32 s89, s89, s92\n"
"s_add_u32 s89, s89, 0x2000\n"
"s_add_u32 s74, s89, s0\n"
"buffer_load_dwordx4 a[16:19], v128, s[24:27], s89 offen sc1\n"
"buffer_load_dwordx2 v[142:143], v140, s[24:27], 0 offen sc1\n"
"v_rcp_f32 v234, v234\n"
"v_mfma_f32_16x16x32_bf16 v[68:71], a[224:227], a[136:139], v[68:71]\n"
"ds_read_b128 a[116:119], v128 offset:62464\n"
"v_rcp_f32 v235, v235\n"
"v_mul_f32 v56, v56, v232\n"
"v_mul_f32 v57, v57, v233\n"
"v_mul_f32 v58, v58, v234\n"
"v_mul_f32 v59, v59, v235\n"
"v_cvt_pk_bf16_f32 v48, v56, v57\n"
"v_cvt_pk_bf16_f32 v49, v58, v59\n"
"v_mul_f32 v232, v52, v146\n"
"v_mul_f32 v233, v53, v146\n"
"v_mul_f32 v234, v54, v146\n"
"v_mul_f32 v235, v55, v146\n"
"v_mfma_f32_16x16x32_bf16 v[88:91], a[228:231], a[136:139], v[88:91]\n"
"v_exp_f32 v232, v232\n"
"v_exp_f32 v233, v233\n"
"v_exp_f32 v234, v234\n"
"v_exp_f32 v235, v235\n"
"v_mul_f32 v60, v52, v60\n"
"v_mul_f32 v61, v53, v61\n"
"v_mul_f32 v62, v54, v62\n"
"v_mul_f32 v63, v55, v63\n"
"v_fma_f32 v232, v232, v147, v147\n"
"v_mfma_f32_16x16x32_bf16 v[72:75], a[224:227], a[140:143], v[72:75]\n"
"ds_read_b128 a[120:123], v128 offset:63488\n"
"v_fma_f32 v233, v233, v147, v147\n"
"v_fma_f32 v234, v234, v147, v147\n"
"v_fma_f32 v235, v235, v147, v147\n"
"v_rcp_f32 v232, v232\n"
"v_rcp_f32 v233, v233\n"
"v_rcp_f32 v234, v234\n"
"v_rcp_f32 v235, v235\n"
"v_mul_f32 v60, v60, v232\n"
"v_mfma_f32_16x16x32_bf16 v[92:95], a[228:231], a[140:143], v[92:95]\n"
"buffer_load_dwordx4 a[20:23], v128, s[24:27], s74 offen sc1\n"
"v_mul_f32 v61, v61, v233\n"
"v_mul_f32 v62, v62, v234\n"
"v_mul_f32 v63, v63, v235\n"
"v_cvt_pk_bf16_f32 v50, v60, v61\n"
"v_cvt_pk_bf16_f32 v51, v62, v63\n"
"buffer_store_dwordx4 v[48:51], v145, s[24:27], s75 offen offset:1536 sc1\n"
"v_mfma_f32_16x16x32_bf16 v[76:79], a[224:227], a[144:147], v[76:79]\n"
"ds_read_b128 a[124:127], v128 offset:64512\n"
"v_mfma_f32_16x16x32_bf16 v[96:99], a[228:231], a[144:147], v[96:99]\n"
"v_mfma_f32_16x16x32_bf16 v[80:83], a[224:227], a[148:151], v[80:83]\n"
"ds_read_b128 a[128:131], v137\n"
"v_mfma_f32_16x16x32_bf16 v[100:103], a[228:231], a[148:151], v[100:103]\n"
"s_waitcnt vmcnt(21) lgkmcnt(5)\n"
"v_mfma_f32_16x16x32_bf16 v[64:67], a[232:235], a[92:95], v[64:67]\n"
"ds_read_b128 a[132:135], v137 offset:1024\n"
"v_mfma_f32_16x16x32_bf16 v[84:87], a[236:239], a[92:95], v[84:87]\n"
"buffer_load_dwordx4 a[24:27], v128, s[24:27], s89 offen offset:1024 sc1\n"
"v_mfma_f32_16x16x32_bf16 v[68:71], a[232:235], a[96:99], v[68:71]\n"
"ds_read_b128 a[136:139], v137 offset:2048\n"
"v_mfma_f32_16x16x32_bf16 v[88:91], a[236:239], a[96:99], v[88:91]\n"
"v_mfma_f32_16x16x32_bf16 v[72:75], a[232:235], a[100:103], v[72:75]\n"
"ds_read_b128 a[140:143], v137 offset:3072\n"
"v_mfma_f32_16x16x32_bf16 v[92:95], a[236:239], a[100:103], v[92:95]\n"
"buffer_load_dwordx4 a[28:31], v128, s[24:27], s74 offen offset:1024 sc1\n"
"v_mfma_f32_16x16x32_bf16 v[76:79], a[232:235], a[104:107], v[76:79]\n"
"ds_read_b128 a[144:147], v137 offset:4096\n"
"v_mfma_f32_16x16x32_bf16 v[96:99], a[236:239], a[104:107], v[96:99]\n"
"v_mfma_f32_16x16x32_bf16 v[80:83], a[232:235], a[108:111], v[80:83]\n"
"ds_read_b128 a[148:151], v137 offset:5120\n"
"v_mfma_f32_16x16x32_bf16 v[100:103], a[236:239], a[108:111], v[100:103]\n"
"s_waitcnt vmcnt(15) lgkmcnt(5)\n"
"v_mfma_f32_16x16x32_bf16 v[64:67], a[240:243], a[112:115], v[64:67]\n"
"ds_read_b128 a[92:95], v137 offset:6144\n"
"v_mfma_f32_16x16x32_bf16 v[84:87], a[244:247], a[112:115], v[84:87]\n"
"s_mul_i32 s99, s1, 0x6e000\n"
"s_add_u32 s99, s99, s90\n"
"s_add_u32 s99, s99, 0xdc000\n"
"s_add_u32 m0, s88, 0x19000\n"
"s_nop 0\n"
"buffer_load_dwordx4 v128, s[24:27], s99 offen sc1 lds\n"
"v_mfma_f32_16x16x32_bf16 v[68:71], a[240:243], a[116:119], v[68:71]\n"
"ds_read_b128 a[96:99], v137 offset:7168\n"
"buffer_load_dwordx4 v128, s[24:27], s99 offen offset:1024 sc1 lds\n"
"v_mfma_f32_16x16x32_bf16 v[88:91], a[244:247], a[116:119], v[88:91]\n"
"buffer_load_dwordx4 v128, s[24:27], s99 offen offset:2048 sc1 lds\n"
"v_mfma_f32_16x16x32_bf16 v[72:75], a[240:243], a[120:123], v[72:75]\n"
"ds_read_b128 a[100:103], v137 offset:8192\n"
"buffer_load_dwordx4 v128, s[24:27], s99 offen offset:3072 sc1 lds\n"
"v_mfma_f32_16x16x32_bf16 v[92:95], a[244:247], a[120:123], v[92:95]\n"
"s_add_u32 m0, s88, 0x1a000\n"
"s_nop 0\n"
"buffer_load_dwordx4 v130, s[24:27], s99 offen sc1 lds\n"
"v_mfma_f32_16x16x32_bf16 v[76:79], a[240:243], a[124:127], v[76:79]\n"
"s_add_u32 s80, s76, 1\n"
"ds_read_b128 a[104:107], v137 offset:9216\n"
"buffer_load_dwordx4 a[32:35], v128, s[24:27], s89 offen offset:2048 sc1\n"
"s_waitcnt vmcnt(10)\n"
"v_subrev_u32 v141, s80, v142\n"
"v_cmp_lt_u32 vcc, 2, v141\n"
"v_cmp_ne_u32 s[82:83], s77, v143\n"
"s_nop 0\n"
"s_or_b32 s82, s82, vcc_lo\n"
"s_and_b32 s82, s82, 0xf80000ff\n"
"s_cbranch_scc1 L%=_f2cM_slow\n"
"L%=_f2cM_back:\n"
"v_and_b32 v248, 15, v253\n"
"v_mfma_f32_16x16x32_bf16 v[96:99], a[244:247], a[124:127], v[96:99]\n"
"buffer_load_dwordx4 a[36:39], v128, s[24:27], s74 offen offset:2048 sc1\n"
"v_lshrrev_b32 v249, 3, v248\n"
"v_lshlrev_b32 v249, 10, v249\n"
"v_and_b32 v250, 7, v253\n"
"v_lshl_add_u32 v249, v250, 7, v249\n"
"v_lshrrev_b32 v248, 1, v248\n"
"v_lshrrev_b32 v250, 4, v253\n"
"v_xor_b32 v248, v248, v250\n"
"v_lshl_add_u32 v174, v248, 4, v249\n"
"v_xor_b32 v177, 64, v174\n"
"v_mfma_f32_16x16x32_bf16 v[80:83], a[240:243], a[128:131], v[80:83]\n"
"s_mov_b32 s87, 0x1000\n"
"ds_read_b128 a[108:111], v137 offset:10240\n"
"v_add_u32 v175, 0x10000, v174\n"
"v_add_u32 v176, 0x20000, v174\n"
"v_add_u32 v178, 0x10000, v177\n"
"v_add_u32 v179, 0x20000, v177\n"
"buffer_load_dwordx4 v[124:127], v128, s[24:27], s87 offen sc1\n"
"buffer_load_dwordx4 v[236:239], v128, s[24:27], s87 offen offset:1024 sc1\n"
"v_mfma_f32_16x16x32_bf16 v[100:103], a[244:247], a[128:131], v[100:103]\n"
"s_waitcnt lgkmcnt(5)\n"
"v_mfma_f32_16x16x32_bf16 v[64:67], a[248:251], a[132:135], v[64:67]\n"
"ds_read_b128 a[112:115], v137 offset:11264\n"
"v_mfma_f32_16x16x32_bf16 v[84:87], a[252:255], a[132:135], v[84:87]\n"
"s_mul_i32 s89, s1, 0x1400\n"
"s_add_u32 s89, s89, s92\n"
"s_add_u32 s89, s89, 0xb000\n"
"s_add_u32 s74, s89, s0\n"
"buffer_load_dwordx4 a[40:43], v128, s[24:27], s89 offen sc1\n"
"v_mfma_f32_16x16x32_bf16 v[68:71], a[248:251], a[136:139], v[68:71]\n"
"ds_read_b128 a[116:119], v137 offset:12288\n"
"v_mfma_f32_16x16x32_bf16 v[88:91], a[252:255], a[136:139], v[88:91]\n"
"v_mfma_f32_16x16x32_bf16 v[72:75], a[248:251], a[140:143], v[72:75]\n"
"ds_read_b128 a[120:123], v137 offset:13312\n"
"v_mfma_f32_16x16x32_bf16 v[92:95], a[252:255], a[140:143], v[92:95]\n"
"buffer_load_dwordx4 a[44:47], v128, s[24:27], s74 offen sc1\n"
"v_mfma_f32_16x16x32_bf16 v[76:79], a[248:251], a[144:147], v[76:79]\n"
"ds_read_b128 a[124:127], v137 offset:14336\n"
"v_mfma_f32_16x16x32_bf16 v[96:99], a[252:255], a[144:147], v[96:99]\n"
"s_waitcnt vmcnt(13)\n"
"s_add_u32 s80, s76, 3\n"
"v_mov_b32 v230, s80\n"
"s_lshl_b32 s80, s66, 2\n"
"s_add_u32 s80, s80, s64\n"
"s_lshl_b32 s80, s80, 4\n"
"s_add_u32 s80, s80, s67\n"
"s_lshl_b32 s80, s80, 3\n"
"s_add_u32 s80, s80, 0x64000\n"
"buffer_store_dwordx2 v[230:231], v229, s[24:27], s80 offen sc1\n"
"v_mfma_f32_16x16x32_bf16 v[80:83], a[248:251], a[148:151], v[80:83]\n"
"ds_read_b128 a[128:131], v137 offset:15360\n"
"v_mfma_f32_16x16x32_bf16 v[100:103], a[252:255], a[148:151], v[100:103]\n"
"s_waitcnt lgkmcnt(5)\n"
"s_barrier\n"
"v_mfma_f32_16x16x32_bf16 v[64:67], a[0:3], a[92:95], v[64:67]\n"
"ds_read_b128 a[132:135], v137 offset:57344\n"
"v_mfma_f32_16x16x32_bf16 v[84:87], a[4:7], a[92:95], v[84:87]\n"
"s_mul_i32 s89, s1, 0x1400\n"
"s_add_u32 s89, s89, s92\n"
"s_add_u32 s89, s89, 0xb400\n"
"s_add_u32 s74, s89, s0\n"
"buffer_load_dwordx4 a[48:51], v128, s[24:27], s89 offen sc1\n"
"v_mfma_f32_16x16x32_bf16 v[68:71], a[0:3], a[96:99], v[68:71]\n"
"ds_read_b128 a[136:139], v137 offset:58368\n"
"v_mfma_f32_16x16x32_bf16 v[88:91], a[4:7], a[96:99], v[88:91]\n"
"v_mfma_f32_16x16x32_bf16 v[72:75], a[0:3], a[100:103], v[72:75]\n"
"ds_read_b128 a[140:143], v137 offset:59392\n"
"v_mfma_f32_16x16x32_bf16 v[92:95], a[4:7], a[100:103], v[92:95]\n"
"buffer_load_dwordx4 a[52:55], v128, s[24:27], s74 offen sc1\n"
"v_mfma_f32_16x16x32_bf16 v[76:79], a[0:3], a[104:107], v[76:79]\n"
"ds_read_b128 a[144:147], v137 offset:60416\n"
"v_mfma_f32_16x16x32_bf16 v[96:99], a[4:7], a[104:107], v[96:99]\n"
"v_mfma_f32_16x16x32_bf16 v[80:83], a[0:3], a[108:111], v[80:83]\n"
"ds_read_b128 a[148:151], v137 offset:61440\n"
"v_mfma_f32_16x16x32_bf16 v[100:103], a[4:7], a[108:111], v[100:103]\n"
"s_waitcnt lgkmcnt(5)\n"
"v_mfma_f32_16x16x32_bf16 v[64:67], a[8:11], a[112:115], v[64:67]\n"
"ds_read_b128 a[92:95], v137 offset:62464\n"
"v_mfma_f32_16x16x32_bf16 v[84:87], a[12:15], a[112:115], v[84:87]\n"
"buffer_load_dwordx4 a[56:59], v128, s[24:27], s89 offen offset:1024 sc1\n"
"v_mfma_f32_16x16x32_bf16 v[68:71], a[8:11], a[116:119], v[68:71]\n"
"ds_read_b128 a[96:99], v137 offset:63488\n"
"v_mfma_f32_16x16x32_bf16 v[88:91], a[12:15], a[116:119], v[88:91]\n"
"v_mfma_f32_16x16x32_bf16 v[72:75], a[8:11], a[120:123], v[72:75]\n"
"ds_read_b128 a[100:103], v137 offset:64512\n"
"v_mfma_f32_16x16x32_bf16 v[92:95], a[12:15], a[120:123], v[92:95]\n"
"buffer_load_dwordx4 a[60:63], v128, s[24:27], s74 offen offset:1024 sc1\n"
"v_mfma_f32_16x16x32_bf16 v[76:79], a[8:11], a[124:127], v[76:79]\n"
"ds_read_b128 a[104:107], v138\n"
"v_mfma_f32_16x16x32_bf16 v[96:99], a[12:15], a[124:127], v[96:99]\n"
"v_mfma_f32_16x16x32_bf16 v[80:83], a[8:11], a[128:131], v[80:83]\n"
"ds_read_b128 a[108:111], v138 offset:1024\n"
"v_mfma_f32_16x16x32_bf16 v[100:103], a[12:15], a[128:131], v[100:103]\n"
"s_waitcnt lgkmcnt(5)\n"
"v_mfma_f32_16x16x32_bf16 v[64:67], a[16:19], a[132:135], v[64:67]\n"
"s_lshl_b32 s80, s98, 2\n"
"ds_read_b128 a[112:115], v138 offset:2048\n"
"v_bfe_u32 v188, v253, 4, 1\n"
"v_lshlrev_b32 v188, 4, v188\n"
"v_lshrrev_b32 v131, 5, v253\n"
"v_lshl_add_u32 v188, v131, 2, v188\n"
"s_add_u32 s80, s80, s64\n"
"s_lshl_b32 s80, s80, 2\n"
"s_add_u32 s80, s80, s1\n"
"s_lshl_b32 s80, s80, 3\n"
"v_add_u32 v188, s80, v188\n"
"v_mfma_f32_16x16x32_bf16 v[84:87], a[20:23], a[132:135], v[84:87]\n"
"s_mul_i32 s99, s1, 0x6e000\n"
"s_add_u32 s99, s99, s90\n"
"s_add_u32 s99, s99, 0x104000\n"
"s_add_u32 m0, s88, 0xa000\n"
"s_nop 0\n"
"buffer_load_dwordx4 v128, s[24:27], s99 offen sc1 lds\n"
"v_mfma_f32_16x16x32_bf16 v[68:71], a[16:19], a[136:139], v[68:71]\n"
"ds_read_b128 a[116:119], v138 offset:3072\n"
"buffer_load_dwordx4 v128, s[24:27], s99 offen offset:1024 sc1 lds\n"
"v_mfma_f32_16x16x32_bf16 v[88:91], a[20:23], a[136:139], v[88:91]\n"
"buffer_load_dwordx4 v128, s[24:27], s99 offen offset:2048 sc1 lds\n"
"v_and_b32 v131, 15, v253\n"
"v_mfma_f32_16x16x32_bf16 v[72:75], a[16:19], a[140:143], v[72:75]\n"
"s_mul_i32 s80, s97, 0x50\n"
"ds_read_b128 a[120:123], v138 offset:4096\n"
"v_add_lshl_u32 v131, v131, s80, 1\n"
"v_mul_u32_u24 v189, 0x500, v188\n"
"v_add_u32 v189, v189, v131\n"
"v_mfma_f32_16x16x32_bf16 v[92:95], a[20:23], a[140:143], v[92:95]\n"
"buffer_load_dwordx4 a[64:67], v128, s[24:27], s89 offen offset:2048 sc1\n"
"v_lshlrev_b32 v227, 1, v189\n"
"v_lshlrev_b32 v188, 4, v188\n"
"v_mfma_f32_16x16x32_bf16 v[76:79], a[16:19], a[144:147], v[76:79]\n"
"ds_read_b128 a[124:127], v138 offset:5120\n"
"v_add_u32 v228, 0x1400, v227\n"
"v_add_u32 v188, 0x50000, v188\n"
"v_add_u32 v243, 0, v253\n"
"v_mul_u32_u24 v247, 0x199a, v243\n"
"v_mfma_f32_16x16x32_bf16 v[96:99], a[20:23], a[144:147], v[96:99]\n"
"buffer_load_dwordx4 a[68:71], v128, s[24:27], s74 offen offset:2048 sc1\n"
"v_lshrrev_b32 v247, 16, v247\n"
"v_mul_u32_u24 v251, 10, v247\n"
"v_mfma_f32_16x16x32_bf16 v[80:83], a[16:19], a[148:151], v[80:83]\n"
"ds_read_b128 a[128:131], v138 offset:6144\n"
"v_sub_u32 v243, v243, v251\n"
"v_bfe_u32 v251, v247, 2, 1\n"
"v_lshlrev_b32 v251, 4, v251\n"
"v_and_b32 v240, 3, v247\n"
"v_mfma_f32_16x16x32_bf16 v[100:103], a[20:23], a[148:151], v[100:103]\n"
"s_waitcnt vmcnt(21) lgkmcnt(5)\n"
"v_add_u32 v251, v251, v240\n"
"v_lshrrev_b32 v240, 3, v247\n"
"v_lshl_add_u32 v251, v240, 2, v251\n"
"v_mul_u32_u24 v251, 0x500, v251\n"
"v_lshl_add_u32 v240, v243, 4, v251\n"
"v_mfma_f32_16x16x32_bf16 v[64:67], a[24:27], a[92:95], v[64:67]\n"
"ds_read_b128 a[132:135], v138 offset:7168\n"
"s_waitcnt vmcnt(13)\n"
"v_cmp_ne_u64 s[80:81], s[76:77], v[124:125]\n"
"v_cmp_ne_u64 s[82:83], s[76:77], v[126:127]\n"
"s_waitcnt vmcnt(12)\n"
"v_cmp_ne_u64 s[84:85], s[76:77], v[236:237]\n"
"v_cmp_ne_u64 vcc, s[76:77], v[238:239]\n"
"s_or_b64 s[80:81], s[80:81], s[82:83]\n"
"s_or_b64 s[84:85], s[84:85], vcc\n"
"s_or_b64 s[80:81], s[80:81], s[84:85]\n"
"s_or_b32 s80, s80, s81\n"
"s_cmp_eq_u32 s3, 0\n"
"s_cselect_b32 s81, -1, 0\n"
"s_and_b32 s80, s80, s81\n"
"s_cbranch_scc1 L%=_f0PM_slow\n"
"L%=_f0PM_back:\n"
"v_mfma_f32_16x16x32_bf16 v[84:87], a[28:31], a[92:95], v[84:87]\n"
"s_add_u32 m0, s88, 0xa000\n"
"s_nop 0\n"
"buffer_load_dwordx4 v128, s[24:27], s99 offen offset:3072 sc1 lds\n"
"s_or_b32 s80, s3, s64\n"
"s_cselect_b32 s80, 0x7000000, 0\n"
"v_mov_b32 v230, s76\n"
"buffer_store_dwordx2 v[230:231], v229, s[24:27], s80 offen sc1\n"
"v_mfma_f32_16x16x32_bf16 v[68:71], a[24:27], a[96:99], v[68:71]\n"
"s_lshr_b32 s82, s3, 3\n"
"ds_read_b128 a[136:139], v138 offset:8192\n"
"s_lshl_b32 s82, s82, 2\n"
"s_add_u32 s82, s82, s64\n"
"s_mul_i32 s82, s82, 5\n"
"v_min_u32 v246, 4, v253\n"
"v_add_u32 v246, s82, v246\n"
"v_lshlrev_b32 v246, 7, v246\n"
"v_min_u32 v246, L%=_end-L%=_pc-8, v246\n"
"s_getpc_b64 s[80:81]\n"
"L%=_st_ct8:\n"
"s_sub_u32 s80, s80, L%=_st_ct8-L%=_pc\n"
"s_subb_u32 s81, s81, 0\n"
"global_load_dword v246, v246, s[80:81]\n"
"v_mfma_f32_16x16x32_bf16 v[88:91], a[28:31], a[96:99], v[88:91]\n"
"s_add_u32 m0, s88, 0xb000\n"
"s_nop 0\n"
"buffer_load_dwordx4 v130, s[24:27], s99 offen sc1 lds\n"
"v_add_u32 v243, 64, v253\n"
"v_mfma_f32_16x16x32_bf16 v[72:75], a[24:27], a[100:103], v[72:75]\n"
"ds_read_b128 a[140:143], v138 offset:9216\n"
"v_mul_u32_u24 v247, 0x199a, v243\n"
"v_lshrrev_b32 v247, 16, v247\n"
"v_mul_u32_u24 v251, 10, v247\n"
"v_sub_u32 v243, v243, v251\n"
"v_mfma_f32_16x16x32_bf16 v[92:95], a[28:31], a[100:103], v[92:95]\n"
"buffer_load_dwordx4 a[72:75], v128, s[24:27], s89 offen offset:3072 sc1\n"
"v_bfe_u32 v251, v247, 2, 1\n"
"v_lshlrev_b32 v251, 4, v251\n"
"v_mfma_f32_16x16x32_bf16 v[76:79], a[24:27], a[104:107], v[76:79]\n"
"ds_read_b128 a[144:147], v138 offset:10240\n"
"v_and_b32 v241, 3, v247\n"
"v_add_u32 v251, v251, v241\n"
"v_lshrrev_b32 v241, 3, v247\n"
"v_lshl_add_u32 v251, v241, 2, v251\n"
"v_mfma_f32_16x16x32_bf16 v[96:99], a[28:31], a[104:107], v[96:99]\n"
"buffer_load_dwordx4 a[76:79], v128, s[24:27], s74 offen offset:3072 sc1\n"
"v_mul_u32_u24 v251, 0x500, v251\n"
"v_lshl_add_u32 v241, v243, 4, v251\n"
"v_mfma_f32_16x16x32_bf16 v[80:83], a[24:27], a[108:111], v[80:83]\n"
"ds_read_b128 a[148:151], v138 offset:11264\n"
"v_add_u32 v243, 0x80, v253\n"
"v_mul_u32_u24 v247, 0x199a, v243\n"
"v_lshrrev_b32 v247, 16, v247\n"
"v_mul_u32_u24 v251, 10, v247\n"
"v_mfma_f32_16x16x32_bf16 v[100:103], a[28:31], a[108:111], v[100:103]\n"
"s_waitcnt lgkmcnt(5)\n"
"v_sub_u32 v243, v243, v251\n"
"v_bfe_u32 v251, v247, 2, 1\n"
"v_lshlrev_b32 v251, 4, v251\n"
"v_and_b32 v242, 3, v247\n"
"v_add_u32 v251, v251, v242\n"
"s_barrier\n"
"v_mfma_f32_16x16x32_bf16 v[64:67], a[32:35], a[112:115], v[64:67]\n"
"ds_read_b128 a[92:95], v138 offset:12288\n"
"v_mfma_f32_16x16x32_bf16 v[84:87], a[36:39], a[112:115], v[84:87]\n"
"s_mul_i32 s99, s1, 0x6e000\n"
"s_add_u32 s99, s99, s90\n"
"s_add_u32 s99, s99, 0x12c000\n"
"s_cmp_ge_u32 s64, 3\n"
"s_cselect_b32 s99, 0x7000000, s99\n"
"s_add_u32 m0, s88, 0xf000\n"
"s_nop 0\n"
"buffer_load_dwordx4 v128, s[24:27], s99 offen sc1 lds\n"
"v_mfma_f32_16x16x32_bf16 v[68:71], a[32:35], a[116:119], v[68:71]\n"
"ds_read_b128 a[96:99], v138 offset:13312\n"
"buffer_load_dwordx4 v128, s[24:27], s99 offen offset:1024 sc1 lds\n"
"v_mfma_f32_16x16x32_bf16 v[88:91], a[36:39], a[116:119], v[88:91]\n"
"buffer_load_dwordx4 v128, s[24:27], s99 offen offset:2048 sc1 lds\n"
"v_lshrrev_b32 v242, 3, v247\n"
"v_mfma_f32_16x16x32_bf16 v[72:75], a[32:35], a[120:123], v[72:75]\n"
"ds_read_b128 a[100:103], v138 offset:14336\n"
"v_lshl_add_u32 v251, v242, 2, v251\n"
"v_mul_u32_u24 v251, 0x500, v251\n"
"v_lshl_add_u32 v242, v243, 4, v251\n"
"v_cmp_gt_u32 vcc, 32, v253\n"
"v_mfma_f32_16x16x32_bf16 v[92:95], a[36:39], a[120:123], v[92:95]\n"
"s_mul_i32 s89, s1, 0x1000\n"
"s_add_u32 s89, s89, s92\n"
"s_add_u32 s89, s89, 0xd800\n"
"s_add_u32 s74, s89, s0\n"
"buffer_load_dwordx4 a[80:83], v128, s[24:27], s89 offen sc1\n"
"v_mfma_f32_16x16x32_bf16 v[76:79], a[32:35], a[124:127], v[76:79]\n"
"s_lshl_b32 s80, s98, 2\n"
"ds_read_b128 a[104:107], v138 offset:15360\n"
"v_mov_b32 v243, 0x40000000\n"
"s_add_u32 s80, s80, s64\n"
"s_lshl_b32 s80, s80, 2\n"
"s_add_u32 s80, s80, s1\n"
"s_mul_i32 s70, s80, 0x2800\n"
"s_mul_i32 s80, s97, 0xa0\n"
"s_add_u32 s70, s70, s80\n"
"v_mfma_f32_16x16x32_bf16 v[96:99], a[36:39], a[124:127], v[96:99]\n"
"buffer_load_dwordx4 a[84:87], v128, s[24:27], s74 offen sc1\n"
"v_cndmask_b32 v242, v243, v242, vcc\n"
"v_mfma_f32_16x16x32_bf16 v[80:83], a[32:35], a[128:131], v[80:83]\n"
"ds_read_b128 a[108:111], v138 offset:16384\n"
"v_mfma_f32_16x16x32_bf16 v[100:103], a[36:39], a[128:131], v[100:103]\n"
"s_waitcnt vmcnt(21) lgkmcnt(5)\n"
"v_mfma_f32_16x16x32_bf16 v[64:67], a[40:43], a[132:135], v[64:67]\n"
"ds_read_b128 a[112:115], v138 offset:17408\n"
"v_mfma_f32_16x16x32_bf16 v[84:87], a[44:47], a[132:135], v[84:87]\n"
"buffer_load_dwordx4 v128, s[24:27], s99 offen offset:3072 sc1 lds\n"
"v_mfma_f32_16x16x32_bf16 v[68:71], a[40:43], a[136:139], v[68:71]\n"
"ds_read_b128 a[116:119], v138 offset:18432\n"
"v_mfma_f32_16x16x32_bf16 v[88:91], a[44:47], a[136:139], v[88:91]\n"
"s_add_u32 m0, s88, 0x10000\n"
"s_nop 0\n"
"buffer_load_dwordx4 v130, s[24:27], s99 offen sc1 lds\n"
"v_mfma_f32_16x16x32_bf16 v[72:75], a[40:43], a[140:143], v[72:75]\n"
"ds_read_b128 a[120:123], v138 offset:19456\n"
"v_mfma_f32_16x16x32_bf16 v[92:95], a[44:47], a[140:143], v[92:95]\n"
"buffer_load_dwordx4 a[88:91], v128, s[24:27], s89 offen offset:1024 sc1\n"
"v_mfma_f32_16x16x32_bf16 v[76:79], a[40:43], a[144:147], v[76:79]\n"
"ds_read_b128 a[124:127], v138 offset:20480\n"
"v_mfma_f32_16x16x32_bf16 v[96:99], a[44:47], a[144:147], v[96:99]\n"
"buffer_load_dwordx4 a[152:155], v128, s[24:27], s74 offen offset:1024 sc1\n"
"v_mfma_f32_16x16x32_bf16 v[80:83], a[40:43], a[148:151], v[80:83]\n"
"ds_read_b128 a[128:131], v138 offset:21504\n"
"v_mfma_f32_16x16x32_bf16 v[100:103], a[44:47], a[148:151], v[100:103]\n"
"s_waitcnt vmcnt(22) lgkmcnt(5)\n"
"v_mfma_f32_16x16x32_bf16 v[64:67], a[48:51], a[92:95], v[64:67]\n"
"ds_read_b128 a[132:135], v138 offset:22528\n"
"v_mfma_f32_16x16x32_bf16 v[84:87], a[52:55], a[92:95], v[84:87]\n"
"buffer_load_dwordx4 a[156:159], v128, s[24:27], s89 offen offset:2048 sc1\n"
"v_mfma_f32_16x16x32_bf16 v[68:71], a[48:51], a[96:99], v[68:71]\n"
"ds_read_b128 a[136:139], v138 offset:23552\n"
"v_mfma_f32_16x16x32_bf16 v[88:91], a[52:55], a[96:99], v[88:91]\n"
"v_mfma_f32_16x16x32_bf16 v[72:75], a[48:51], a[100:103], v[72:75]\n"
"ds_read_b128 a[140:143], v138 offset:24576\n"
"v_mfma_f32_16x16x32_bf16 v[92:95], a[52:55], a[100:103], v[92:95]\n"
"buffer_load_dwordx4 a[160:163], v128, s[24:27], s74 offen offset:2048 sc1\n"
"v_mfma_f32_16x16x32_bf16 v[76:79], a[48:51], a[104:107], v[76:79]\n"
"ds_read_b128 a[144:147], v138 offset:25600\n"
"v_mfma_f32_16x16x32_bf16 v[96:99], a[52:55], a[104:107], v[96:99]\n"
"v_mfma_f32_16x16x32_bf16 v[80:83], a[48:51], a[108:111], v[80:83]\n"
"ds_read_b128 a[148:151], v138 offset:26624\n"
"v_mfma_f32_16x16x32_bf16 v[100:103], a[52:55], a[108:111], v[100:103]\n"
"s_waitcnt vmcnt(22) lgkmcnt(5)\n"
"v_mfma_f32_16x16x32_bf16 v[64:67], a[56:59], a[112:115], v[64:67]\n"
"ds_read_b128 a[92:95], v138 offset:27648\n"
"v_mfma_f32_16x16x32_bf16 v[84:87], a[60:63], a[112:115], v[84:87]\n"
"buffer_load_dwordx4 a[164:167], v128, s[24:27], s89 offen offset:3072 sc1\n"
"v_mfma_f32_16x16x32_bf16 v[68:71], a[56:59], a[116:119], v[68:71]\n"
"ds_read_b128 a[96:99], v138 offset:28672\n"
"v_mfma_f32_16x16x32_bf16 v[88:91], a[60:63], a[116:119], v[88:91]\n"
"v_mfma_f32_16x16x32_bf16 v[72:75], a[56:59], a[120:123], v[72:75]\n"
"ds_read_b128 a[100:103], v138 offset:29696\n"
"v_mfma_f32_16x16x32_bf16 v[92:95], a[60:63], a[120:123], v[92:95]\n"
"buffer_load_dwordx4 a[168:171], v128, s[24:27], s74 offen offset:3072 sc1\n"
"v_mfma_f32_16x16x32_bf16 v[76:79], a[56:59], a[124:127], v[76:79]\n"
"ds_read_b128 a[104:107], v138 offset:30720\n"
"v_mfma_f32_16x16x32_bf16 v[96:99], a[60:63], a[124:127], v[96:99]\n"
"v_mfma_f32_16x16x32_bf16 v[80:83], a[56:59], a[128:131], v[80:83]\n"
"ds_read_b128 a[108:111], v138 offset:31744\n"
"v_mfma_f32_16x16x32_bf16 v[100:103], a[60:63], a[128:131], v[100:103]\n"
"s_waitcnt lgkmcnt(5)\n"
"s_barrier\n"
"s_waitcnt vmcnt(20)\n"
"v_mfma_f32_16x16x32_bf16 v[64:67], a[64:67], a[132:135], v[64:67]\n"
"ds_read_b128 a[112:115], v174\n"
"s_waitcnt vmcnt(19)\n"
"v_mfma_f32_16x16x32_bf16 v[84:87], a[68:71], a[132:135], v[84:87]\n"
"s_lshl_b32 s99, s1, 7\n"
"s_add_u32 s99, s99, 0x200\n"
"s_add_u32 m0, s88, 0x1e000\n"
"s_nop 0\n"
"buffer_load_dwordx4 v133, s[32:35], s99 offen lds\n"
"v_mfma_f32_16x16x32_bf16 v[68:71], a[64:67], a[136:139], v[68:71]\n"
"ds_read_b128 a[116:119], v174 offset:2048\n"
"buffer_load_dwordx4 v134, s[32:35], s99 offen offset:1024 lds\n"
"v_mfma_f32_16x16x32_bf16 v[88:91], a[68:71], a[136:139], v[88:91]\n"
"buffer_load_dwordx4 v135, s[32:35], s99 offen offset:2048 lds\n"
"s_add_u32 m0, s65, 0x14000\n"
"s_nop 0\n"
"buffer_load_dwordx4 v240, s[4:7], s70 offen lds\n"
"v_mfma_f32_16x16x32_bf16 v[72:75], a[64:67], a[140:143], v[72:75]\n"
"s_add_u32 m0, s65, 0x14400\n"
"ds_read_b128 a[120:123], v174 offset:4096\n"
"s_nop 0\n"
"buffer_load_dwordx4 v241, s[4:7], s70 offen lds\n"
"v_mfma_f32_16x16x32_bf16 v[92:95], a[68:71], a[140:143], v[92:95]\n"
"s_mul_i32 s89, s1, 0x1000\n"
"s_add_u32 s89, s89, s92\n"
"s_add_u32 s89, s89, 0xf800\n"
"s_add_u32 s74, s89, s0\n"
"buffer_load_dwordx4 a[172:175], v128, s[24:27], s89 offen sc1\n"
"v_mfma_f32_16x16x32_bf16 v[76:79], a[64:67], a[144:147], v[76:79]\n"
"s_add_u32 m0, s65, 0x14800\n"
"ds_read_b128 a[124:127], v174 offset:6144\n"
"s_nop 0\n"
"buffer_load_dwordx4 v242, s[4:7], s70 offen lds\n"
"v_mfma_f32_16x16x32_bf16 v[96:99], a[68:71], a[144:147], v[96:99]\n"
"buffer_load_dwordx4 a[176:179], v128, s[24:27], s74 offen sc1\n"
"buffer_load_dwordx4 v[180:183], v188, s[24:27], 0 offen sc1\n"
"v_mfma_f32_16x16x32_bf16 v[80:83], a[64:67], a[148:151], v[80:83]\n"
"ds_read_b128 a[128:131], v174 offset:8192\n"
"buffer_load_dwordx4 v[184:187], v188, s[24:27], 0 offen offset:16 sc1\n"
"buffer_load_dwordx4 v[40:43], v188, s[24:27], 0 offen offset:32 sc1\n"
"v_mfma_f32_16x16x32_bf16 v[100:103], a[68:71], a[148:151], v[100:103]\n"
"s_waitcnt vmcnt(24) lgkmcnt(5)\n"
"buffer_load_dwordx4 v[44:47], v188, s[24:27], 0 offen offset:48 sc1\n"
"buffer_load_ushort v139, v131, s[56:59], 0 offen\n"
"v_mfma_f32_16x16x32_bf16 v[64:67], a[72:75], a[92:95], v[64:67]\n"
"ds_read_b128 a[132:135], v177\n"
"buffer_load_ushort v132, v131, s[56:59], 0 offen offset:32\n"
"buffer_load_ushort v172, v131, s[56:59], 0 offen offset:64\n"
"v_mfma_f32_16x16x32_bf16 v[84:87], a[76:79], a[92:95], v[84:87]\n"
"s_add_u32 m0, s88, 0x1e000\n"
"s_nop 0\n"
"buffer_load_dwordx4 v136, s[32:35], s99 offen offset:3072 lds\n"
"buffer_load_ushort v173, v131, s[56:59], 0 offen offset:96\n"
"v_mfma_f32_16x16x32_bf16 v[68:71], a[72:75], a[96:99], v[68:71]\n"
"ds_read_b128 a[136:139], v177 offset:2048\n"
"buffer_load_ushort v226, v131, s[56:59], 0 offen offset:128\n"
"v_lshrrev_b32 v243, 2, v253\n"
"v_mul_u32_u24 v243, 0xa0, v243\n"
"v_mfma_f32_16x16x32_bf16 v[88:91], a[76:79], a[96:99], v[88:91]\n"
"s_add_u32 m0, s88, 0x1f000\n"
"s_nop 0\n"
"buffer_load_dwordx4 v129, s[32:35], s99 offen lds\n"
"v_and_b32 v247, 3, v253\n"
"v_mfma_f32_16x16x32_bf16 v[72:75], a[72:75], a[100:103], v[72:75]\n"
"ds_read_b128 a[140:143], v177 offset:4096\n"
"v_lshl_add_u32 v243, v247, 3, v243\n"
"v_add_u32 v243, s65, v243\n"
"v_add_u32 v243, 0x14000, v243\n"
"v_mfma_f32_16x16x32_bf16 v[92:95], a[76:79], a[100:103], v[92:95]\n"
"buffer_load_dwordx4 a[180:183], v128, s[24:27], s89 offen offset:1024 sc1\n"
"v_mfma_f32_16x16x32_bf16 v[76:79], a[72:75], a[104:107], v[76:79]\n"
"ds_read_b128 a[144:147], v177 offset:6144\n"
"v_mfma_f32_16x16x32_bf16 v[96:99], a[76:79], a[104:107], v[96:99]\n"
"buffer_load_dwordx4 a[184:187], v128, s[24:27], s74 offen offset:1024 sc1\n"
"v_mfma_f32_16x16x32_bf16 v[80:83], a[72:75], a[108:111], v[80:83]\n"
"ds_read_b128 a[148:151], v177 offset:8192\n"
"v_mfma_f32_16x16x32_bf16 v[100:103], a[76:79], a[108:111], v[100:103]\n"
"s_waitcnt vmcnt(29) lgkmcnt(5)\n"
"v_mfma_f32_16x16x32_bf16 v[64:67], a[80:83], a[112:115], v[64:67]\n"
"s_lshl_b32 s80, s98, 2\n"
"ds_read_b128 a[92:95], v174 offset:10240\n"
"s_add_u32 s80, s80, s64\n"
"s_lshl_b32 s80, s80, 7\n"
"s_add_u32 s80, s80, 0x64000\n"
"v_and_b32 v140, 15, v253\n"
"v_lshl_add_u32 v140, v140, 3, s80\n"
"v_bfe_u32 v141, v253, 4, 1\n"
"v_lshlrev_b32 v141, 13, v141\n"
"v_sub_u32 v140, v140, v141\n"
"v_mfma_f32_16x16x32_bf16 v[84:87], a[84:87], a[112:115], v[84:87]\n"
"buffer_load_dwordx4 a[188:191], v128, s[24:27], s89 offen offset:2048 sc1\n"
"buffer_load_dwordx2 v[142:143], v140, s[24:27], 0 offen sc1\n"
"v_mfma_f32_16x16x32_bf16 v[68:71], a[80:83], a[116:119], v[68:71]\n"
"ds_read_b128 a[96:99], v174 offset:12288\n"
"v_mfma_f32_16x16x32_bf16 v[88:91], a[84:87], a[116:119], v[88:91]\n"
"v_mfma_f32_16x16x32_bf16 v[72:75], a[80:83], a[120:123], v[72:75]\n"
"ds_read_b128 a[100:103], v174 offset:14336\n"
"v_mfma_f32_16x16x32_bf16 v[92:95], a[84:87], a[120:123], v[92:95]\n"
"buffer_load_dwordx4 a[192:195], v128, s[24:27], s74 offen offset:2048 sc1\n"
"v_mfma_f32_16x16x32_bf16 v[76:79], a[80:83], a[124:127], v[76:79]\n"
"ds_read_b128 a[104:107], v174 offset:16384\n"
"v_mfma_f32_16x16x32_bf16 v[96:99], a[84:87], a[124:127], v[96:99]\n"
"v_mfma_f32_16x16x32_bf16 v[80:83], a[80:83], a[128:131], v[80:83]\n"
"ds_read_b128 a[108:111], v174 offset:18432\n"
"v_mfma_f32_16x16x32_bf16 v[100:103], a[84:87], a[128:131], v[100:103]\n"
"s_waitcnt vmcnt(28) lgkmcnt(5)\n"
"v_mfma_f32_16x16x32_bf16 v[64:67], a[88:91], a[132:135], v[64:67]\n"
"ds_read_b128 a[112:115], v177 offset:10240\n"
"v_mfma_f32_16x16x32_bf16 v[84:87], a[152:155], a[132:135], v[84:87]\n"
"buffer_load_dwordx4 a[196:199], v128, s[24:27], s89 offen offset:3072 sc1\n"
"v_mfma_f32_16x16x32_bf16 v[68:71], a[88:91], a[136:139], v[68:71]\n"
"ds_read_b128 a[116:119], v177 offset:12288\n"
"v_mfma_f32_16x16x32_bf16 v[88:91], a[152:155], a[136:139], v[88:91]\n"
"v_mfma_f32_16x16x32_bf16 v[72:75], a[88:91], a[140:143], v[72:75]\n"
"ds_read_b128 a[120:123], v177 offset:14336\n"
"v_mfma_f32_16x16x32_bf16 v[92:95], a[152:155], a[140:143], v[92:95]\n"
"buffer_load_dwordx4 a[200:203], v128, s[24:27], s74 offen offset:3072 sc1\n"
"v_mfma_f32_16x16x32_bf16 v[76:79], a[88:91], a[144:147], v[76:79]\n"
"ds_read_b128 a[124:127], v177 offset:16384\n"
"v_mfma_f32_16x16x32_bf16 v[96:99], a[152:155], a[144:147], v[96:99]\n"
"v_mfma_f32_16x16x32_bf16 v[80:83], a[88:91], a[148:151], v[80:83]\n"
"ds_read_b128 a[128:131], v177 offset:18432\n"
"v_mfma_f32_16x16x32_bf16 v[100:103], a[152:155], a[148:151], v[100:103]\n"
"s_waitcnt lgkmcnt(5)\n"
"s_waitcnt vmcnt(19)\n"
"ds_read_b64_tr_b16 v[48:49], v243\n"
"ds_read_b64_tr_b16 v[50:51], v243 offset:32\n"
"ds_read_b64_tr_b16 v[52:53], v243 offset:64\n"
"ds_read_b64_tr_b16 v[54:55], v243 offset:96\n"
"ds_read_b64_tr_b16 v[56:57], v243 offset:128\n"
"s_waitcnt lgkmcnt(0)\n"
"s_barrier\n"
"v_mfma_f32_16x16x32_bf16 v[64:67], a[156:159], a[92:95], v[64:67]\n"
"ds_read_b128 a[132:135], v174 offset:20480\n"
"v_mfma_f32_16x16x32_bf16 v[84:87], a[160:163], a[92:95], v[84:87]\n"
"s_lshl_b32 s99, s1, 7\n"
"s_add_u32 s99, s99, 0x300\n"
"s_add_u32 m0, s88, 0x23000\n"
"s_nop 0\n"
"buffer_load_dwordx4 v133, s[32:35], s99 offen lds\n"
"v_mfma_f32_16x16x32_bf16 v[68:71], a[156:159], a[96:99], v[68:71]\n"
"ds_read_b128 a[136:139], v174 offset:22528\n"
"s_waitcnt vmcnt(18)\n"
"v_mul_f32 v180, v180, v182\n"
"s_waitcnt vmcnt(17)\n"
"v_mul_f32 v184, v184, v186\n"
"s_waitcnt vmcnt(16)\n"
"v_mul_f32 v40, v40, v42\n"
"s_waitcnt vmcnt(15)\n"
"v_mul_f32 v44, v44, v46\n"
"v_mfma_f32_16x16x32_bf16 v[88:91], a[160:163], a[96:99], v[88:91]\n"
"buffer_load_dwordx4 v134, s[32:35], s99 offen offset:1024 lds\n"
"s_waitcnt vmcnt(15)\n"
"v_lshlrev_b32 v139, 16, v139\n"
"v_mfma_f32_16x16x32_bf16 v[72:75], a[156:159], a[100:103], v[72:75]\n"
"ds_read_b128 a[140:143], v174 offset:24576\n"
"s_waitcnt vmcnt(14)\n"
"v_lshlrev_b32 v132, 16, v132\n"
"s_waitcnt vmcnt(13)\n"
"v_lshlrev_b32 v172, 16, v172\n"
"s_waitcnt vmcnt(11)\n"
"v_lshlrev_b32 v173, 16, v173\n"
"s_waitcnt vmcnt(10)\n"
"v_lshlrev_b32 v226, 16, v226\n"
"v_mfma_f32_16x16x32_bf16 v[92:95], a[160:163], a[100:103], v[92:95]\n"
"buffer_load_dwordx4 v135, s[32:35], s99 offen offset:2048 lds\n"
"v_lshlrev_b32 v152, 16, v48\n"
"v_mfma_f32_16x16x32_bf16 v[76:79], a[156:159], a[104:107], v[76:79]\n"
"ds_read_b128 a[144:147], v174 offset:26624\n"
"v_and_b32 v153, 0xffff0000, v48\n"
"v_lshlrev_b32 v154, 16, v49\n"
"v_and_b32 v155, 0xffff0000, v49\n"
"v_fma_f32 v240, v152, v182, -v180\n"
"v_mfma_f32_16x16x32_bf16 v[96:99], a[160:163], a[104:107], v[96:99]\n"
"v_fma_f32 v241, v153, v186, -v184\n"
"v_fma_f32 v242, v154, v42, -v40\n"
"v_fma_f32 v243, v155, v46, -v44\n"
"v_fmac_f32 v152, v240, v139\n"
"v_fmac_f32 v153, v241, v139\n"
"v_mfma_f32_16x16x32_bf16 v[80:83], a[156:159], a[108:111], v[80:83]\n"
"ds_read_b128 a[148:151], v174 offset:28672\n"
"v_fmac_f32 v154, v242, v139\n"
"v_fmac_f32 v155, v243, v139\n"
"v_lshlrev_b32 v156, 16, v50\n"
"v_and_b32 v157, 0xffff0000, v50\n"
"v_mfma_f32_16x16x32_bf16 v[100:103], a[160:163], a[108:111], v[100:103]\n"
"v_lshlrev_b32 v158, 16, v51\n"
"v_and_b32 v159, 0xffff0000, v51\n"
"v_fma_f32 v240, v156, v182, -v180\n"
"v_fma_f32 v241, v157, v186, -v184\n"
"v_fma_f32 v242, v158, v42, -v40\n"
"v_mfma_f32_16x16x32_bf16 v[64:67], a[164:167], a[112:115], v[64:67]\n"
"ds_read_b128 a[92:95], v177 offset:20480\n"
"v_fma_f32 v243, v159, v46, -v44\n"
"v_fmac_f32 v156, v240, v132\n"
"v_fmac_f32 v157, v241, v132\n"
"v_fmac_f32 v158, v242, v132\n"
"v_mfma_f32_16x16x32_bf16 v[84:87], a[168:171], a[112:115], v[84:87]\n"
"buffer_load_dwordx4 v136, s[32:35], s99 offen offset:3072 lds\n"
"v_fmac_f32 v159, v243, v132\n"
"v_mfma_f32_16x16x32_bf16 v[68:71], a[164:167], a[116:119], v[68:71]\n"
"s_add_u32 s80, s76, 2\n"
"ds_read_b128 a[96:99], v177 offset:22528\n"
"s_waitcnt vmcnt(7)\n"
"v_subrev_u32 v141, s80, v142\n"
"v_cmp_lt_u32 vcc, 1, v141\n"
"v_cmp_ne_u32 s[82:83], s77, v143\n"
"s_nop 0\n"
"s_or_b32 s82, s82, vcc_lo\n"
"s_and_b32 s82, s82, 0xffff07ff\n"
"s_cbranch_scc1 L%=_f2dM_slow\n"
"L%=_f2dM_back:\n"
"v_mfma_f32_16x16x32_bf16 v[88:91], a[168:171], a[116:119], v[88:91]\n"
"v_lshlrev_b32 v160, 16, v52\n"
"v_and_b32 v161, 0xffff0000, v52\n"
"v_lshlrev_b32 v162, 16, v53\n"
"v_and_b32 v163, 0xffff0000, v53\n"
"v_fma_f32 v240, v160, v182, -v180\n"
"v_mfma_f32_16x16x32_bf16 v[72:75], a[164:167], a[120:123], v[72:75]\n"
"ds_read_b128 a[100:103], v177 offset:24576\n"
"v_fma_f32 v241, v161, v186, -v184\n"
"v_fma_f32 v242, v162, v42, -v40\n"
"v_fma_f32 v243, v163, v46, -v44\n"
"v_fmac_f32 v160, v240, v172\n"
"v_mfma_f32_16x16x32_bf16 v[92:95], a[168:171], a[120:123], v[92:95]\n"
"s_add_u32 m0, s88, 0x24000\n"
"s_nop 0\n"
"buffer_load_dwordx4 v129, s[32:35], s99 offen lds\n"
"v_fmac_f32 v161, v241, v172\n"
"v_mfma_f32_16x16x32_bf16 v[76:79], a[164:167], a[124:127], v[76:79]\n"
"ds_read_b128 a[104:107], v177 offset:26624\n"
"v_fmac_f32 v162, v242, v172\n"
"v_fmac_f32 v163, v243, v172\n"
"v_lshlrev_b32 v164, 16, v54\n"
"v_and_b32 v165, 0xffff0000, v54\n"
"v_mfma_f32_16x16x32_bf16 v[96:99], a[168:171], a[124:127], v[96:99]\n"
"v_lshlrev_b32 v166, 16, v55\n"
"v_and_b32 v167, 0xffff0000, v55\n"
"v_fma_f32 v240, v164, v182, -v180\n"
"v_fma_f32 v241, v165, v186, -v184\n"
"v_fma_f32 v242, v166, v42, -v40\n"
"v_mfma_f32_16x16x32_bf16 v[80:83], a[164:167], a[128:131], v[80:83]\n"
"ds_read_b128 a[108:111], v177 offset:28672\n"
"v_fma_f32 v243, v167, v46, -v44\n"
"v_fmac_f32 v164, v240, v173\n"
"v_fmac_f32 v165, v241, v173\n"
"v_fmac_f32 v166, v242, v173\n"
"v_mfma_f32_16x16x32_bf16 v[100:103], a[168:171], a[128:131], v[100:103]\n"
"s_waitcnt lgkmcnt(5)\n"
"v_fmac_f32 v167, v243, v173\n"
"v_lshlrev_b32 v168, 16, v56\n"
"v_and_b32 v169, 0xffff0000, v56\n"
"v_lshlrev_b32 v170, 16, v57\n"
"v_and_b32 v171, 0xffff0000, v57\n"
"v_mfma_f32_16x16x32_bf16 v[64:67], a[172:175], a[132:135], v[64:67]\n"
"ds_read_b128 a[112:115], v174 offset:30720\n"
"v_fma_f32 v240, v168, v182, -v180\n"
"v_fma_f32 v241, v169, v186, -v184\n"
"v_fma_f32 v242, v170, v42, -v40\n"
"v_fma_f32 v243, v171, v46, -v44\n"
"v_mfma_f32_16x16x32_bf16 v[84:87], a[176:179], a[132:135], v[84:87]\n"
"s_mul_i32 s74, s1, 0x2c00\n"
"s_add_u32 s74, s74, s92\n"
"s_add_u32 s74, s74, 0x5800\n"
"s_add_u32 s74, s74, s0\n"
"buffer_load_dwordx4 a[204:207], v128, s[24:27], s74 offen sc1\n"
"v_mfma_f32_16x16x32_bf16 v[68:71], a[172:175], a[136:139], v[68:71]\n"
"ds_read_b128 a[116:119], v174 offset:32768\n"
"v_fmac_f32 v168, v240, v226\n"
"v_fmac_f32 v169, v241, v226\n"
"v_fmac_f32 v170, v242, v226\n"
"v_fmac_f32 v171, v243, v226\n"
"v_mfma_f32_16x16x32_bf16 v[88:91], a[176:179], a[136:139], v[88:91]\n"
"buffer_load_dwordx4 a[208:211], v128, s[24:27], s74 offen offset:1024 sc1\n"
"v_mfma_f32_16x16x32_bf16 v[72:75], a[172:175], a[140:143], v[72:75]\n"
"ds_read_b128 a[120:123], v174 offset:34816\n"
"v_mfma_f32_16x16x32_bf16 v[92:95], a[176:179], a[140:143], v[92:95]\n"
"buffer_load_dwordx4 a[212:215], v128, s[24:27], s74 offen offset:2048 sc1\n"
"v_mfma_f32_16x16x32_bf16 v[76:79], a[172:175], a[144:147], v[76:79]\n"
"ds_read_b128 a[124:127], v174 offset:36864\n"
"v_mfma_f32_16x16x32_bf16 v[96:99], a[176:179], a[144:147], v[96:99]\n"
"buffer_load_dwordx4 a[216:219], v128, s[24:27], s74 offen offset:3072 sc1\n"
"v_mfma_f32_16x16x32_bf16 v[80:83], a[172:175], a[148:151], v[80:83]\n"
"ds_read_b128 a[128:131], v174 offset:38912\n"
"v_mfma_f32_16x16x32_bf16 v[100:103], a[176:179], a[148:151], v[100:103]\n"
"s_waitcnt lgkmcnt(5)\n"
"v_mfma_f32_16x16x32_bf16 v[64:67], a[180:183], a[92:95], v[64:67]\n"
"ds_read_b128 a[132:135], v177 offset:30720\n"
"v_mfma_f32_16x16x32_bf16 v[84:87], a[184:187], a[92:95], v[84:87]\n"
"s_mul_i32 s74, s1, 0x2c00\n"
"s_add_u32 s74, s74, s92\n"
"s_add_u32 s74, s74, 0x6800\n"
"s_add_u32 s74, s74, s0\n"
"buffer_load_dwordx4 a[220:223], v128, s[24:27], s74 offen sc1\n"
"v_mfma_f32_16x16x32_bf16 v[68:71], a[180:183], a[96:99], v[68:71]\n"
"ds_read_b128 a[136:139], v177 offset:32768\n"
"v_mfma_f32_16x16x32_bf16 v[88:91], a[184:187], a[96:99], v[88:91]\n"
"buffer_load_dwordx4 a[224:227], v128, s[24:27], s74 offen offset:1024 sc1\n"
"v_mfma_f32_16x16x32_bf16 v[72:75], a[180:183], a[100:103], v[72:75]\n"
"ds_read_b128 a[140:143], v177 offset:34816\n"
"v_mfma_f32_16x16x32_bf16 v[92:95], a[184:187], a[100:103], v[92:95]\n"
"buffer_load_dwordx4 a[228:231], v128, s[24:27], s74 offen offset:2048 sc1\n"
"v_mfma_f32_16x16x32_bf16 v[76:79], a[180:183], a[104:107], v[76:79]\n"
"ds_read_b128 a[144:147], v177 offset:36864\n"
"v_mfma_f32_16x16x32_bf16 v[96:99], a[184:187], a[104:107], v[96:99]\n"
"buffer_load_dwordx4 a[232:235], v128, s[24:27], s74 offen offset:3072 sc1\n"
"v_mfma_f32_16x16x32_bf16 v[80:83], a[180:183], a[108:111], v[80:83]\n"
"ds_read_b128 a[148:151], v177 offset:38912\n"
"v_mfma_f32_16x16x32_bf16 v[100:103], a[184:187], a[108:111], v[100:103]\n"
"s_waitcnt lgkmcnt(5)\n"
"s_barrier\n"
"v_mfma_f32_16x16x32_bf16 v[64:67], a[188:191], a[112:115], v[64:67]\n"
"ds_read_b128 a[92:95], v137 offset:36864\n"
"s_waitcnt vmcnt(15)\n"
"v_mfma_f32_16x16x32_bf16 v[84:87], a[192:195], a[112:115], v[84:87]\n"
"s_lshl_b32 s99, s1, 7\n"
"s_add_u32 s99, s99, 0x400\n"
"s_add_u32 m0, s88, 0x14000\n"
"s_nop 0\n"
"buffer_load_dwordx4 v133, s[32:35], s99 offen lds\n"
"v_mfma_f32_16x16x32_bf16 v[68:71], a[188:191], a[116:119], v[68:71]\n"
"ds_read_b128 a[96:99], v137 offset:37888\n"
"buffer_load_dwordx4 v134, s[32:35], s99 offen offset:1024 lds\n"
"v_mfma_f32_16x16x32_bf16 v[88:91], a[192:195], a[116:119], v[88:91]\n"
"buffer_load_dwordx4 v135, s[32:35], s99 offen offset:2048 lds\n"
"v_mfma_f32_16x16x32_bf16 v[72:75], a[188:191], a[120:123], v[72:75]\n"
"ds_read_b128 a[100:103], v137 offset:38912\n"
"v_mfma_f32_16x16x32_bf16 v[92:95], a[192:195], a[120:123], v[92:95]\n"
"s_mul_i32 s74, s1, 0x2c00\n"
"s_add_u32 s74, s74, s92\n"
"s_add_u32 s74, s74, 0x7800\n"
"s_add_u32 s74, s74, s0\n"
"buffer_load_dwordx4 a[236:239], v128, s[24:27], s74 offen sc1\n"
"v_mfma_f32_16x16x32_bf16 v[76:79], a[188:191], a[124:127], v[76:79]\n"
"ds_read_b128 a[104:107], v137 offset:39936\n"
"v_mfma_f32_16x16x32_bf16 v[96:99], a[192:195], a[124:127], v[96:99]\n"
"buffer_load_dwordx4 a[240:243], v128, s[24:27], s74 offen offset:1024 sc1\n"
"v_mfma_f32_16x16x32_bf16 v[80:83], a[188:191], a[128:131], v[80:83]\n"
"ds_read_b128 a[108:111], v137 offset:40960\n"
"v_mfma_f32_16x16x32_bf16 v[100:103], a[192:195], a[128:131], v[100:103]\n"
"s_waitcnt vmcnt(18) lgkmcnt(5)\n"
"v_mfma_f32_16x16x32_bf16 v[64:67], a[196:199], a[132:135], v[64:67]\n"
"ds_read_b128 a[112:115], v137 offset:41984\n"
"v_mfma_f32_16x16x32_bf16 v[84:87], a[200:203], a[132:135], v[84:87]\n"
"buffer_load_dwordx4 v136, s[32:35], s99 offen offset:3072 lds\n"
"v_mfma_f32_16x16x32_bf16 v[68:71], a[196:199], a[136:139], v[68:71]\n"
"ds_read_b128 a[116:119], v137 offset:43008\n"
"v_mfma_f32_16x16x32_bf16 v[88:91], a[200:203], a[136:139], v[88:91]\n"
"s_add_u32 m0, s88, 0x15000\n"
"s_nop 0\n"
"buffer_load_dwordx4 v129, s[32:35], s99 offen lds\n"
"v_mfma_f32_16x16x32_bf16 v[72:75], a[196:199], a[140:143], v[72:75]\n"
"ds_read_b128 a[120:123], v137 offset:44032\n"
"v_mfma_f32_16x16x32_bf16 v[92:95], a[200:203], a[140:143], v[92:95]\n"
"buffer_load_dwordx4 a[244:247], v128, s[24:27], s74 offen offset:2048 sc1\n"
"v_mfma_f32_16x16x32_bf16 v[76:79], a[196:199], a[144:147], v[76:79]\n"
"ds_read_b128 a[124:127], v137 offset:45056\n"
"v_mfma_f32_16x16x32_bf16 v[96:99], a[200:203], a[144:147], v[96:99]\n"
"s_mul_i32 s74, s1, 0x1000\n"
"s_add_u32 s74, s74, s92\n"
"s_add_u32 s74, s74, 0x11800\n"
"s_add_u32 s74, s74, s0\n"
"buffer_load_dwordx4 a[248:251], v128, s[24:27], s74 offen sc1\n"
"v_mfma_f32_16x16x32_bf16 v[80:83], a[196:199], a[148:151], v[80:83]\n"
"ds_read_b128 a[128:131], v137 offset:46080\n"
"v_mfma_f32_16x16x32_bf16 v[100:103], a[200:203], a[148:151], v[100:103]\n"
"s_waitcnt vmcnt(16) lgkmcnt(5)\n"
"v_mfma_f32_16x16x32_bf16 v[84:87], a[204:207], a[92:95], v[84:87]\n"
"ds_read_b128 a[132:135], v137 offset:47104\n"
"v_mfma_f32_16x16x32_bf16 v[88:91], a[204:207], a[96:99], v[88:91]\n"
"ds_read_b128 a[136:139], v137 offset:48128\n"
"buffer_load_dwordx4 a[252:255], v128, s[24:27], s74 offen offset:1024 sc1\n"
"v_mfma_f32_16x16x32_bf16 v[92:95], a[204:207], a[100:103], v[92:95]\n"
"ds_read_b128 a[140:143], v137 offset:49152\n"
"v_mfma_f32_16x16x32_bf16 v[96:99], a[204:207], a[104:107], v[96:99]\n"
"ds_read_b128 a[144:147], v137 offset:50176\n"
"v_mfma_f32_16x16x32_bf16 v[100:103], a[204:207], a[108:111], v[100:103]\n"
"ds_read_b128 a[148:151], v137 offset:51200\n"
"s_waitcnt vmcnt(16) lgkmcnt(5)\n"
"v_mfma_f32_16x16x32_bf16 v[84:87], a[208:211], a[112:115], v[84:87]\n"
"ds_read_b128 a[92:95], v137 offset:52224\n"
"v_mfma_f32_16x16x32_bf16 v[88:91], a[208:211], a[116:119], v[88:91]\n"
"ds_read_b128 a[96:99], v137 offset:53248\n"
"buffer_load_dwordx4 a[0:3], v128, s[24:27], s74 offen offset:2048 sc1\n"
"v_mfma_f32_16x16x32_bf16 v[92:95], a[208:211], a[120:123], v[92:95]\n"
"ds_read_b128 a[100:103], v137 offset:54272\n"
"v_mfma_f32_16x16x32_bf16 v[96:99], a[208:211], a[124:127], v[96:99]\n"
"ds_read_b128 a[104:107], v137 offset:55296\n"
"v_mfma_f32_16x16x32_bf16 v[100:103], a[208:211], a[128:131], v[100:103]\n"
"ds_read_b128 a[108:111], v137 offset:56320\n"
"s_barrier\n"
"s_waitcnt lgkmcnt(5)\n"
"s_waitcnt vmcnt(16)\n"
"v_mfma_f32_16x16x32_bf16 v[84:87], a[212:215], a[132:135], v[84:87]\n"
"ds_read_b128 a[112:115], v128 offset:40960\n"
"v_mfma_f32_16x16x32_bf16 v[88:91], a[212:215], a[136:139], v[88:91]\n"
"ds_read_b128 a[116:119], v128 offset:41984\n"
"buffer_load_dwordx4 a[4:7], v128, s[24:27], s74 offen offset:3072 sc1\n"
"v_mfma_f32_16x16x32_bf16 v[92:95], a[212:215], a[140:143], v[92:95]\n"
"ds_read_b128 a[120:123], v128 offset:43008\n"
"v_mfma_f32_16x16x32_bf16 v[96:99], a[212:215], a[144:147], v[96:99]\n"
"ds_read_b128 a[124:127], v128 offset:44032\n"
"v_mfma_f32_16x16x32_bf16 v[100:103], a[212:215], a[148:151], v[100:103]\n"
"ds_read_b128 a[128:131], v128 offset:45056\n"
"s_waitcnt vmcnt(16) lgkmcnt(5)\n"
"v_mfma_f32_16x16x32_bf16 v[84:87], a[216:219], a[92:95], v[84:87]\n"
"ds_read_b128 a[132:135], v128 offset:46080\n"
"v_mfma_f32_16x16x32_bf16 v[88:91], a[216:219], a[96:99], v[88:91]\n"
"s_mul_i32 s74, s1, 0x1000\n"
"s_add_u32 s74, s74, s92\n"
"s_add_u32 s74, s74, 0x13800\n"
"s_add_u32 s74, s74, s0\n"
"ds_read_b128 a[136:139], v128 offset:47104\n"
"buffer_load_dwordx4 a[8:11], v128, s[24:27], s74 offen sc1\n"
"v_mfma_f32_16x16x32_bf16 v[92:95], a[216:219], a[100:103], v[92:95]\n"
"ds_read_b128 a[140:143], v128 offset:48128\n"
"v_mfma_f32_16x16x32_bf16 v[96:99], a[216:219], a[104:107], v[96:99]\n"
"ds_read_b128 a[144:147], v128 offset:49152\n"
"v_mfma_f32_16x16x32_bf16 v[100:103], a[216:219], a[108:111], v[100:103]\n"
"ds_read_b128 a[148:151], v128 offset:50176\n"
"s_waitcnt vmcnt(16) lgkmcnt(5)\n"
"v_mfma_f32_16x16x32_bf16 v[84:87], a[220:223], a[112:115], v[84:87]\n"
"s_lshl_b32 s80, s98, 2\n"
"ds_read_b128 a[92:95], v128 offset:51200\n"
"s_add_u32 s80, s80, s64\n"
"s_lshl_b32 s80, s80, 7\n"
"s_add_u32 s80, s80, 0x64000\n"
"v_and_b32 v140, 15, v253\n"
"v_lshl_add_u32 v140, v140, 3, s80\n"
"v_mfma_f32_16x16x32_bf16 v[88:91], a[220:223], a[116:119], v[88:91]\n"
"ds_read_b128 a[96:99], v128 offset:52224\n"
"buffer_load_dwordx4 a[12:15], v128, s[24:27], s74 offen offset:1024 sc1\n"
"v_mfma_f32_16x16x32_bf16 v[92:95], a[220:223], a[120:123], v[92:95]\n"
"ds_read_b128 a[100:103], v128 offset:53248\n"
"buffer_load_dwordx2 v[142:143], v140, s[24:27], 0 offen sc1\n"
"v_mfma_f32_16x16x32_bf16 v[96:99], a[220:223], a[124:127], v[96:99]\n"
"ds_read_b128 a[104:107], v128 offset:54272\n"
"v_mfma_f32_16x16x32_bf16 v[100:103], a[220:223], a[128:131], v[100:103]\n"
"ds_read_b128 a[108:111], v128 offset:55296\n"
"s_waitcnt vmcnt(17) lgkmcnt(5)\n"
"v_mfma_f32_16x16x32_bf16 v[84:87], a[224:227], a[132:135], v[84:87]\n"
"ds_read_b128 a[112:115], v128 offset:56320\n"
"v_mfma_f32_16x16x32_bf16 v[88:91], a[224:227], a[136:139], v[88:91]\n"
"ds_read_b128 a[116:119], v128 offset:57344\n"
"buffer_load_dwordx4 a[16:19], v128, s[24:27], s74 offen offset:2048 sc1\n"
"v_mfma_f32_16x16x32_bf16 v[92:95], a[224:227], a[140:143], v[92:95]\n"
"ds_read_b128 a[120:123], v128 offset:58368\n"
"v_mfma_f32_16x16x32_bf16 v[96:99], a[224:227], a[144:147], v[96:99]\n"
"ds_read_b128 a[124:127], v128 offset:59392\n"
"v_mfma_f32_16x16x32_bf16 v[100:103], a[224:227], a[148:151], v[100:103]\n"
"ds_read_b128 a[128:131], v128 offset:60416\n"
"s_waitcnt vmcnt(17) lgkmcnt(5)\n"
"v_mfma_f32_16x16x32_bf16 v[84:87], a[228:231], a[92:95], v[84:87]\n"
"ds_read_b128 a[132:135], v128 offset:61440\n"
"v_mfma_f32_16x16x32_bf16 v[88:91], a[228:231], a[96:99], v[88:91]\n"
"ds_read_b128 a[136:139], v128 offset:62464\n"
"buffer_load_dwordx4 a[20:23], v128, s[24:27], s74 offen offset:3072 sc1\n"
"v_mfma_f32_16x16x32_bf16 v[92:95], a[228:231], a[100:103], v[92:95]\n"
"ds_read_b128 a[140:143], v128 offset:63488\n"
"v_mfma_f32_16x16x32_bf16 v[96:99], a[228:231], a[104:107], v[96:99]\n"
"ds_read_b128 a[144:147], v128 offset:64512\n"
"v_mfma_f32_16x16x32_bf16 v[100:103], a[228:231], a[108:111], v[100:103]\n"
"ds_read_b128 a[148:151], v137\n"
"s_waitcnt vmcnt(17) lgkmcnt(5)\n"
"v_mfma_f32_16x16x32_bf16 v[84:87], a[232:235], a[112:115], v[84:87]\n"
"ds_read_b128 a[92:95], v137 offset:1024\n"
"v_mfma_f32_16x16x32_bf16 v[88:91], a[232:235], a[116:119], v[88:91]\n"
"ds_read_b128 a[96:99], v137 offset:2048\n"
"v_mfma_f32_16x16x32_bf16 v[92:95], a[232:235], a[120:123], v[92:95]\n"
"ds_read_b128 a[100:103], v137 offset:3072\n"
"v_mfma_f32_16x16x32_bf16 v[96:99], a[232:235], a[124:127], v[96:99]\n"
"ds_read_b128 a[104:107], v137 offset:4096\n"
"v_mfma_f32_16x16x32_bf16 v[100:103], a[232:235], a[128:131], v[100:103]\n"
"ds_read_b128 a[108:111], v137 offset:5120\n"
"s_waitcnt vmcnt(13) lgkmcnt(5)\n"
"v_mfma_f32_16x16x32_bf16 v[84:87], a[236:239], a[132:135], v[84:87]\n"
"ds_read_b128 a[112:115], v137 offset:6144\n"
"v_mfma_f32_16x16x32_bf16 v[88:91], a[236:239], a[136:139], v[88:91]\n"
"ds_read_b128 a[116:119], v137 offset:7168\n"
"v_mfma_f32_16x16x32_bf16 v[92:95], a[236:239], a[140:143], v[92:95]\n"
"ds_read_b128 a[120:123], v137 offset:8192\n"
"v_mfma_f32_16x16x32_bf16 v[96:99], a[236:239], a[144:147], v[96:99]\n"
"ds_read_b128 a[124:127], v137 offset:9216\n"
"v_mfma_f32_16x16x32_bf16 v[100:103], a[236:239], a[148:151], v[100:103]\n"
"ds_read_b128 a[128:131], v137 offset:10240\n"
"s_waitcnt vmcnt(10)\n"
"s_barrier\n"
"s_waitcnt lgkmcnt(5)\n"
"v_mfma_f32_16x16x32_bf16 v[84:87], a[240:243], a[92:95], v[84:87]\n"
"ds_read_b128 a[132:135], v175 offset:57344\n"
"v_mfma_f32_16x16x32_bf16 v[88:91], a[240:243], a[96:99], v[88:91]\n"
"ds_read_b128 a[136:139], v175 offset:59392\n"
"v_mfma_f32_16x16x32_bf16 v[92:95], a[240:243], a[100:103], v[92:95]\n"
"ds_read_b128 a[140:143], v175 offset:61440\n"
"v_mfma_f32_16x16x32_bf16 v[96:99], a[240:243], a[104:107], v[96:99]\n"
"ds_read_b128 a[144:147], v175 offset:63488\n"
"v_mfma_f32_16x16x32_bf16 v[100:103], a[240:243], a[108:111], v[100:103]\n"
"ds_read_b128 a[148:151], v176\n"
"s_waitcnt vmcnt(9) lgkmcnt(5)\n"
"v_mfma_f32_16x16x32_bf16 v[84:87], a[244:247], a[112:115], v[84:87]\n"
"ds_read_b128 a[92:95], v178 offset:57344\n"
"v_mfma_f32_16x16x32_bf16 v[88:91], a[244:247], a[116:119], v[88:91]\n"
"ds_read_b128 a[96:99], v178 offset:59392\n"
"v_mfma_f32_16x16x32_bf16 v[92:95], a[244:247], a[120:123], v[92:95]\n"
"ds_read_b128 a[100:103], v178 offset:61440\n"
"v_mfma_f32_16x16x32_bf16 v[96:99], a[244:247], a[124:127], v[96:99]\n"
"ds_read_b128 a[104:107], v178 offset:63488\n"
"v_mfma_f32_16x16x32_bf16 v[100:103], a[244:247], a[128:131], v[100:103]\n"
"ds_read_b128 a[108:111], v179\n"
"s_waitcnt vmcnt(8) lgkmcnt(5)\n"
"v_mfma_f32_16x16x32_bf16 v[84:87], a[248:251], a[132:135], v[84:87]\n"
"ds_read_b128 a[112:115], v176 offset:2048\n"
"v_mfma_f32_16x16x32_bf16 v[88:91], a[248:251], a[136:139], v[88:91]\n"
"ds_read_b128 a[116:119], v176 offset:4096\n"
"v_mfma_f32_16x16x32_bf16 v[92:95], a[248:251], a[140:143], v[92:95]\n"
"s_add_u32 s80, s76, 3\n"
"ds_read_b128 a[120:123], v176 offset:6144\n"
"s_waitcnt vmcnt(2)\n"
"v_subrev_u32 v141, s80, v142\n"
"v_cmp_lt_u32 vcc, 0, v141\n"
"v_cmp_ne_u32 s[82:83], s77, v143\n"
"s_nop 0\n"
"s_or_b32 s82, s82, vcc_lo\n"
"s_and_b32 s82, s82, 0x7800\n"
"s_cbranch_scc1 L%=_f2eM_slow\n"
"L%=_f2eM_back:\n"
"v_mfma_f32_16x16x32_bf16 v[96:99], a[248:251], a[144:147], v[96:99]\n"
"ds_read_b128 a[124:127], v176 offset:8192\n"
"v_mfma_f32_16x16x32_bf16 v[100:103], a[248:251], a[148:151], v[100:103]\n"
"ds_read_b128 a[128:131], v176 offset:10240\n"
"s_waitcnt lgkmcnt(5)\n"
"v_mfma_f32_16x16x32_bf16 v[84:87], a[252:255], a[92:95], v[84:87]\n"
"ds_read_b128 a[132:135], v179 offset:2048\n"
"v_mfma_f32_16x16x32_bf16 v[88:91], a[252:255], a[96:99], v[88:91]\n"
"s_mul_i32 s74, s1, 0x1000\n"
"s_add_u32 s74, s74, s92\n"
"s_add_u32 s74, s74, 0x15800\n"
"s_add_u32 s74, s74, s0\n"
"ds_read_b128 a[136:139], v179 offset:4096\n"
"buffer_load_dwordx4 a[24:27], v128, s[24:27], s74 offen sc1\n"
"v_mfma_f32_16x16x32_bf16 v[92:95], a[252:255], a[100:103], v[92:95]\n"
"ds_read_b128 a[140:143], v179 offset:6144\n"
"v_mfma_f32_16x16x32_bf16 v[96:99], a[252:255], a[104:107], v[96:99]\n"
"ds_read_b128 a[144:147], v179 offset:8192\n"
"buffer_load_dwordx4 a[28:31], v128, s[24:27], s74 offen offset:1024 sc1\n"
"v_mfma_f32_16x16x32_bf16 v[100:103], a[252:255], a[108:111], v[100:103]\n"
"ds_read_b128 a[148:151], v179 offset:10240\n"
"s_waitcnt lgkmcnt(5)\n"
"v_mfma_f32_16x16x32_bf16 v[84:87], a[0:3], a[112:115], v[84:87]\n"
"ds_read_b128 a[92:95], v176 offset:12288\n"
"v_mfma_f32_16x16x32_bf16 v[88:91], a[0:3], a[116:119], v[88:91]\n"
"ds_read_b128 a[96:99], v176 offset:14336\n"
"buffer_load_dwordx4 a[32:35], v128, s[24:27], s74 offen offset:2048 sc1\n"
"v_mfma_f32_16x16x32_bf16 v[92:95], a[0:3], a[120:123], v[92:95]\n"
"ds_read_b128 a[100:103], v176 offset:16384\n"
"v_mfma_f32_16x16x32_bf16 v[96:99], a[0:3], a[124:127], v[96:99]\n"
"ds_read_b128 a[104:107], v176 offset:18432\n"
"buffer_load_dwordx4 a[36:39], v128, s[24:27], s74 offen offset:3072 sc1\n"
"v_mfma_f32_16x16x32_bf16 v[100:103], a[0:3], a[128:131], v[100:103]\n"
"ds_read_b128 a[108:111], v176 offset:20480\n"
"s_waitcnt lgkmcnt(5)\n"
"v_mfma_f32_16x16x32_bf16 v[84:87], a[4:7], a[132:135], v[84:87]\n"
"ds_read_b128 a[112:115], v179 offset:12288\n"
"v_mfma_f32_16x16x32_bf16 v[88:91], a[4:7], a[136:139], v[88:91]\n"
"s_mul_i32 s89, s1, 0x2c00\n"
"s_add_u32 s89, s89, s92\n"
"s_add_u32 s89, s89, 0x5800\n"
"ds_read_b128 a[116:119], v179 offset:14336\n"
"buffer_load_dwordx4 a[40:43], v128, s[24:27], s89 offen sc1\n"
"v_mfma_f32_16x16x32_bf16 v[92:95], a[4:7], a[140:143], v[92:95]\n"
"ds_read_b128 a[120:123], v179 offset:16384\n"
"v_mfma_f32_16x16x32_bf16 v[96:99], a[4:7], a[144:147], v[96:99]\n"
"ds_read_b128 a[124:127], v179 offset:18432\n"
"buffer_load_dwordx4 a[44:47], v128, s[24:27], s89 offen offset:1024 sc1\n"
"v_mfma_f32_16x16x32_bf16 v[100:103], a[4:7], a[148:151], v[100:103]\n"
"ds_read_b128 a[128:131], v179 offset:20480\n"
"s_waitcnt lgkmcnt(5)\n"
"v_mfma_f32_16x16x32_bf16 v[84:87], a[8:11], a[92:95], v[84:87]\n"
"ds_read_b128 a[132:135], v176 offset:22528\n"
"v_mov_b32 v1, s76\n"
"v_mov_b32 v3, s76\n"
"v_mfma_f32_16x16x32_bf16 v[88:91], a[8:11], a[96:99], v[88:91]\n"
"ds_read_b128 a[136:139], v176 offset:24576\n"
"buffer_load_dwordx4 a[48:51], v128, s[24:27], s89 offen offset:2048 sc1\n"
"v_mfma_f32_16x16x32_bf16 v[92:95], a[8:11], a[100:103], v[92:95]\n"
"ds_read_b128 a[140:143], v176 offset:26624\n"
"v_mov_b32 v5, s76\n"
"v_mov_b32 v7, s76\n"
"v_mfma_f32_16x16x32_bf16 v[96:99], a[8:11], a[104:107], v[96:99]\n"
"ds_read_b128 a[144:147], v176 offset:28672\n"
"buffer_load_dwordx4 a[52:55], v128, s[24:27], s89 offen offset:3072 sc1\n"
"v_mfma_f32_16x16x32_bf16 v[100:103], a[8:11], a[108:111], v[100:103]\n"
"ds_read_b128 a[148:151], v176 offset:30720\n"
"v_mov_b32 v9, s76\n"
"v_mov_b32 v11, s76\n"
"s_waitcnt lgkmcnt(5)\n"
"v_mfma_f32_16x16x32_bf16 v[84:87], a[12:15], a[112:115], v[84:87]\n"
"ds_read_b128 a[92:95], v179 offset:22528\n"
"v_mov_b32 v13, s76\n"
"v_mov_b32 v15, s76\n"
"v_mfma_f32_16x16x32_bf16 v[88:91], a[12:15], a[116:119], v[88:91]\n"
"s_mul_i32 s89, s1, 0x2c00\n"
"s_add_u32 s89, s89, s92\n"
"s_add_u32 s89, s89, 0x6800\n"
"ds_read_b128 a[96:99], v179 offset:24576\n"
"buffer_load_dwordx4 a[56:59], v128, s[24:27], s89 offen sc1\n"
"v_mfma_f32_16x16x32_bf16 v[92:95], a[12:15], a[120:123], v[92:95]\n"
"ds_read_b128 a[100:103], v179 offset:26624\n"
"v_mov_b32 v17, s76\n"
"v_mov_b32 v19, s76\n"
"v_mfma_f32_16x16x32_bf16 v[96:99], a[12:15], a[124:127], v[96:99]\n"
"ds_read_b128 a[104:107], v179 offset:28672\n"
"buffer_load_dwordx4 a[60:63], v128, s[24:27], s89 offen offset:1024 sc1\n"
"v_mfma_f32_16x16x32_bf16 v[100:103], a[12:15], a[128:131], v[100:103]\n"
"s_xor_b32 s80, s3, 4\n"
"ds_read_b128 a[108:111], v179 offset:30720\n"
"s_lshl_b32 s80, s80, 2\n"
"s_add_u32 s80, s80, s64\n"
"s_mul_i32 s80, s80, 0x1400\n"
"s_add_u32 s68, s80, 0x1000000\n"
"s_waitcnt vmcnt(11) lgkmcnt(5)\n"
"v_mfma_f32_16x16x32_bf16 v[84:87], a[16:19], a[132:135], v[84:87]\n"
"s_lshl_b32 s80, s3, 2\n"
"ds_read_b128 a[112:115], v175 offset:16384\n"
"s_add_u32 s80, s80, s64\n"
"s_mul_i32 s80, s80, 0x1400\n"
"s_add_u32 s69, s80, 0x1000000\n"
"v_mfma_f32_16x16x32_bf16 v[88:91], a[16:19], a[136:139], v[88:91]\n"
"ds_read_b128 a[116:119], v175 offset:18432\n"
"buffer_load_dwordx4 a[64:67], v128, s[24:27], s89 offen offset:2048 sc1\n"
"v_mfma_f32_16x16x32_bf16 v[92:95], a[16:19], a[140:143], v[92:95]\n"
"ds_read_b128 a[120:123], v175 offset:20480\n"
"v_mfma_f32_16x16x32_bf16 v[96:99], a[16:19], a[144:147], v[96:99]\n"
"ds_read_b128 a[124:127], v175 offset:22528\n"
"v_mfma_f32_16x16x32_bf16 v[100:103], a[16:19], a[148:151], v[100:103]\n"
"ds_read_b128 a[128:131], v175 offset:24576\n"
"s_waitcnt vmcnt(11) lgkmcnt(5)\n"
"v_mfma_f32_16x16x32_bf16 v[84:87], a[20:23], a[92:95], v[84:87]\n"
"ds_read_b128 a[132:135], v178 offset:16384\n"
"v_mfma_f32_16x16x32_bf16 v[88:91], a[20:23], a[96:99], v[88:91]\n"
"ds_read_b128 a[136:139], v178 offset:18432\n"
"buffer_load_dwordx4 a[68:71], v128, s[24:27], s89 offen offset:3072 sc1\n"
"v_mfma_f32_16x16x32_bf16 v[92:95], a[20:23], a[100:103], v[92:95]\n"
"ds_read_b128 a[140:143], v178 offset:20480\n"
"v_mfma_f32_16x16x32_bf16 v[96:99], a[20:23], a[104:107], v[96:99]\n"
"ds_read_b128 a[144:147], v178 offset:22528\n"
"v_mfma_f32_16x16x32_bf16 v[100:103], a[20:23], a[108:111], v[100:103]\n"
"ds_read_b128 a[148:151], v178 offset:24576\n"
"s_waitcnt vmcnt(11) lgkmcnt(5)\n"
"v_mfma_f32_16x16x32_bf16 v[84:87], a[24:27], a[112:115], v[84:87]\n"
"ds_read_b128 a[92:95], v175 offset:26624\n"
"v_mfma_f32_16x16x32_bf16 v[88:91], a[24:27], a[116:119], v[88:91]\n"
"s_mul_i32 s89, s1, 0x2c00\n"
"s_add_u32 s89, s89, s92\n"
"s_add_u32 s89, s89, 0x7800\n"
"ds_read_b128 a[96:99], v175 offset:28672\n"
"buffer_load_dwordx4 a[72:75], v128, s[24:27], s89 offen sc1\n"
"v_mfma_f32_16x16x32_bf16 v[92:95], a[24:27], a[120:123], v[92:95]\n"
"ds_read_b128 a[100:103], v175 offset:30720\n"
"v_mfma_f32_16x16x32_bf16 v[96:99], a[24:27], a[124:127], v[96:99]\n"
"ds_read_b128 a[104:107], v175 offset:32768\n"
"v_mfma_f32_16x16x32_bf16 v[100:103], a[24:27], a[128:131], v[100:103]\n"
"ds_read_b128 a[108:111], v175 offset:34816\n"
"s_waitcnt vmcnt(11) lgkmcnt(5)\n"
"v_mfma_f32_16x16x32_bf16 v[84:87], a[28:31], a[132:135], v[84:87]\n"
"ds_read_b128 a[112:115], v178 offset:26624\n"
"v_mfma_f32_16x16x32_bf16 v[88:91], a[28:31], a[136:139], v[88:91]\n"
"ds_read_b128 a[116:119], v178 offset:28672\n"
"buffer_load_dwordx4 a[76:79], v128, s[24:27], s89 offen offset:1024 sc1\n"
"v_mfma_f32_16x16x32_bf16 v[92:95], a[28:31], a[140:143], v[92:95]\n"
"ds_read_b128 a[120:123], v178 offset:30720\n"
"v_mfma_f32_16x16x32_bf16 v[96:99], a[28:31], a[144:147], v[96:99]\n"
"ds_read_b128 a[124:127], v178 offset:32768\n"
"v_mfma_f32_16x16x32_bf16 v[100:103], a[28:31], a[148:151], v[100:103]\n"
"ds_read_b128 a[128:131], v178 offset:34816\n"
"s_waitcnt vmcnt(11) lgkmcnt(5)\n"
"v_mfma_f32_16x16x32_bf16 v[84:87], a[32:35], a[92:95], v[84:87]\n"
"ds_read_b128 a[132:135], v137 offset:36864\n"
"v_mfma_f32_16x16x32_bf16 v[88:91], a[32:35], a[96:99], v[88:91]\n"
"ds_read_b128 a[136:139], v137 offset:37888\n"
"buffer_load_dwordx4 a[80:83], v128, s[24:27], s89 offen offset:2048 sc1\n"
"v_mfma_f32_16x16x32_bf16 v[92:95], a[32:35], a[100:103], v[92:95]\n"
"ds_read_b128 a[140:143], v137 offset:38912\n"
"v_mfma_f32_16x16x32_bf16 v[96:99], a[32:35], a[104:107], v[96:99]\n"
"ds_read_b128 a[144:147], v137 offset:39936\n"
"v_mfma_f32_16x16x32_bf16 v[100:103], a[32:35], a[108:111], v[100:103]\n"
"ds_read_b128 a[148:151], v137 offset:40960\n"
"s_waitcnt vmcnt(11) lgkmcnt(5)\n"
"v_mfma_f32_16x16x32_bf16 v[84:87], a[36:39], a[112:115], v[84:87]\n"
"ds_read_b128 a[92:95], v137 offset:41984\n"
"v_mfma_f32_16x16x32_bf16 v[88:91], a[36:39], a[116:119], v[88:91]\n"
"s_mul_i32 s89, s1, 0x1000\n"
"s_add_u32 s89, s89, s92\n"
"s_add_u32 s89, s89, 0x11800\n"
"ds_read_b128 a[96:99], v137 offset:43008\n"
"buffer_load_dwordx4 a[84:87], v128, s[24:27], s89 offen sc1\n"
"v_mfma_f32_16x16x32_bf16 v[92:95], a[36:39], a[120:123], v[92:95]\n"
"ds_read_b128 a[100:103], v137 offset:44032\n"
"v_mfma_f32_16x16x32_bf16 v[96:99], a[36:39], a[124:127], v[96:99]\n"
"ds_read_b128 a[104:107], v137 offset:45056\n"
"v_mfma_f32_16x16x32_bf16 v[100:103], a[36:39], a[128:131], v[100:103]\n"
"ds_read_b128 a[108:111], v137 offset:46080\n"
"s_waitcnt vmcnt(11) lgkmcnt(5)\n"
"v_mfma_f32_16x16x32_bf16 v[64:67], a[40:43], a[132:135], v[64:67]\n"
"ds_read_b128 a[112:115], v137 offset:47104\n"
"v_mfma_f32_16x16x32_bf16 v[68:71], a[40:43], a[136:139], v[68:71]\n"
"ds_read_b128 a[116:119], v137 offset:48128\n"
"buffer_load_dwordx4 a[88:91], v128, s[24:27], s89 offen offset:1024 sc1\n"
"v_mfma_f32_16x16x32_bf16 v[72:75], a[40:43], a[140:143], v[72:75]\n"
"ds_read_b128 a[120:123], v137 offset:49152\n"
"v_cvt_pk_bf16_f32 v0, v84, v85\n"
"v_cvt_pk_bf16_f32 v2, v86, v87\n"
"buffer_store_dwordx4 v[0:3], v128, s[24:27], s68 offen sc1\n"
"v_mfma_f32_16x16x32_bf16 v[76:79], a[40:43], a[144:147], v[76:79]\n"
"ds_read_b128 a[124:127], v137 offset:50176\n"
"v_cvt_pk_bf16_f32 v4, v88, v89\n"
"v_cvt_pk_bf16_f32 v6, v90, v91\n"
"buffer_store_dwordx4 v[4:7], v128, s[24:27], s68 offen offset:1024 sc1\n"
"v_mfma_f32_16x16x32_bf16 v[80:83], a[40:43], a[148:151], v[80:83]\n"
"ds_read_b128 a[128:131], v137 offset:51200\n"
"v_cvt_pk_bf16_f32 v8, v92, v93\n"
"v_cvt_pk_bf16_f32 v10, v94, v95\n"
"buffer_store_dwordx4 v[8:11], v128, s[24:27], s68 offen offset:2048 sc1\n"
"s_waitcnt vmcnt(14) lgkmcnt(5)\n"
"v_mfma_f32_16x16x32_bf16 v[64:67], a[44:47], a[92:95], v[64:67]\n"
"ds_read_b128 a[132:135], v137 offset:52224\n"
"v_cvt_pk_bf16_f32 v12, v96, v97\n"
"v_cvt_pk_bf16_f32 v14, v98, v99\n"
"buffer_store_dwordx4 v[12:15], v128, s[24:27], s68 offen offset:3072 sc1\n"
"v_mfma_f32_16x16x32_bf16 v[68:71], a[44:47], a[96:99], v[68:71]\n"
"ds_read_b128 a[136:139], v137 offset:53248\n"
"buffer_load_dwordx4 a[152:155], v128, s[24:27], s89 offen offset:2048 sc1\n"
"v_mfma_f32_16x16x32_bf16 v[72:75], a[44:47], a[100:103], v[72:75]\n"
"ds_read_b128 a[140:143], v137 offset:54272\n"
"v_cvt_pk_bf16_f32 v16, v100, v101\n"
"v_cvt_pk_bf16_f32 v18, v102, v103\n"
"buffer_store_dwordx4 v[16:19], v130, s[24:27], s68 offen sc1\n"
"v_mfma_f32_16x16x32_bf16 v[76:79], a[44:47], a[104:107], v[76:79]\n"
"ds_read_b128 a[144:147], v137 offset:55296\n"
"v_mfma_f32_16x16x32_bf16 v[80:83], a[44:47], a[108:111], v[80:83]\n"
"ds_read_b128 a[148:151], v137 offset:56320\n"
"s_waitcnt vmcnt(16) lgkmcnt(5)\n"
"v_mfma_f32_16x16x32_bf16 v[64:67], a[48:51], a[112:115], v[64:67]\n"
"ds_read_b128 a[92:95], v128 offset:40960\n"
"v_mfma_f32_16x16x32_bf16 v[68:71], a[48:51], a[116:119], v[68:71]\n"
"ds_read_b128 a[96:99], v128 offset:41984\n"
"buffer_load_dwordx4 a[156:159], v128, s[24:27], s89 offen offset:3072 sc1\n"
"v_mfma_f32_16x16x32_bf16 v[72:75], a[48:51], a[120:123], v[72:75]\n"
"ds_read_b128 a[100:103], v128 offset:43008\n"
"v_mfma_f32_16x16x32_bf16 v[76:79], a[48:51], a[124:127], v[76:79]\n"
"ds_read_b128 a[104:107], v128 offset:44032\n"
"v_mfma_f32_16x16x32_bf16 v[80:83], a[48:51], a[128:131], v[80:83]\n"
"ds_read_b128 a[108:111], v128 offset:45056\n"
"s_waitcnt vmcnt(16) lgkmcnt(5)\n"
"v_mfma_f32_16x16x32_bf16 v[64:67], a[52:55], a[132:135], v[64:67]\n"
"ds_read_b128 a[112:115], v128 offset:46080\n"
"v_mfma_f32_16x16x32_bf16 v[68:71], a[52:55], a[136:139], v[68:71]\n"
"s_mul_i32 s89, s1, 0x1000\n"
"s_add_u32 s89, s89, s92\n"
"s_add_u32 s89, s89, 0x13800\n"
"ds_read_b128 a[116:119], v128 offset:47104\n"
"buffer_load_dwordx4 a[160:163], v128, s[24:27], s89 offen sc1\n"
"v_mfma_f32_16x16x32_bf16 v[72:75], a[52:55], a[140:143], v[72:75]\n"
"ds_read_b128 a[120:123], v128 offset:48128\n"
"v_mfma_f32_16x16x32_bf16 v[76:79], a[52:55], a[144:147], v[76:79]\n"
"ds_read_b128 a[124:127], v128 offset:49152\n"
"v_mfma_f32_16x16x32_bf16 v[80:83], a[52:55], a[148:151], v[80:83]\n"
"ds_read_b128 a[128:131], v128 offset:50176\n"
"s_waitcnt vmcnt(16) lgkmcnt(5)\n"
"v_mfma_f32_16x16x32_bf16 v[64:67], a[56:59], a[92:95], v[64:67]\n"
"ds_read_b128 a[132:135], v128 offset:51200\n"
"v_mfma_f32_16x16x32_bf16 v[68:71], a[56:59], a[96:99], v[68:71]\n"
"ds_read_b128 a[136:139], v128 offset:52224\n"
"buffer_load_dwordx4 a[164:167], v128, s[24:27], s89 offen offset:1024 sc1\n"
"v_mfma_f32_16x16x32_bf16 v[72:75], a[56:59], a[100:103], v[72:75]\n"
"ds_read_b128 a[140:143], v128 offset:53248\n"
"v_mfma_f32_16x16x32_bf16 v[76:79], a[56:59], a[104:107], v[76:79]\n"
"ds_read_b128 a[144:147], v128 offset:54272\n"
"v_mfma_f32_16x16x32_bf16 v[80:83], a[56:59], a[108:111], v[80:83]\n"
"ds_read_b128 a[148:151], v128 offset:55296\n"
"s_waitcnt vmcnt(16) lgkmcnt(5)\n"
"v_mfma_f32_16x16x32_bf16 v[64:67], a[60:63], a[112:115], v[64:67]\n"
"ds_read_b128 a[92:95], v128 offset:56320\n"
"v_mfma_f32_16x16x32_bf16 v[68:71], a[60:63], a[116:119], v[68:71]\n"
"ds_read_b128 a[96:99], v128 offset:57344\n"
"buffer_load_dwordx4 a[168:171], v128, s[24:27], s89 offen offset:2048 sc1\n"
"v_mfma_f32_16x16x32_bf16 v[72:75], a[60:63], a[120:123], v[72:75]\n"
"ds_read_b128 a[100:103], v128 offset:58368\n"
"v_mfma_f32_16x16x32_bf16 v[76:79], a[60:63], a[124:127], v[76:79]\n"
"ds_read_b128 a[104:107], v128 offset:59392\n"
"buffer_load_dwordx4 a[172:175], v128, s[24:27], s89 offen offset:3072 sc1\n"
"v_mfma_f32_16x16x32_bf16 v[80:83], a[60:63], a[128:131], v[80:83]\n"
"ds_read_b128 a[108:111], v128 offset:60416\n"
"s_waitcnt vmcnt(17) lgkmcnt(5)\n"
"v_mfma_f32_16x16x32_bf16 v[64:67], a[64:67], a[132:135], v[64:67]\n"
"ds_read_b128 a[112:115], v128 offset:61440\n"
"v_mfma_f32_16x16x32_bf16 v[68:71], a[64:67], a[136:139], v[68:71]\n"
"s_mul_i32 s89, s1, 0x1000\n"
"s_add_u32 s89, s89, s92\n"
"s_add_u32 s89, s89, 0x15800\n"
"ds_read_b128 a[116:119], v128 offset:62464\n"
"buffer_load_dwordx4 a[176:179], v128, s[24:27], s89 offen sc1\n"
"v_mfma_f32_16x16x32_bf16 v[72:75], a[64:67], a[140:143], v[72:75]\n"
"ds_read_b128 a[120:123], v128 offset:63488\n"
"v_mfma_f32_16x16x32_bf16 v[76:79], a[64:67], a[144:147], v[76:79]\n"
"ds_read_b128 a[124:127], v128 offset:64512\n"
"buffer_load_dwordx4 a[180:183], v128, s[24:27], s89 offen offset:1024 sc1\n"
"v_mfma_f32_16x16x32_bf16 v[80:83], a[64:67], a[148:151], v[80:83]\n"
"ds_read_b128 a[128:131], v137\n"
"s_waitcnt vmcnt(18) lgkmcnt(5)\n"
"v_mfma_f32_16x16x32_bf16 v[64:67], a[68:71], a[92:95], v[64:67]\n"
"ds_read_b128 a[132:135], v137 offset:1024\n"
"v_mfma_f32_16x16x32_bf16 v[68:71], a[68:71], a[96:99], v[68:71]\n"
"ds_read_b128 a[136:139], v137 offset:2048\n"
"buffer_load_dwordx4 a[184:187], v128, s[24:27], s89 offen offset:2048 sc1\n"
"v_mfma_f32_16x16x32_bf16 v[72:75], a[68:71], a[100:103], v[72:75]\n"
"ds_read_b128 a[140:143], v137 offset:3072\n"
"v_mfma_f32_16x16x32_bf16 v[76:79], a[68:71], a[104:107], v[76:79]\n"
"ds_read_b128 a[144:147], v137 offset:4096\n"
"buffer_load_dwordx4 a[188:191], v128, s[24:27], s89 offen offset:3072 sc1\n"
"v_mfma_f32_16x16x32_bf16 v[80:83], a[68:71], a[108:111], v[80:83]\n"
"ds_read_b128 a[148:151], v137 offset:5120\n"
"s_waitcnt vmcnt(19) lgkmcnt(5)\n"
"v_mfma_f32_16x16x32_bf16 v[64:67], a[72:75], a[112:115], v[64:67]\n"
"ds_read_b128 a[92:95], v137 offset:6144\n"
"v_mfma_f32_16x16x32_bf16 v[68:71], a[72:75], a[116:119], v[68:71]\n"
"ds_read_b128 a[96:99], v137 offset:7168\n"
"v_mfma_f32_16x16x32_bf16 v[72:75], a[72:75], a[120:123], v[72:75]\n"
"ds_read_b128 a[100:103], v137 offset:8192\n"
"v_mfma_f32_16x16x32_bf16 v[76:79], a[72:75], a[124:127], v[76:79]\n"
"ds_read_b128 a[104:107], v137 offset:9216\n"
"v_mfma_f32_16x16x32_bf16 v[80:83], a[72:75], a[128:131], v[80:83]\n"
"ds_read_b128 a[108:111], v137 offset:10240\n"
"s_waitcnt vmcnt(18) lgkmcnt(5)\n"
"v_mfma_f32_16x16x32_bf16 v[64:67], a[76:79], a[132:135], v[64:67]\n"
"ds_read_b128 a[112:115], v175 offset:57344\n"
"v_mfma_f32_16x16x32_bf16 v[68:71], a[76:79], a[136:139], v[68:71]\n"
"ds_read_b128 a[116:119], v175 offset:59392\n"
"v_mfma_f32_16x16x32_bf16 v[72:75], a[76:79], a[140:143], v[72:75]\n"
"ds_read_b128 a[120:123], v175 offset:61440\n"
"v_mfma_f32_16x16x32_bf16 v[76:79], a[76:79], a[144:147], v[76:79]\n"
"ds_read_b128 a[124:127], v175 offset:63488\n"
"v_mfma_f32_16x16x32_bf16 v[80:83], a[76:79], a[148:151], v[80:83]\n"
"ds_read_b128 a[128:131], v176\n"
"s_waitcnt vmcnt(17) lgkmcnt(5)\n"
"v_mfma_f32_16x16x32_bf16 v[64:67], a[80:83], a[92:95], v[64:67]\n"
"ds_read_b128 a[132:135], v178 offset:57344\n"
"v_mfma_f32_16x16x32_bf16 v[68:71], a[80:83], a[96:99], v[68:71]\n"
"ds_read_b128 a[136:139], v178 offset:59392\n"
"v_mfma_f32_16x16x32_bf16 v[72:75], a[80:83], a[100:103], v[72:75]\n"
"ds_read_b128 a[140:143], v178 offset:61440\n"
"v_mfma_f32_16x16x32_bf16 v[76:79], a[80:83], a[104:107], v[76:79]\n"
"ds_read_b128 a[144:147], v178 offset:63488\n"
"v_mfma_f32_16x16x32_bf16 v[80:83], a[80:83], a[108:111], v[80:83]\n"
"ds_read_b128 a[148:151], v179\n"
"s_waitcnt vmcnt(16) lgkmcnt(5)\n"
"v_mfma_f32_16x16x32_bf16 v[64:67], a[84:87], a[112:115], v[64:67]\n"
"ds_read_b128 a[92:95], v176 offset:2048\n"
"v_mfma_f32_16x16x32_bf16 v[68:71], a[84:87], a[116:119], v[68:71]\n"
"ds_read_b128 a[96:99], v176 offset:4096\n"
"v_mfma_f32_16x16x32_bf16 v[72:75], a[84:87], a[120:123], v[72:75]\n"
"ds_read_b128 a[100:103], v176 offset:6144\n"
"v_mfma_f32_16x16x32_bf16 v[76:79], a[84:87], a[124:127], v[76:79]\n"
"ds_read_b128 a[104:107], v176 offset:8192\n"
"v_mfma_f32_16x16x32_bf16 v[80:83], a[84:87], a[128:131], v[80:83]\n"
"ds_read_b128 a[108:111], v176 offset:10240\n"
"s_waitcnt vmcnt(15) lgkmcnt(5)\n"
"v_mfma_f32_16x16x32_bf16 v[64:67], a[88:91], a[132:135], v[64:67]\n"
"ds_read_b128 a[112:115], v179 offset:2048\n"
"v_mfma_f32_16x16x32_bf16 v[68:71], a[88:91], a[136:139], v[68:71]\n"
"ds_read_b128 a[116:119], v179 offset:4096\n"
"v_mfma_f32_16x16x32_bf16 v[72:75], a[88:91], a[140:143], v[72:75]\n"
"ds_read_b128 a[120:123], v179 offset:6144\n"
"v_mfma_f32_16x16x32_bf16 v[76:79], a[88:91], a[144:147], v[76:79]\n"
"ds_read_b128 a[124:127], v179 offset:8192\n"
"v_mfma_f32_16x16x32_bf16 v[80:83], a[88:91], a[148:151], v[80:83]\n"
"ds_read_b128 a[128:131], v179 offset:10240\n"
"s_waitcnt vmcnt(10) lgkmcnt(5)\n"
"v_mfma_f32_16x16x32_bf16 v[64:67], a[152:155], a[92:95], v[64:67]\n"
"ds_read_b128 a[132:135], v176 offset:12288\n"
"buffer_load_dwordx4 v[20:23], v128, s[24:27], s69 offen sc1\n"
"v_mfma_f32_16x16x32_bf16 v[68:71], a[152:155], a[96:99], v[68:71]\n"
"ds_read_b128 a[136:139], v176 offset:14336\n"
"buffer_load_dwordx4 v[24:27], v128, s[24:27], s69 offen offset:1024 sc1\n"
"v_mfma_f32_16x16x32_bf16 v[72:75], a[152:155], a[100:103], v[72:75]\n"
"ds_read_b128 a[140:143], v176 offset:16384\n"
"buffer_load_dwordx4 v[28:31], v128, s[24:27], s69 offen offset:2048 sc1\n"
"v_mfma_f32_16x16x32_bf16 v[76:79], a[152:155], a[104:107], v[76:79]\n"
"ds_read_b128 a[144:147], v176 offset:18432\n"
"buffer_load_dwordx4 v[32:35], v128, s[24:27], s69 offen offset:3072 sc1\n"
"v_mfma_f32_16x16x32_bf16 v[80:83], a[152:155], a[108:111], v[80:83]\n"
"ds_read_b128 a[148:151], v176 offset:20480\n"
"buffer_load_dwordx4 v[36:39], v130, s[24:27], s69 offen sc1\n"
"s_waitcnt vmcnt(13) lgkmcnt(5)\n"
"v_mfma_f32_16x16x32_bf16 v[64:67], a[156:159], a[112:115], v[64:67]\n"
"ds_read_b128 a[92:95], v179 offset:12288\n"
"v_mfma_f32_16x16x32_bf16 v[68:71], a[156:159], a[116:119], v[68:71]\n"
"ds_read_b128 a[96:99], v179 offset:14336\n"
"v_mfma_f32_16x16x32_bf16 v[72:75], a[156:159], a[120:123], v[72:75]\n"
"ds_read_b128 a[100:103], v179 offset:16384\n"
"v_mfma_f32_16x16x32_bf16 v[76:79], a[156:159], a[124:127], v[76:79]\n"
"ds_read_b128 a[104:107], v179 offset:18432\n"
"v_mfma_f32_16x16x32_bf16 v[80:83], a[156:159], a[128:131], v[80:83]\n"
"ds_read_b128 a[108:111], v179 offset:20480\n"
"s_waitcnt vmcnt(12) lgkmcnt(5)\n"
"v_mfma_f32_16x16x32_bf16 v[64:67], a[160:163], a[132:135], v[64:67]\n"
"ds_read_b128 a[112:115], v176 offset:22528\n"
"v_mfma_f32_16x16x32_bf16 v[68:71], a[160:163], a[136:139], v[68:71]\n"
"ds_read_b128 a[116:119], v179 offset:22528\n"
"v_mfma_f32_16x16x32_bf16 v[72:75], a[160:163], a[140:143], v[72:75]\n"
"ds_read_b128 a[120:123], v175 offset:16384\n"
"v_mfma_f32_16x16x32_bf16 v[76:79], a[160:163], a[144:147], v[76:79]\n"
"ds_read_b128 a[124:127], v178 offset:16384\n"
"v_mfma_f32_16x16x32_bf16 v[80:83], a[160:163], a[148:151], v[80:83]\n"
"ds_read_b128 a[128:131], v175 offset:26624\n"
"s_waitcnt vmcnt(11) lgkmcnt(5)\n"
"v_mfma_f32_16x16x32_bf16 v[64:67], a[164:167], a[92:95], v[64:67]\n"
"ds_read_b128 a[132:135], v178 offset:26624\n"
"v_mfma_f32_16x16x32_bf16 v[68:71], a[164:167], a[96:99], v[68:71]\n"
"ds_read_b128 a[136:139], v176 offset:24576\n"
"v_mfma_f32_16x16x32_bf16 v[72:75], a[164:167], a[100:103], v[72:75]\n"
"ds_read_b128 a[140:143], v179 offset:24576\n"
"v_mfma_f32_16x16x32_bf16 v[76:79], a[164:167], a[104:107], v[76:79]\n"
"ds_read_b128 a[144:147], v175 offset:18432\n"
"v_mfma_f32_16x16x32_bf16 v[80:83], a[164:167], a[108:111], v[80:83]\n"
"ds_read_b128 a[148:151], v178 offset:18432\n"
"s_waitcnt vmcnt(10) lgkmcnt(5)\n"
"v_mfma_f32_16x16x32_bf16 v[64:67], a[168:171], a[112:115], v[64:67]\n"
"ds_read_b128 a[92:95], v175 offset:28672\n"
"s_waitcnt vmcnt(9)\n"
"v_mfma_f32_16x16x32_bf16 v[64:67], a[172:175], a[116:119], v[64:67]\n"
"ds_read_b128 a[96:99], v178 offset:28672\n"
"s_waitcnt vmcnt(8)\n"
"v_mfma_f32_16x16x32_bf16 v[64:67], a[176:179], a[120:123], v[64:67]\n"
"ds_read_b128 a[100:103], v176 offset:26624\n"
"s_waitcnt vmcnt(7)\n"
"v_mfma_f32_16x16x32_bf16 v[64:67], a[180:183], a[124:127], v[64:67]\n"
"ds_read_b128 a[104:107], v179 offset:26624\n"
"s_waitcnt vmcnt(6)\n"
"v_mfma_f32_16x16x32_bf16 v[64:67], a[184:187], a[128:131], v[64:67]\n"
"ds_read_b128 a[108:111], v175 offset:20480\n"
"s_waitcnt vmcnt(5) lgkmcnt(5)\n"
"v_mfma_f32_16x16x32_bf16 v[64:67], a[188:191], a[132:135], v[64:67]\n"
"ds_read_b128 a[112:115], v178 offset:20480\n"
"v_mfma_f32_16x16x32_bf16 v[68:71], a[168:171], a[136:139], v[68:71]\n"
"ds_read_b128 a[116:119], v175 offset:30720\n"
"v_mfma_f32_16x16x32_bf16 v[68:71], a[172:175], a[140:143], v[68:71]\n"
"ds_read_b128 a[120:123], v178 offset:30720\n"
"v_mfma_f32_16x16x32_bf16 v[68:71], a[176:179], a[144:147], v[68:71]\n"
"ds_read_b128 a[124:127], v176 offset:28672\n"
"v_mfma_f32_16x16x32_bf16 v[68:71], a[180:183], a[148:151], v[68:71]\n"
"ds_read_b128 a[128:131], v179 offset:28672\n"
"s_waitcnt lgkmcnt(5)\n"
"v_mfma_f32_16x16x32_bf16 v[68:71], a[184:187], a[92:95], v[68:71]\n"
"ds_read_b128 a[132:135], v175 offset:22528\n"
"v_mfma_f32_16x16x32_bf16 v[68:71], a[188:191], a[96:99], v[68:71]\n"
"ds_read_b128 a[136:139], v178 offset:22528\n"
"v_mfma_f32_16x16x32_bf16 v[72:75], a[168:171], a[100:103], v[72:75]\n"
"ds_read_b128 a[140:143], v175 offset:32768\n"
"v_mfma_f32_16x16x32_bf16 v[72:75], a[172:175], a[104:107], v[72:75]\n"
"ds_read_b128 a[144:147], v178 offset:32768\n"
"v_mfma_f32_16x16x32_bf16 v[72:75], a[176:179], a[108:111], v[72:75]\n"
"ds_read_b128 a[148:151], v176 offset:30720\n"
"s_waitcnt lgkmcnt(5)\n"
"v_mfma_f32_16x16x32_bf16 v[72:75], a[180:183], a[112:115], v[72:75]\n"
"ds_read_b128 a[92:95], v179 offset:30720\n"
"v_mfma_f32_16x16x32_bf16 v[72:75], a[184:187], a[116:119], v[72:75]\n"
"ds_read_b128 a[96:99], v175 offset:24576\n"
"v_mfma_f32_16x16x32_bf16 v[72:75], a[188:191], a[120:123], v[72:75]\n"
"ds_read_b128 a[100:103], v178 offset:24576\n"
"v_mfma_f32_16x16x32_bf16 v[76:79], a[168:171], a[124:127], v[76:79]\n"
"ds_read_b128 a[104:107], v175 offset:34816\n"
"v_mfma_f32_16x16x32_bf16 v[76:79], a[172:175], a[128:131], v[76:79]\n"
"ds_read_b128 a[108:111], v178 offset:34816\n"
"s_waitcnt lgkmcnt(5)\n"
"v_mfma_f32_16x16x32_bf16 v[76:79], a[176:179], a[132:135], v[76:79]\n"
"s_waitcnt vmcnt(4)\n"
"v_xor_b32 v244, s76, v21\n"
"v_bitop3_b32 v244, v23, s76, v244 bitop3:0xbe\n"
"s_waitcnt vmcnt(3)\n"
"v_bitop3_b32 v244, v25, s76, v244 bitop3:0xbe\n"
"v_bitop3_b32 v244, v27, s76, v244 bitop3:0xbe\n"
"s_waitcnt vmcnt(2)\n"
"v_bitop3_b32 v244, v29, s76, v244 bitop3:0xbe\n"
"v_bitop3_b32 v244, v31, s76, v244 bitop3:0xbe\n"
"s_waitcnt vmcnt(1)\n"
"v_bitop3_b32 v244, v33, s76, v244 bitop3:0xbe\n"
"v_bitop3_b32 v244, v35, s76, v244 bitop3:0xbe\n"
"s_waitcnt vmcnt(0)\n"
"v_bitop3_b32 v244, v37, s76, v244 bitop3:0xbe\n"
"v_bitop3_b32 v244, v39, s76, v244 bitop3:0xbe\n"
"v_cmp_ne_u32 vcc, 0, v244\n"
"s_nop 0\n"
"s_cbranch_vccnz L%=_stxM_slow\n"
"L%=_stxM_back:\n"
"v_mfma_f32_16x16x32_bf16 v[76:79], a[180:183], a[136:139], v[76:79]\n"
"v_lshlrev_b32 v244, 16, v20\n"
"v_and_b32 v245, 0xffff0000, v20\n"
"v_add_f32 v152, v152, v244\n"
"v_add_f32 v153, v153, v245\n"
"v_mfma_f32_16x16x32_bf16 v[76:79], a[184:187], a[140:143], v[76:79]\n"
"v_lshlrev_b32 v244, 16, v22\n"
"v_and_b32 v245, 0xffff0000, v22\n"
"v_add_f32 v154, v154, v244\n"
"v_add_f32 v155, v155, v245\n"
"v_mfma_f32_16x16x32_bf16 v[76:79], a[188:191], a[144:147], v[76:79]\n"
"v_lshlrev_b32 v244, 16, v24\n"
"v_and_b32 v245, 0xffff0000, v24\n"
"v_add_f32 v156, v156, v244\n"
"v_add_f32 v157, v157, v245\n"
"v_mfma_f32_16x16x32_bf16 v[80:83], a[168:171], a[148:151], v[80:83]\n"
"v_lshlrev_b32 v244, 16, v26\n"
"v_and_b32 v245, 0xffff0000, v26\n"
"v_add_f32 v158, v158, v244\n"
"v_add_f32 v159, v159, v245\n"
"s_waitcnt lgkmcnt(0)\n"
"v_mfma_f32_16x16x32_bf16 v[80:83], a[172:175], a[92:95], v[80:83]\n"
"v_lshlrev_b32 v244, 16, v28\n"
"v_and_b32 v245, 0xffff0000, v28\n"
"v_add_f32 v160, v160, v244\n"
"v_add_f32 v161, v161, v245\n"
"v_mfma_f32_16x16x32_bf16 v[80:83], a[176:179], a[96:99], v[80:83]\n"
"v_lshlrev_b32 v244, 16, v30\n"
"v_and_b32 v245, 0xffff0000, v30\n"
"v_add_f32 v162, v162, v244\n"
"v_add_f32 v163, v163, v245\n"
"v_mfma_f32_16x16x32_bf16 v[80:83], a[180:183], a[100:103], v[80:83]\n"
"v_lshlrev_b32 v244, 16, v32\n"
"v_and_b32 v245, 0xffff0000, v32\n"
"v_add_f32 v164, v164, v244\n"
"v_add_f32 v165, v165, v245\n"
"v_mfma_f32_16x16x32_bf16 v[80:83], a[184:187], a[104:107], v[80:83]\n"
"v_lshlrev_b32 v244, 16, v34\n"
"v_and_b32 v245, 0xffff0000, v34\n"
"v_add_f32 v166, v166, v244\n"
"v_add_f32 v167, v167, v245\n"
"v_mfma_f32_16x16x32_bf16 v[80:83], a[188:191], a[108:111], v[80:83]\n"
"v_lshlrev_b32 v244, 16, v36\n"
"v_and_b32 v245, 0xffff0000, v36\n"
"v_add_f32 v168, v168, v244\n"
"v_add_f32 v169, v169, v245\n"
"v_lshlrev_b32 v244, 16, v38\n"
"v_and_b32 v245, 0xffff0000, v38\n"
"v_add_f32 v170, v170, v244\n"
"v_add_f32 v171, v171, v245\n"
"v_add_f32 v152, v152, v64\n"
"v_add_f32 v153, v153, v65\n"
"v_add_f32 v154, v154, v66\n"
"v_add_f32 v155, v155, v67\n"
"buffer_store_dword v152, v227, s[40:43], 0 offen\n"
"buffer_store_dword v153, v227, s[40:43], 0 offen offset:2560\n"
"buffer_store_dword v154, v228, s[40:43], 0 offen\n"
"buffer_store_dword v155, v228, s[40:43], 0 offen offset:2560\n"
"v_add_f32 v156, v156, v68\n"
"v_add_f32 v157, v157, v69\n"
"v_add_f32 v158, v158, v70\n"
"v_add_f32 v159, v159, v71\n"
"buffer_store_dword v156, v227, s[40:43], 0 offen offset:64\n"
"buffer_store_dword v157, v227, s[40:43], 0 offen offset:2624\n"
"buffer_store_dword v158, v228, s[40:43], 0 offen offset:64\n"
"buffer_store_dword v159, v228, s[40:43], 0 offen offset:2624\n"
"v_add_f32 v160, v160, v72\n"
"v_add_f32 v161, v161, v73\n"
"v_add_f32 v162, v162, v74\n"
"v_add_f32 v163, v163, v75\n"
"buffer_store_dword v160, v227, s[40:43], 0 offen offset:128\n"
"buffer_store_dword v161, v227, s[40:43], 0 offen offset:2688\n"
"buffer_store_dword v162, v228, s[40:43], 0 offen offset:128\n"
"buffer_store_dword v163, v228, s[40:43], 0 offen offset:2688\n"
"v_add_f32 v164, v164, v76\n"
"v_add_f32 v165, v165, v77\n"
"v_add_f32 v166, v166, v78\n"
"v_add_f32 v167, v167, v79\n"
"buffer_store_dword v164, v227, s[40:43], 0 offen offset:192\n"
"buffer_store_dword v165, v227, s[40:43], 0 offen offset:2752\n"
"buffer_store_dword v166, v228, s[40:43], 0 offen offset:192\n"
"buffer_store_dword v167, v228, s[40:43], 0 offen offset:2752\n"
"v_add_f32 v168, v168, v80\n"
"v_add_f32 v169, v169, v81\n"
"v_add_f32 v170, v170, v82\n"
"v_add_f32 v171, v171, v83\n"
"buffer_store_dword v168, v227, s[40:43], 0 offen offset:256\n"
"buffer_store_dword v169, v227, s[40:43], 0 offen offset:2816\n"
"buffer_store_dword v170, v228, s[40:43], 0 offen offset:256\n"
"buffer_store_dword v171, v228, s[40:43], 0 offen offset:2816\n"
"s_endpgm\n"
"L%=_S_start:\n"
"s_and_b32 s83, s3, 7\n"
"s_lshr_b32 s82, s3, 3\n"
"s_and_b32 s85, s83, 1\n"
"s_lshl_b32 s86, s85, 3\n"
"s_and_b32 s85, s82, 7\n"
"s_add_u32 s66, s86, s85\n"
"s_and_b32 s86, s83, 3\n"
"s_lshl_b32 s86, s86, 2\n"
"s_and_b32 s87, s82, 3\n"
"s_add_u32 s98, s86, s87\n"
"s_lshr_b32 s97, s82, 2\n"
"s_lshl_b32 s65, s64, 12\n"
"s_mul_i32 s88, s64, 0x1400\n"
"s_lshl_b32 s2, s67, 4\n"
"s_add_u32 s2, s2, s66\n"
"v_lshrrev_b32 v234, 3, v253\n"
"v_and_b32 v235, 7, v253\n"
"v_lshrrev_b32 v236, 1, v234\n"
"v_xor_b32 v237, v235, v236\n"
"v_lshrrev_b32 v238, 2, v234\n"
"v_and_b32 v239, 3, v234\n"
"v_lshl_add_u32 v238, v238, 4, v239\n"
"v_mul_u32_u24 v238, 0x500, v238\n"
"v_lshl_add_u32 v129, v237, 4, v238\n"
"v_xor_b32 v130, 64, v129\n"
"v_lshlrev_b32 v128, 4, v253\n"
"s_lshl_b32 s82, s66, 2\n"
"s_add_u32 s82, s82, s64\n"
"s_mul_i32 s68, s82, 0xa000\n"
"s_add_u32 s69, s68, 0x1400\n"
"s_add_u32 s70, s68, 0x2800\n"
"s_add_u32 s71, s68, 0x3c00\n"
"s_and_b32 s82, s64, 1\n"
"s_lshl_b32 s83, s82, 3\n"
"s_and_b32 s84, s64, 2\n"
"v_add_u32 v238, s83, v234\n"
"v_mul_u32_u24 v238, 0xd80, v238\n"
"v_and_b32 v239, 1, v236\n"
"v_or_b32 v239, s84, v239\n"
"v_bfe_u32 v240, v253, 1, 2\n"
"v_xor_b32 v239, v239, v240\n"
"v_lshl_add_u32 v238, v239, 5, v238\n"
"v_and_b32 v239, 1, v253\n"
"v_lshl_add_u32 v131, v239, 4, v238\n"
"s_lshl_b32 s83, s64, 3\n"
"s_lshl_b32 s82, s82, 2\n"
"v_add_u32 v238, s83, v234\n"
"v_mul_u32_u24 v238, 0x500, v238\n"
"v_xor_b32 v239, s82, v237\n"
"v_lshl_add_u32 v132, v239, 4, v238\n"
"s_sub_u32 s80, s67, 11\n"
"s_mov_b32 s93, s67\n"
"s_add_u32 s94, s67, 5\n"
"s_min_u32 s94, s94, 19\n"
"s_add_u32 s96, s80, 22\n"
"s_mul_i32 s73, s93, 0xa000\n"
"s_mul_i32 s74, s94, 0xa000\n"
"s_cmp_lt_u32 s67, 15\n"
"s_cselect_b32 s74, s74, 0x7000000\n"
"s_lshl_b32 s72, s96, 7\n"
"s_and_b32 s80, s79, 3\n"
"s_lshl_b32 s80, s80, 3\n"
"v_lshrrev_b32 v232, 3, v253\n"
"v_mul_u32_u24 v233, 0xd80, v232\n"
"s_waitcnt lgkmcnt(0)\n"
"s_lshr_b32 s80, s75, s80\n"
"s_and_b32 s80, s80, 0xff\n"
"s_cmp_ge_u32 s80, 27\n"
"s_cselect_b32 s81, 27, 0\n"
"s_cselect_b32 s87, 0x200, 0\n"
"s_cselect_b32 s60, s12, s8\n"
"s_cselect_b32 s61, s13, s9\n"
"s_sub_u32 s81, s80, s81\n"
"s_lshl_b32 s82, s81, 10\n"
"s_add_u32 s89, s82, s87\n"
"s_lshl_b32 s81, s81, 7\n"
"s_mul_i32 s82, s64, 0x6c00\n"
"s_add_u32 s86, s81, s82\n"
"s_mov_b32 s62, s10\n"
"s_mov_b32 s63, s11\n"
"s_mov_b32 s0, s60\n"
"s_mov_b32 s1, s61\n"
"s_mov_b32 s84, 0x1b000\n"
"s_mov_b32 s85, 0x1b000\n"
"s_cmp_lt_u32 s80, 54\n"
"s_cbranch_scc1 L%=_S_j2go\n"
"s_sub_u32 s81, s80, 54\n"
"s_mov_b32 s60, s16\n"
"s_mov_b32 s61, s17\n"
"s_mov_b32 s0, s20\n"
"s_mov_b32 s1, s21\n"
"s_lshl_b32 s89, s81, 9\n"
"s_add_u32 s89, s89, 0x6c00\n"
"s_lshl_b32 s81, s81, 2\n"
"s_add_u32 s81, s81, s64\n"
"s_mul_i32 s86, s81, 0x2800\n"
"s_mov_b32 s84, 0x80\n"
"s_mov_b32 s85, 0xfffffb80\n"
"v_mul_u32_u24 v233, 0x500, v232\n"
"s_cmp_lt_u32 s80, 0x4a\n"
"s_cselect_b32 s62, s18, 0\n"
"s_cselect_b32 s89, s89, 0x7000000\n"
"s_cselect_b32 s86, s86, 0\n"
"L%=_S_j2go:\n"
"v_and_b32 v232, 7, v253\n"
"v_lshl_add_u32 v233, v232, 4, v233\n"
"s_add_u32 s89, s89, 0x8000\n"
"s_mov_b32 s99, s80\n"
"s_cmp_ge_u32 s64, 2\n"
"s_cselect_b32 s8, s12, s8\n"
"s_cselect_b32 s9, s13, s9\n"
"v_mov_b32 v247, v233\n"
"v_mov_b32 v251, 0\n"
"buffer_load_dwordx2 v[230:231], v251, s[24:27], 0 offen sc1\n"
"s_lshl_b32 s80, s66, 4\n"
"s_add_u32 s80, s80, s67\n"
"s_lshl_b32 s80, s80, 2\n"
"s_add_u32 s80, s80, s64\n"
"s_mul_i32 s80, s80, 0xa00\n"
"v_lshrrev_b32 v232, 5, v253\n"
"v_mul_u32_u24 v232, 0x500, v232\n"
"v_and_b32 v233, 31, v253\n"
"v_lshl_add_u32 v234, v233, 4, v232\n"
"v_lshl_add_u32 v232, v233, 3, v232\n"
"buffer_load_dwordx4 v[96:99], v234, s[4:7], s80 offen\n"
"buffer_load_dwordx4 v[100:103], v234, s[4:7], s80 offen offset:512\n"
"buffer_load_dwordx2 v[104:105], v232, s[4:7], s80 offen offset:1024\n"
"buffer_load_dwordx4 v[0:3], v247, s[60:63], s86 offen\n"
"s_add_u32 s86, s86, s84\n"
"buffer_load_dwordx4 v[4:7], v247, s[60:63], s86 offen\n"
"s_add_u32 s86, s86, s84\n"
"buffer_load_dwordx4 v[8:11], v247, s[60:63], s86 offen\n"
"s_add_u32 s86, s86, s84\n"
"buffer_load_dwordx4 v[12:15], v247, s[60:63], s86 offen\n"
"s_add_u32 s86, s86, s84\n"
"buffer_load_dwordx4 v[16:19], v247, s[60:63], s86 offen\n"
"s_add_u32 s86, s86, s84\n"
"buffer_load_dwordx4 v[20:23], v247, s[60:63], s86 offen\n"
"s_add_u32 s86, s86, s84\n"
"buffer_load_dwordx4 v[24:27], v247, s[60:63], s86 offen\n"
"s_add_u32 s86, s86, s84\n"
"buffer_load_dwordx4 v[28:31], v247, s[60:63], s86 offen\n"
"s_add_u32 s86, s86, s84\n"
"buffer_load_dwordx4 v[32:35], v247, s[60:63], s86 offen\n"
"s_add_u32 s86, s86, s84\n"
"buffer_load_dwordx4 v[36:39], v247, s[60:63], s86 offen\n"
"s_add_u32 s86, s86, s85\n"
"s_mov_b32 s60, s0\n"
"s_mov_b32 s61, s1\n"
"v_and_b32 v232, 31, v253\n"
"v_lshrrev_b32 v233, 5, v253\n"
"v_bfe_u32 v234, v232, 1, 3\n"
"v_xor_b32 v234, v234, v233\n"
"v_lshlrev_b32 v234, 4, v234\n"
"v_and_b32 v235, 7, v232\n"
"v_lshl_add_u32 v234, v235, 7, v234\n"
"v_lshrrev_b32 v235, 3, v232\n"
"v_lshl_add_u32 v236, v235, 10, v234\n"
"v_add_u32 v236, s65, v236\n"
"v_add_u32 v133, 0x8000, v236\n"
"v_lshl_add_u32 v236, v235, 12, v234\n"
"v_add_u32 v137, 0x18000, v236\n"
"v_xor_b32 v134, 32, v133\n"
"v_xor_b32 v138, 32, v137\n"
"v_xor_b32 v135, 64, v133\n"
"v_xor_b32 v139, 64, v137\n"
"v_xor_b32 v136, 0x60, v133\n"
"v_xor_b32 v140, 0x60, v137\n"
"v_bfe_u32 v234, v253, 4, 1\n"
"v_lshlrev_b32 v234, 1, v234\n"
"v_or_b32 v235, v234, v233\n"
"v_lshlrev_b32 v235, 12, v235\n"
"v_bfe_u32 v236, v253, 2, 2\n"
"v_lshl_add_u32 v235, v236, 7, v235\n"
"v_lshrrev_b32 v236, 1, v236\n"
"v_or_b32 v236, v236, v234\n"
"v_lshl_add_u32 v235, v236, 5, v235\n"
"v_and_b32 v236, 3, v253\n"
"v_lshl_add_u32 v235, v236, 3, v235\n"
"v_add_u32 v141, 0x18000, v235\n"
"v_xor_b32 v142, 32, v141\n"
"v_xor_b32 v143, 64, v141\n"
"v_xor_b32 v144, 0x60, v141\n"
"s_waitcnt vmcnt(13)\n"
"v_readfirstlane_b32 s76, v230\n"
"v_readfirstlane_b32 s77, v231\n"
"s_and_b32 s76, s76, 0xfffffffc\n"
"s_add_u32 s76, s76, 4\n"
"s_addc_u32 s77, s77, 0\n"
"s_mul_i32 s78, s76, 3\n"
"s_add_u32 s78, s78, s77\n"
"v_mov_b32 v230, s76\n"
"v_mov_b32 v231, s77\n"
"v_cmp_eq_u32 vcc, 0, v253\n"
"v_mov_b32 v229, 0x40000000\n"
"s_nop 0\n"
"v_cndmask_b32 v229, v229, 0, vcc\n"
"v_mov_b32 v232, 0\n"
"v_mov_b32 v234, 0\n"
"s_waitcnt vmcnt(12)\n"
"v_dot2c_f32_bf16 v232, 0x3f803f80, v96\n"
"v_dot2c_f32_bf16 v234, v96, v96\n"
"v_dot2c_f32_bf16 v232, 0x3f803f80, v97\n"
"v_dot2c_f32_bf16 v234, v97, v97\n"
"v_dot2c_f32_bf16 v232, 0x3f803f80, v98\n"
"v_dot2c_f32_bf16 v234, v98, v98\n"
"v_dot2c_f32_bf16 v232, 0x3f803f80, v99\n"
"v_dot2c_f32_bf16 v234, v99, v99\n"
"s_waitcnt vmcnt(11)\n"
"v_dot2c_f32_bf16 v232, 0x3f803f80, v100\n"
"v_dot2c_f32_bf16 v234, v100, v100\n"
"v_dot2c_f32_bf16 v232, 0x3f803f80, v101\n"
"v_dot2c_f32_bf16 v234, v101, v101\n"
"v_dot2c_f32_bf16 v232, 0x3f803f80, v102\n"
"v_dot2c_f32_bf16 v234, v102, v102\n"
"v_dot2c_f32_bf16 v232, 0x3f803f80, v103\n"
"v_dot2c_f32_bf16 v234, v103, v103\n"
"s_waitcnt vmcnt(10)\n"
"v_dot2c_f32_bf16 v232, 0x3f803f80, v104\n"
"v_dot2c_f32_bf16 v234, v104, v104\n"
"v_dot2c_f32_bf16 v232, 0x3f803f80, v105\n"
"v_dot2c_f32_bf16 v234, v105, v105\n"
"s_nop 1\n"
"v_add_f32_dpp v232, v232, v232 row_shr:1 row_mask:0xf bank_mask:0xf bound_ctrl:1\n"
"v_add_f32_dpp v234, v234, v234 row_shr:1 row_mask:0xf bank_mask:0xf bound_ctrl:1\n"
"s_lshl_b32 s80, s66, 4\n"
"s_add_u32 s80, s80, s67\n"
"s_lshl_b32 s80, s80, 2\n"
"s_add_u32 s80, s80, s64\n"
"s_lshl_b32 s80, s80, 5\n"
"v_lshrrev_b32 v236, 5, v253\n"
"v_lshl_add_u32 v236, v236, 4, s80\n"
"v_add_f32_dpp v232, v232, v232 row_shr:2 row_mask:0xf bank_mask:0xf bound_ctrl:1\n"
"v_add_f32_dpp v234, v234, v234 row_shr:2 row_mask:0xf bank_mask:0xf bound_ctrl:1\n"
"v_and_b32 v235, 31, v253\n"
"v_add_f32_dpp v232, v232, v232 row_shr:4 row_mask:0xf bank_mask:0xf bound_ctrl:1\n"
"v_add_f32_dpp v234, v234, v234 row_shr:4 row_mask:0xf bank_mask:0xf bound_ctrl:1\n"
"v_cmp_eq_u32 vcc, 31, v235\n"
"v_add_f32_dpp v232, v232, v232 row_shr:8 row_mask:0xf bank_mask:0xf bound_ctrl:1\n"
"v_add_f32_dpp v234, v234, v234 row_shr:8 row_mask:0xf bank_mask:0xf bound_ctrl:1\n"
"v_mov_b32 v235, 0x40000000\n"
"v_add_f32_dpp v232, v232, v232 row_bcast:15 row_mask:0xa bank_mask:0xf\n"
"v_add_f32_dpp v234, v234, v234 row_bcast:15 row_mask:0xa bank_mask:0xf\n"
"v_cndmask_b32 v236, v235, v236, vcc\n"
"v_mul_f32 v232, 0x3acccccd, v232\n"
"v_mul_f32 v234, 0x3acccccd, v234\n"
"v_fma_f32 v234, -v232, v232, v234\n"
"v_max_f32 v234, 0, v234\n"
"v_add_f32 v234, 0x3727c5ac, v234\n"
"v_rsq_f32 v234, v234\n"
"v_mov_b32 v233, s76\n"
"v_mov_b32 v235, s78\n"
"s_mov_b32 s80, 0x50000\n"
"buffer_store_dwordx4 v[232:235], v236, s[24:27], s80 offen sc1\n"
"v_add_u32 v170, 0xd400, v131\n"
"v_add_u32 v171, 0x1a800, v131\n"
"v_add_u32 v172, 0x27c00, v131\n"
"v_mov_b32 v248, 0x3f80\n"
"v_mov_b32 v249, 0x3f800000\n"
"v_mov_b32 v64, 0\n"
"v_mov_b32 v65, 0\n"
"v_mov_b32 v66, 0\n"
"v_mov_b32 v67, 0\n"
"v_mov_b32 v68, 0\n"
"v_mov_b32 v69, 0\n"
"v_mov_b32 v70, 0\n"
"v_mov_b32 v71, 0\n"
"v_mov_b32 v72, 0\n"
"v_mov_b32 v73, 0\n"
"v_mov_b32 v74, 0\n"
"v_mov_b32 v75, 0\n"
"v_mov_b32 v76, 0\n"
"v_mov_b32 v77, 0\n"
"v_mov_b32 v78, 0\n"
"v_mov_b32 v79, 0\n"
"buffer_load_dwordx4 v[40:43], v247, s[60:63], s86 offen\n"
"s_add_u32 s86, s86, s84\n"
"s_waitcnt vmcnt(11)\n"
"v_dot2c_f32_bf16 v64, v248, v0\n"
"v_dot2c_f32_bf16 v65, v249, v0\n"
"v_dot2c_f32_bf16 v66, v248, v1\n"
"v_dot2c_f32_bf16 v67, v249, v1\n"
"v_dot2c_f32_bf16 v68, v248, v2\n"
"v_dot2c_f32_bf16 v69, v249, v2\n"
"v_dot2c_f32_bf16 v70, v248, v3\n"
"v_dot2c_f32_bf16 v71, v249, v3\n"
"buffer_load_dwordx4 v[44:47], v247, s[60:63], s86 offen\n"
"s_add_u32 s86, s86, s84\n"
"s_waitcnt vmcnt(11)\n"
"v_dot2c_f32_bf16 v64, v248, v4\n"
"v_dot2c_f32_bf16 v65, v249, v4\n"
"v_dot2c_f32_bf16 v66, v248, v5\n"
"v_dot2c_f32_bf16 v67, v249, v5\n"
"v_dot2c_f32_bf16 v68, v248, v6\n"
"v_dot2c_f32_bf16 v69, v249, v6\n"
"v_dot2c_f32_bf16 v70, v248, v7\n"
"v_dot2c_f32_bf16 v71, v249, v7\n"
"buffer_load_dwordx4 v[48:51], v247, s[60:63], s86 offen\n"
"s_add_u32 s86, s86, s84\n"
"s_waitcnt vmcnt(11)\n"
"v_dot2c_f32_bf16 v64, v248, v8\n"
"v_dot2c_f32_bf16 v65, v249, v8\n"
"v_dot2c_f32_bf16 v66, v248, v9\n"
"v_dot2c_f32_bf16 v67, v249, v9\n"
"v_dot2c_f32_bf16 v68, v248, v10\n"
"v_dot2c_f32_bf16 v69, v249, v10\n"
"v_dot2c_f32_bf16 v70, v248, v11\n"
"v_dot2c_f32_bf16 v71, v249, v11\n"
"buffer_load_dwordx4 v[52:55], v247, s[60:63], s86 offen\n"
"s_add_u32 s86, s86, s84\n"
"s_waitcnt vmcnt(11)\n"
"v_dot2c_f32_bf16 v64, v248, v12\n"
"v_dot2c_f32_bf16 v65, v249, v12\n"
"v_dot2c_f32_bf16 v66, v248, v13\n"
"v_dot2c_f32_bf16 v67, v249, v13\n"
"v_dot2c_f32_bf16 v68, v248, v14\n"
"v_dot2c_f32_bf16 v69, v249, v14\n"
"v_dot2c_f32_bf16 v70, v248, v15\n"
"v_dot2c_f32_bf16 v71, v249, v15\n"
"buffer_load_dwordx4 v[56:59], v247, s[60:63], s86 offen\n"
"s_add_u32 s86, s86, s84\n"
"s_waitcnt vmcnt(11)\n"
"v_dot2c_f32_bf16 v64, v248, v16\n"
"v_dot2c_f32_bf16 v65, v249, v16\n"
"v_dot2c_f32_bf16 v66, v248, v17\n"
"v_dot2c_f32_bf16 v67, v249, v17\n"
"v_dot2c_f32_bf16 v68, v248, v18\n"
"v_dot2c_f32_bf16 v69, v249, v18\n"
"v_dot2c_f32_bf16 v70, v248, v19\n"
"v_dot2c_f32_bf16 v71, v249, v19\n"
"buffer_load_dwordx4 v[60:63], v247, s[60:63], s86 offen\n"
"s_add_u32 s86, s86, s84\n"
"s_waitcnt vmcnt(11)\n"
"v_dot2c_f32_bf16 v64, v248, v20\n"
"v_dot2c_f32_bf16 v65, v249, v20\n"
"v_dot2c_f32_bf16 v66, v248, v21\n"
"v_dot2c_f32_bf16 v67, v249, v21\n"
"v_dot2c_f32_bf16 v68, v248, v22\n"
"v_dot2c_f32_bf16 v69, v249, v22\n"
"v_dot2c_f32_bf16 v70, v248, v23\n"
"v_dot2c_f32_bf16 v71, v249, v23\n"
"buffer_load_dwordx4 v[174:177], v247, s[60:63], s86 offen\n"
"s_add_u32 s86, s86, s84\n"
"s_waitcnt vmcnt(11)\n"
"v_dot2c_f32_bf16 v64, v248, v24\n"
"v_dot2c_f32_bf16 v65, v249, v24\n"
"v_dot2c_f32_bf16 v66, v248, v25\n"
"v_dot2c_f32_bf16 v67, v249, v25\n"
"v_dot2c_f32_bf16 v68, v248, v26\n"
"v_dot2c_f32_bf16 v69, v249, v26\n"
"v_dot2c_f32_bf16 v70, v248, v27\n"
"v_dot2c_f32_bf16 v71, v249, v27\n"
"buffer_load_dwordx4 v[178:181], v247, s[60:63], s86 offen\n"
"s_add_u32 s86, s86, s84\n"
"s_waitcnt vmcnt(11)\n"
"v_dot2c_f32_bf16 v64, v248, v28\n"
"v_dot2c_f32_bf16 v65, v249, v28\n"
"v_dot2c_f32_bf16 v66, v248, v29\n"
"v_dot2c_f32_bf16 v67, v249, v29\n"
"v_dot2c_f32_bf16 v68, v248, v30\n"
"v_dot2c_f32_bf16 v69, v249, v30\n"
"v_dot2c_f32_bf16 v70, v248, v31\n"
"v_dot2c_f32_bf16 v71, v249, v31\n"
"buffer_load_dwordx4 v[182:185], v247, s[60:63], s86 offen\n"
"s_add_u32 s86, s86, s84\n"
"s_waitcnt vmcnt(11)\n"
"v_dot2c_f32_bf16 v64, v248, v32\n"
"v_dot2c_f32_bf16 v65, v249, v32\n"
"v_dot2c_f32_bf16 v66, v248, v33\n"
"v_dot2c_f32_bf16 v67, v249, v33\n"
"v_dot2c_f32_bf16 v68, v248, v34\n"
"v_dot2c_f32_bf16 v69, v249, v34\n"
"v_dot2c_f32_bf16 v70, v248, v35\n"
"v_dot2c_f32_bf16 v71, v249, v35\n"
"buffer_load_dwordx4 v[186:189], v247, s[60:63], s86 offen\n"
"s_add_u32 s86, s86, s84\n"
"s_waitcnt vmcnt(11)\n"
"v_dot2c_f32_bf16 v64, v248, v36\n"
"v_dot2c_f32_bf16 v65, v249, v36\n"
"v_dot2c_f32_bf16 v66, v248, v37\n"
"v_dot2c_f32_bf16 v67, v249, v37\n"
"v_dot2c_f32_bf16 v68, v248, v38\n"
"v_dot2c_f32_bf16 v69, v249, v38\n"
"v_dot2c_f32_bf16 v70, v248, v39\n"
"v_dot2c_f32_bf16 v71, v249, v39\n"
"s_add_u32 m0, s65, 0x10000\n"
"s_nop 0\n"
"buffer_load_dwordx4 v129, s[4:7], s68 offen lds\n"
"s_waitcnt vmcnt(10)\n"
"v_dot2c_f32_bf16 v72, v248, v40\n"
"v_dot2c_f32_bf16 v73, v249, v40\n"
"v_dot2c_f32_bf16 v74, v248, v41\n"
"v_dot2c_f32_bf16 v75, v249, v41\n"
"v_dot2c_f32_bf16 v76, v248, v42\n"
"v_dot2c_f32_bf16 v77, v249, v42\n"
"v_dot2c_f32_bf16 v78, v248, v43\n"
"v_dot2c_f32_bf16 v79, v249, v43\n"
"s_add_u32 m0, s65, 0x10400\n"
"s_nop 0\n"
"buffer_load_dwordx4 v130, s[4:7], s69 offen lds\n"
"s_waitcnt vmcnt(10)\n"
"v_dot2c_f32_bf16 v72, v248, v44\n"
"v_dot2c_f32_bf16 v73, v249, v44\n"
"v_dot2c_f32_bf16 v74, v248, v45\n"
"v_dot2c_f32_bf16 v75, v249, v45\n"
"v_dot2c_f32_bf16 v76, v248, v46\n"
"v_dot2c_f32_bf16 v77, v249, v46\n"
"v_dot2c_f32_bf16 v78, v248, v47\n"
"v_dot2c_f32_bf16 v79, v249, v47\n"
"s_add_u32 m0, s65, 0x10800\n"
"s_nop 0\n"
"buffer_load_dwordx4 v129, s[4:7], s70 offen lds\n"
"s_waitcnt vmcnt(10)\n"
"v_dot2c_f32_bf16 v72, v248, v48\n"
"v_dot2c_f32_bf16 v73, v249, v48\n"
"v_dot2c_f32_bf16 v74, v248, v49\n"
"v_dot2c_f32_bf16 v75, v249, v49\n"
"v_dot2c_f32_bf16 v76, v248, v50\n"
"v_dot2c_f32_bf16 v77, v249, v50\n"
"v_dot2c_f32_bf16 v78, v248, v51\n"
"v_dot2c_f32_bf16 v79, v249, v51\n"
"s_add_u32 m0, s65, 0x10c00\n"
"s_nop 0\n"
"buffer_load_dwordx4 v130, s[4:7], s71 offen lds\n"
"s_waitcnt vmcnt(10)\n"
"v_dot2c_f32_bf16 v72, v248, v52\n"
"v_dot2c_f32_bf16 v73, v249, v52\n"
"v_dot2c_f32_bf16 v74, v248, v53\n"
"v_dot2c_f32_bf16 v75, v249, v53\n"
"v_dot2c_f32_bf16 v76, v248, v54\n"
"v_dot2c_f32_bf16 v77, v249, v54\n"
"v_dot2c_f32_bf16 v78, v248, v55\n"
"v_dot2c_f32_bf16 v79, v249, v55\n"
"s_add_u32 m0, s65, 0x1c000\n"
"s_nop 0\n"
"buffer_load_dwordx4 v131, s[8:11], s72 offen lds\n"
"s_waitcnt vmcnt(10)\n"
"v_dot2c_f32_bf16 v72, v248, v56\n"
"v_dot2c_f32_bf16 v73, v249, v56\n"
"v_dot2c_f32_bf16 v74, v248, v57\n"
"v_dot2c_f32_bf16 v75, v249, v57\n"
"v_dot2c_f32_bf16 v76, v248, v58\n"
"v_dot2c_f32_bf16 v77, v249, v58\n"
"v_dot2c_f32_bf16 v78, v248, v59\n"
"v_dot2c_f32_bf16 v79, v249, v59\n"
"s_nop 0\n"
"buffer_load_dwordx4 v170, s[8:11], s72 offen offset:1024 lds\n"
"s_waitcnt vmcnt(10)\n"
"v_dot2c_f32_bf16 v72, v248, v60\n"
"v_dot2c_f32_bf16 v73, v249, v60\n"
"v_dot2c_f32_bf16 v74, v248, v61\n"
"v_dot2c_f32_bf16 v75, v249, v61\n"
"v_dot2c_f32_bf16 v76, v248, v62\n"
"v_dot2c_f32_bf16 v77, v249, v62\n"
"v_dot2c_f32_bf16 v78, v248, v63\n"
"v_dot2c_f32_bf16 v79, v249, v63\n"
"s_nop 0\n"
"buffer_load_dwordx4 v171, s[8:11], s72 offen offset:2048 lds\n"
"s_waitcnt vmcnt(10)\n"
"v_dot2c_f32_bf16 v72, v248, v174\n"
"v_dot2c_f32_bf16 v73, v249, v174\n"
"v_dot2c_f32_bf16 v74, v248, v175\n"
"v_dot2c_f32_bf16 v75, v249, v175\n"
"v_dot2c_f32_bf16 v76, v248, v176\n"
"v_dot2c_f32_bf16 v77, v249, v176\n"
"v_dot2c_f32_bf16 v78, v248, v177\n"
"v_dot2c_f32_bf16 v79, v249, v177\n"
"s_nop 0\n"
"buffer_load_dwordx4 v172, s[8:11], s72 offen offset:3072 lds\n"
"s_add_u32 s72, s72, 0x36000\n"
"s_waitcnt vmcnt(10)\n"
"v_dot2c_f32_bf16 v72, v248, v178\n"
"v_dot2c_f32_bf16 v73, v249, v178\n"
"v_dot2c_f32_bf16 v74, v248, v179\n"
"v_dot2c_f32_bf16 v75, v249, v179\n"
"v_dot2c_f32_bf16 v76, v248, v180\n"
"v_dot2c_f32_bf16 v77, v249, v180\n"
"v_dot2c_f32_bf16 v78, v248, v181\n"
"v_dot2c_f32_bf16 v79, v249, v181\n"
"s_waitcnt vmcnt(9)\n"
"v_dot2c_f32_bf16 v72, v248, v182\n"
"v_dot2c_f32_bf16 v73, v249, v182\n"
"v_dot2c_f32_bf16 v74, v248, v183\n"
"v_dot2c_f32_bf16 v75, v249, v183\n"
"v_dot2c_f32_bf16 v76, v248, v184\n"
"v_dot2c_f32_bf16 v77, v249, v184\n"
"v_dot2c_f32_bf16 v78, v248, v185\n"
"v_dot2c_f32_bf16 v79, v249, v185\n"
"s_waitcnt vmcnt(8)\n"
"v_dot2c_f32_bf16 v72, v248, v186\n"
"v_dot2c_f32_bf16 v73, v249, v186\n"
"v_dot2c_f32_bf16 v74, v248, v187\n"
"v_dot2c_f32_bf16 v75, v249, v187\n"
"v_dot2c_f32_bf16 v76, v248, v188\n"
"v_dot2c_f32_bf16 v77, v249, v188\n"
"v_dot2c_f32_bf16 v78, v248, v189\n"
"v_dot2c_f32_bf16 v79, v249, v189\n"
"s_cmp_lt_u32 s99, 54\n"
"s_cbranch_scc0 L%=_S_j2b\n"
"v_add_f32 v64, v64, v72\n"
"v_add_f32 v65, v65, v73\n"
"v_add_f32 v66, v66, v74\n"
"v_add_f32 v67, v67, v75\n"
"v_add_f32 v68, v68, v76\n"
"v_add_f32 v69, v69, v77\n"
"v_add_f32 v70, v70, v78\n"
"v_add_f32 v71, v71, v79\n"
"v_permlane32_swap_b32 v64, v68\n"
"v_permlane32_swap_b32 v65, v69\n"
"v_permlane32_swap_b32 v66, v70\n"
"v_permlane32_swap_b32 v67, v71\n"
"v_add_f32 v64, v64, v68\n"
"v_add_f32 v65, v65, v69\n"
"v_add_f32 v66, v66, v70\n"
"v_add_f32 v67, v67, v71\n"
"s_nop 0\n"
"v_permlane16_swap_b32 v64, v66\n"
"v_permlane16_swap_b32 v65, v67\n"
"v_add_f32 v64, v64, v66\n"
"v_add_f32 v65, v65, v67\n"
"s_nop 0\n"
"v_add_f32_dpp v64, v64, v64 row_ror:8 row_mask:0xf bank_mask:0xf\n"
"v_add_f32_dpp v65, v65, v65 row_ror:8 row_mask:0xf bank_mask:0xf\n"
"v_and_b32 v238, 7, v253\n"
"v_lshlrev_b32 v238, 3, v238\n"
"v_bfe_u32 v239, v253, 4, 1\n"
"v_lshl_add_u32 v238, v239, 1, v238\n"
"v_lshrrev_b32 v239, 5, v253\n"
"v_lshl_add_u32 v238, v239, 2, v238\n"
"s_lshl_b32 s81, s64, 8\n"
"v_lshl_add_u32 v238, v238, 2, s81\n"
"ds_write_b64 v238, v[64:65] offset:16384\n"
"s_waitcnt lgkmcnt(0)\n"
"s_barrier\n"
"v_lshlrev_b32 v238, 2, v253\n"
"ds_read_b32 v64, v238 offset:16384\n"
"ds_read_b32 v65, v238 offset:16640\n"
"ds_read_b32 v66, v238 offset:16896\n"
"ds_read_b32 v67, v238 offset:17152\n"
"s_waitcnt lgkmcnt(2)\n"
"v_add_f32 v64, v64, v65\n"
"s_waitcnt lgkmcnt(0)\n"
"v_add_f32 v66, v66, v67\n"
"s_cmp_eq_u32 s64, 0\n"
"s_cselect_b32 s81, s89, 0x7000000\n"
"v_add_f32 v64, v64, v66\n"
"v_and_b32 v238, 15, v253\n"
"v_lshrrev_b32 v239, 4, v253\n"
"v_lshl_add_u32 v238, v238, 2, v239\n"
"v_lshlrev_b32 v238, 3, v238\n"
"v_mov_b32 v65, s76\n"
"buffer_store_dwordx2 v[64:65], v238, s[24:27], s81 offen sc1\n"
"s_nop 2\n"
"s_branch L%=_S_j2done\n"
"L%=_S_j2b:\n"
"s_nop 2\n"
"v_add_f32 v64, v64, v68\n"
"v_add_f32 v65, v65, v69\n"
"v_add_f32 v66, v66, v70\n"
"v_add_f32 v67, v67, v71\n"
"v_add_f32 v72, v72, v76\n"
"v_add_f32 v73, v73, v77\n"
"v_add_f32 v74, v74, v78\n"
"v_add_f32 v75, v75, v79\n"
"v_add_f32 v64, v64, v66\n"
"v_add_f32 v65, v65, v67\n"
"v_add_f32 v72, v72, v74\n"
"v_add_f32 v73, v73, v75\n"
"v_add_f32 v64, v64, v65\n"
"v_add_f32 v72, v72, v73\n"
"s_nop 0\n"
"v_add_f32_dpp v64, v64, v64 quad_perm:[1,0,3,2] row_mask:0xf bank_mask:0xf\n"
"v_add_f32_dpp v72, v72, v72 quad_perm:[1,0,3,2] row_mask:0xf bank_mask:0xf\n"
"s_nop 0\n"
"v_add_f32_dpp v64, v64, v64 quad_perm:[2,3,0,1] row_mask:0xf bank_mask:0xf\n"
"v_add_f32_dpp v72, v72, v72 quad_perm:[2,3,0,1] row_mask:0xf bank_mask:0xf\n"
"s_nop 0\n"
"v_add_f32_dpp v64, v64, v64 row_half_mirror row_mask:0xf bank_mask:0xf\n"
"v_add_f32_dpp v72, v72, v72 row_half_mirror row_mask:0xf bank_mask:0xf\n"
"s_lshl_b32 s81, s64, 7\n"
"v_lshrrev_b32 v238, 3, v253\n"
"v_lshl_add_u32 v238, v238, 4, s81\n"
"v_and_b32 v239, 7, v253\n"
"v_cmp_eq_u32 vcc, 0, v239\n"
"v_mov_b32 v239, 0x40000000\n"
"s_nop 0\n"
"v_cndmask_b32 v238, v239, v238, vcc\n"
"v_mov_b32 v65, s76\n"
"v_mov_b32 v66, v72\n"
"v_mov_b32 v67, s76\n"
"s_barrier\n"
"buffer_store_dwordx4 v[64:67], v238, s[24:27], s89 offen sc1\n"
"s_nop 2\n"
"L%=_S_j2done:\n"
"s_lshl_b32 s80, s2, 3\n"
"s_add_u32 s80, s80, 0x1000\n"
"s_cmp_eq_u32 s64, 0\n"
"s_cselect_b32 s80, s80, 0x7000000\n"
"v_mov_b32 v230, s76\n"
"buffer_store_dwordx2 v[230:231], v229, s[24:27], s80 offen sc1\n"
"s_add_u32 m0, s65, 0x7f80\n"
"s_nop 0\n"
"buffer_load_dwordx4 v129, s[4:7], s68 offen offset:128 lds\n"
"s_add_u32 m0, s65, 0x8380\n"
"s_nop 0\n"
"buffer_load_dwordx4 v130, s[4:7], s69 offen offset:128 lds\n"
"s_add_u32 m0, s65, 0x8780\n"
"s_nop 0\n"
"buffer_load_dwordx4 v129, s[4:7], s70 offen offset:128 lds\n"
"s_add_u32 m0, s65, 0x8b80\n"
"s_nop 0\n"
"buffer_load_dwordx4 v130, s[4:7], s71 offen offset:128 lds\n"
"s_add_u32 m0, s65, 0x20000\n"
"s_nop 0\n"
"buffer_load_dwordx4 v131, s[8:11], s72 offen lds\n"
"buffer_load_dwordx4 v170, s[8:11], s72 offen offset:1024 lds\n"
"buffer_load_dwordx4 v171, s[8:11], s72 offen offset:2048 lds\n"
"buffer_load_dwordx4 v172, s[8:11], s72 offen offset:3072 lds\n"
"s_add_u32 s72, s72, 0x36000\n"
"s_lshl_b32 s80, s93, 6\n"
"v_and_b32 v232, 31, v253\n"
"v_lshlrev_b32 v232, 1, v232\n"
"buffer_load_ushort v120, v232, s[44:47], s80 offen\n"
"buffer_load_ushort v121, v232, s[48:51], s80 offen\n"
"buffer_load_ushort v122, v232, s[52:55], s80 offen\n"
"s_lshl_b32 s80, s94, 6\n"
"v_and_b32 v232, 31, v253\n"
"v_lshlrev_b32 v232, 1, v232\n"
"buffer_load_ushort v124, v232, s[44:47], s80 offen\n"
"buffer_load_ushort v125, v232, s[48:51], s80 offen\n"
"buffer_load_ushort v126, v232, s[52:55], s80 offen\n"
"s_waitcnt vmcnt(20)\n"
"s_waitcnt vmcnt(16)\n"
"s_barrier\n"
"ds_read_b128 a[0:3], v133 offset:32768\n"
"ds_read_b64_tr_b16 a[160:161], v141 offset:16384\n"
"ds_read_b64_tr_b16 a[162:163], v141 offset:16896\n"
"ds_read_b64_tr_b16 a[164:165], v142 offset:16384\n"
"ds_read_b64_tr_b16 a[166:167], v142 offset:16896\n"
"ds_read_b64_tr_b16 a[168:169], v143 offset:16384\n"
"ds_read_b64_tr_b16 a[170:171], v143 offset:16896\n"
"ds_read_b64_tr_b16 a[172:173], v144 offset:16384\n"
"ds_read_b64_tr_b16 a[174:175], v144 offset:16896\n"
"ds_read_b128 a[4:7], v134 offset:32768\n"
"ds_read_b64_tr_b16 a[176:177], v141 offset:17408\n"
"ds_read_b64_tr_b16 a[178:179], v141 offset:17920\n"
"ds_read_b64_tr_b16 a[180:181], v142 offset:17408\n"
"ds_read_b64_tr_b16 a[182:183], v142 offset:17920\n"
"ds_read_b64_tr_b16 a[184:185], v143 offset:17408\n"
"ds_read_b64_tr_b16 a[186:187], v143 offset:17920\n"
"ds_read_b64_tr_b16 a[188:189], v144 offset:17408\n"
"ds_read_b64_tr_b16 a[190:191], v144 offset:17920\n"
"s_waitcnt lgkmcnt(9)\n"
"v_mfma_f32_32x32x16_bf16 v[0:15], a[160:163], a[0:3], 0\n"
"s_add_u32 m0, s65, 0xbf00\n"
"ds_read_b128 a[8:11], v135 offset:32768\n"
"ds_read_b64_tr_b16 a[192:193], v141 offset:18432\n"
"ds_read_b64_tr_b16 a[194:195], v141 offset:18944\n"
"buffer_load_dwordx4 v129, s[4:7], s68 offen offset:256 lds\n"
"v_mfma_f32_32x32x16_bf16 v[16:31], a[164:167], a[0:3], 0\n"
"s_add_u32 m0, s65, 0xc300\n"
"ds_read_b64_tr_b16 a[196:197], v142 offset:18432\n"
"ds_read_b64_tr_b16 a[198:199], v142 offset:18944\n"
"buffer_load_dwordx4 v130, s[4:7], s69 offen offset:256 lds\n"
"v_and_b32 v238, 31, v253\n"
"v_mfma_f32_32x32x16_bf16 v[32:47], a[168:171], a[0:3], 0\n"
"s_add_u32 m0, s65, 0xc700\n"
"ds_read_b64_tr_b16 a[200:201], v143 offset:18432\n"
"ds_read_b64_tr_b16 a[202:203], v143 offset:18944\n"
"buffer_load_dwordx4 v129, s[4:7], s70 offen offset:256 lds\n"
"s_lshl_b32 s80, s64, 1\n"
"v_mfma_f32_32x32x16_bf16 v[48:63], a[172:175], a[0:3], 0\n"
"s_waitcnt lgkmcnt(7)\n"
"s_add_u32 m0, s65, 0xcb00\n"
"ds_read_b64_tr_b16 a[204:205], v144 offset:18432\n"
"ds_read_b64_tr_b16 a[206:207], v144 offset:18944\n"
"buffer_load_dwordx4 v130, s[4:7], s71 offen offset:256 lds\n"
"v_lshrrev_b32 v239, 5, v253\n"
"v_mfma_f32_32x32x16_bf16 v[0:15], a[176:179], a[4:7], v[0:15]\n"
"s_add_u32 m0, s65, 0x24000\n"
"ds_read_b128 a[12:15], v136 offset:32768\n"
"ds_read_b64_tr_b16 a[160:161], v141 offset:19456\n"
"ds_read_b64_tr_b16 a[162:163], v141 offset:19968\n"
"buffer_load_dwordx4 v131, s[8:11], s72 offen lds\n"
"v_mfma_f32_32x32x16_bf16 v[16:31], a[180:183], a[4:7], v[16:31]\n"
"ds_read_b64_tr_b16 a[164:165], v142 offset:19456\n"
"ds_read_b64_tr_b16 a[166:167], v142 offset:19968\n"
"buffer_load_dwordx4 v170, s[8:11], s72 offen offset:1024 lds\n"
"v_add_u32 v239, s80, v239\n"
"v_mfma_f32_32x32x16_bf16 v[32:47], a[184:187], a[4:7], v[32:47]\n"
"ds_read_b64_tr_b16 a[168:169], v143 offset:19456\n"
"ds_read_b64_tr_b16 a[170:171], v143 offset:19968\n"
"buffer_load_dwordx4 v171, s[8:11], s72 offen offset:2048 lds\n"
"v_lshlrev_b32 v215, 3, v238\n"
"v_mfma_f32_32x32x16_bf16 v[48:63], a[188:191], a[4:7], v[48:63]\n"
"s_waitcnt lgkmcnt(7)\n"
"ds_read_b64_tr_b16 a[172:173], v144 offset:19456\n"
"ds_read_b64_tr_b16 a[174:175], v144 offset:19968\n"
"buffer_load_dwordx4 v172, s[8:11], s72 offen offset:3072 lds\n"
"s_add_u32 s72, s72, 0x36000\n"
"s_waitcnt vmcnt(14)\n"
"s_waitcnt lgkmcnt(8)\n"
"s_barrier\n"
"v_mfma_f32_32x32x16_bf16 v[0:15], a[192:195], a[8:11], v[0:15]\n"
"s_add_u32 m0, s65, 0xfe80\n"
"ds_read_b128 a[16:19], v133\n"
"ds_read_b64_tr_b16 a[176:177], v141 offset:32768\n"
"ds_read_b64_tr_b16 a[178:179], v141 offset:33280\n"
"buffer_load_dwordx4 v129, s[4:7], s68 offen offset:384 lds\n"
"v_mfma_f32_32x32x16_bf16 v[16:31], a[196:199], a[8:11], v[16:31]\n"
"s_mul_i32 s80, s66, 0x2800\n"
"ds_read_b64_tr_b16 a[180:181], v142 offset:32768\n"
"ds_read_b64_tr_b16 a[182:183], v142 offset:33280\n"
"v_add_u32 v215, 0x14000, v215\n"
"v_lshl_add_u32 v214, v239, 8, v215\n"
"v_lshlrev_b32 v240, 4, v238\n"
"v_cmp_eq_u32 vcc, 7, v239\n"
"v_mov_b32 v241, 0x40000000\n"
"v_mfma_f32_32x32x16_bf16 v[32:47], a[200:203], a[8:11], v[32:47]\n"
"s_add_u32 m0, s65, 0x10280\n"
"ds_read_b64_tr_b16 a[184:185], v143 offset:32768\n"
"ds_read_b64_tr_b16 a[186:187], v143 offset:33280\n"
"buffer_load_dwordx4 v130, s[4:7], s69 offen offset:384 lds\n"
"v_add_u32 v242, s80, v240\n"
"v_mfma_f32_32x32x16_bf16 v[48:63], a[204:207], a[8:11], v[48:63]\n"
"s_waitcnt lgkmcnt(7)\n"
"ds_read_b64_tr_b16 a[188:189], v144 offset:32768\n"
"ds_read_b64_tr_b16 a[190:191], v144 offset:33280\n"
"v_cndmask_b32 v216, v241, v242, vcc\n"
"v_mul_u32_u24 v242, 0x5000, v239\n"
"v_add_u32 v242, v242, v240\n"
"v_lshlrev_b32 v239, 1, v239\n"
"v_cmp_gt_u32 vcc, s66, v239\n"
"v_add_u32 v239, 1, v239\n"
"v_mfma_f32_32x32x16_bf16 v[0:15], a[160:163], a[12:15], v[0:15]\n"
"s_add_u32 m0, s65, 0x10680\n"
"ds_read_b128 a[20:23], v134\n"
"ds_read_b64_tr_b16 a[192:193], v141 offset:33792\n"
"ds_read_b64_tr_b16 a[194:195], v141 offset:34304\n"
"buffer_load_dwordx4 v129, s[4:7], s70 offen offset:384 lds\n"
"v_mfma_f32_32x32x16_bf16 v[16:31], a[164:167], a[12:15], v[16:31]\n"
"ds_read_b64_tr_b16 a[196:197], v142 offset:33792\n"
"ds_read_b64_tr_b16 a[198:199], v142 offset:34304\n"
"v_add_u32 v240, 0x2800, v242\n"
"v_cndmask_b32 v217, v241, v242, vcc\n"
"v_cndmask_b32 v220, 0, -1, vcc\n"
"v_cmp_gt_u32 vcc, s66, v239\n"
"v_mov_b32 v242, 0\n"
"v_mov_b32 v243, 0\n"
"v_mfma_f32_32x32x16_bf16 v[32:47], a[168:171], a[12:15], v[32:47]\n"
"s_add_u32 m0, s65, 0x10a80\n"
"ds_read_b64_tr_b16 a[200:201], v143 offset:33792\n"
"ds_read_b64_tr_b16 a[202:203], v143 offset:34304\n"
"buffer_load_dwordx4 v130, s[4:7], s71 offen offset:384 lds\n"
"v_cndmask_b32 v218, v241, v240, vcc\n"
"v_mfma_f32_32x32x16_bf16 v[48:63], a[172:175], a[12:15], v[48:63]\n"
"s_waitcnt lgkmcnt(7)\n"
"ds_read_b64_tr_b16 a[204:205], v144 offset:33792\n"
"ds_read_b64_tr_b16 a[206:207], v144 offset:34304\n"
"v_cndmask_b32 v222, 0, -1, vcc\n"
"s_lshl_b32 s80, s64, 10\n"
"s_add_u32 s80, s80, 0x17000\n"
"v_lshrrev_b32 v240, 5, v253\n"
"v_lshl_add_u32 v240, v238, 1, v240\n"
"v_lshl_add_u32 v227, v240, 4, s80\n"
"v_mfma_f32_32x32x16_bf16 v[0:15], a[176:179], a[16:19], v[0:15]\n"
"s_add_u32 m0, s65, 0x18000\n"
"ds_read_b128 a[24:27], v135\n"
"ds_read_b64_tr_b16 a[160:161], v141 offset:34816\n"
"ds_read_b64_tr_b16 a[162:163], v141 offset:35328\n"
"buffer_load_dwordx4 v131, s[8:11], s72 offen lds\n"
"v_mfma_f32_32x32x16_bf16 v[16:31], a[180:183], a[16:19], v[16:31]\n"
"ds_read_b64_tr_b16 a[164:165], v142 offset:34816\n"
"ds_read_b64_tr_b16 a[166:167], v142 offset:35328\n"
"v_lshrrev_b32 v240, 4, v253\n"
"v_bfe_u32 v241, v253, 2, 2\n"
"v_lshl_add_u32 v240, v240, 3, v241\n"
"v_and_b32 v241, 1, v253\n"
"v_lshl_add_u32 v240, v240, 1, v241\n"
"v_bfe_u32 v241, v253, 1, 1\n"
"v_mfma_f32_32x32x16_bf16 v[32:47], a[184:187], a[16:19], v[32:47]\n"
"ds_read_b64_tr_b16 a[168:169], v143 offset:34816\n"
"ds_read_b64_tr_b16 a[170:171], v143 offset:35328\n"
"buffer_load_dwordx4 v170, s[8:11], s72 offen offset:1024 lds\n"
"v_lshl_add_u32 v240, v240, 1, v241\n"
"v_mfma_f32_32x32x16_bf16 v[48:63], a[188:191], a[16:19], v[48:63]\n"
"s_waitcnt lgkmcnt(7)\n"
"ds_read_b64_tr_b16 a[172:173], v144 offset:34816\n"
"ds_read_b64_tr_b16 a[174:175], v144 offset:35328\n"
"v_lshl_add_u32 v228, v240, 3, s80\n"
"v_lshlrev_b32 v240, 3, v254\n"
"v_add_u32 v240, 0x14000, v240\n"
"ds_write_b64 v240, v[242:243]\n"
"v_mfma_f32_32x32x16_bf16 v[0:15], a[192:195], a[20:23], v[0:15]\n"
"ds_read_b128 a[28:31], v136\n"
"ds_read_b64_tr_b16 a[176:177], v141 offset:35840\n"
"ds_read_b64_tr_b16 a[178:179], v141 offset:36352\n"
"buffer_load_dwordx4 v171, s[8:11], s72 offen offset:2048 lds\n"
"ds_write_b64 v240, v[242:243] offset:6144\n"
"v_mfma_f32_32x32x16_bf16 v[16:31], a[196:199], a[20:23], v[16:31]\n"
"s_cmp_lt_u32 s67, 15\n"
"ds_read_b64_tr_b16 a[180:181], v142 offset:35840\n"
"ds_read_b64_tr_b16 a[182:183], v142 offset:36352\n"
"v_and_b32 v219, s76, v220\n"
"v_and_b32 v220, s78, v220\n"
"v_and_b32 v221, s76, v222\n"
"v_and_b32 v222, s78, v222\n"
"s_cselect_b32 s80, -1, 0\n"
"v_mfma_f32_32x32x16_bf16 v[32:47], a[200:203], a[20:23], v[32:47]\n"
"ds_read_b64_tr_b16 a[184:185], v143 offset:35840\n"
"ds_read_b64_tr_b16 a[186:187], v143 offset:36352\n"
"buffer_load_dwordx4 v172, s[8:11], s72 offen offset:3072 lds\n"
"s_add_u32 s72, s72, 0x36000\n"
"v_mfma_f32_32x32x16_bf16 v[48:63], a[204:207], a[20:23], v[48:63]\n"
"s_waitcnt lgkmcnt(9)\n"
"ds_read_b64_tr_b16 a[188:189], v144 offset:35840\n"
"ds_read_b64_tr_b16 a[190:191], v144 offset:36352\n"
"v_and_b32 v223, s80, v219\n"
"v_and_b32 v224, s80, v220\n"
"v_and_b32 v225, s80, v221\n"
"v_and_b32 v226, s80, v222\n"
"s_mov_b32 s60, s24\n"
"s_mov_b32 s61, s25\n"
"s_waitcnt vmcnt(8)\n"
"s_waitcnt lgkmcnt(6)\n"
"s_barrier\n"
"v_mfma_f32_32x32x16_bf16 v[0:15], a[160:163], a[24:27], v[0:15]\n"
"s_add_u32 m0, s65, 0x7e00\n"
"ds_read_b128 a[32:35], v133 offset:16384\n"
"ds_read_b64_tr_b16 a[192:193], v141 offset:49152\n"
"ds_read_b64_tr_b16 a[194:195], v141 offset:49664\n"
"buffer_load_dwordx4 v129, s[4:7], s68 offen offset:512 lds\n"
"v_mfma_f32_32x32x16_bf16 v[16:31], a[164:167], a[24:27], v[16:31]\n"
"s_and_b32 s62, s26, s80\n"
"ds_read_b64_tr_b16 a[196:197], v142 offset:49152\n"
"ds_read_b64_tr_b16 a[198:199], v142 offset:49664\n"
"s_mov_b32 s63, s27\n"
"v_lshlrev_b32 v120, 16, v120\n"
"v_lshlrev_b32 v121, 16, v121\n"
"v_lshlrev_b32 v122, 16, v122\n"
"v_mul_f32 v122, 0x3fb8aa3b, v122\n"
"v_mfma_f32_32x32x16_bf16 v[32:47], a[168:171], a[24:27], v[32:47]\n"
"s_add_u32 m0, s65, 0x8200\n"
"ds_read_b64_tr_b16 a[200:201], v143 offset:49152\n"
"ds_read_b64_tr_b16 a[202:203], v143 offset:49664\n"
"buffer_load_dwordx4 v130, s[4:7], s69 offen offset:512 lds\n"
"v_mfma_f32_32x32x16_bf16 v[48:63], a[172:175], a[24:27], v[48:63]\n"
"s_waitcnt lgkmcnt(7)\n"
"ds_read_b64_tr_b16 a[204:205], v144 offset:49152\n"
"ds_read_b64_tr_b16 a[206:207], v144 offset:49664\n"
"v_exp_f32 v122, v122\n"
"s_nop 0\n"
"v_cvt_pk_bf16_f32 v122, v122, v122\n"
"v_and_b32 v122, 0xffff0000, v122\n"
"v_mul_f32 v238, v122, v120\n"
"v_mul_f32 v239, v122, v121\n"
"v_mfma_f32_32x32x16_bf16 v[0:15], a[176:179], a[28:31], v[0:15]\n"
"s_add_u32 m0, s65, 0x8600\n"
"ds_read_b128 a[36:39], v134 offset:16384\n"
"ds_read_b64_tr_b16 a[160:161], v141 offset:50176\n"
"ds_read_b64_tr_b16 a[162:163], v141 offset:50688\n"
"buffer_load_dwordx4 v129, s[4:7], s70 offen offset:512 lds\n"
"v_mfma_f32_32x32x16_bf16 v[16:31], a[180:183], a[28:31], v[16:31]\n"
"ds_read_b64_tr_b16 a[164:165], v142 offset:50176\n"
"ds_read_b64_tr_b16 a[166:167], v142 offset:50688\n"
"v_mul_f32 v238, 0x3fb8aa3b, v238\n"
"v_mul_f32 v239, 0x3e22f983, v239\n"
"v_mul_f32 v240, 1.0, v238\n"
"v_mul_f32 v241, 1.0, v239\n"
"v_exp_f32 v240, v240\n"
"v_mfma_f32_32x32x16_bf16 v[32:47], a[184:187], a[28:31], v[32:47]\n"
"s_add_u32 m0, s65, 0x8a00\n"
"ds_read_b64_tr_b16 a[168:169], v143 offset:50176\n"
"ds_read_b64_tr_b16 a[170:171], v143 offset:50688\n"
"buffer_load_dwordx4 v130, s[4:7], s71 offen offset:512 lds\n"
"v_fract_f32 v241, v241\n"
"v_mfma_f32_32x32x16_bf16 v[48:63], a[188:191], a[28:31], v[48:63]\n"
"s_waitcnt lgkmcnt(7)\n"
"ds_read_b64_tr_b16 a[172:173], v144 offset:50176\n"
"ds_read_b64_tr_b16 a[174:175], v144 offset:50688\n"
"v_cos_f32 v190, v241\n"
"v_sin_f32 v191, v241\n"
"v_mul_f32 v190, v190, v240\n"
"v_mul_f32 v191, v191, v240\n"
"v_mfma_f32_32x32x16_bf16 v[0:15], a[192:195], a[32:35], v[0:15]\n"
"s_add_u32 m0, s65, 0x1c000\n"
"ds_read_b128 a[40:43], v135 offset:16384\n"
"ds_read_b64_tr_b16 a[176:177], v141 offset:51200\n"
"ds_read_b64_tr_b16 a[178:179], v141 offset:51712\n"
"buffer_load_dwordx4 v131, s[8:11], s72 offen lds\n"
"v_mfma_f32_32x32x16_bf16 v[16:31], a[196:199], a[32:35], v[16:31]\n"
"ds_read_b64_tr_b16 a[180:181], v142 offset:51200\n"
"ds_read_b64_tr_b16 a[182:183], v142 offset:51712\n"
"v_mul_f32 v240, 0x41800000, v238\n"
"v_mul_f32 v241, 0x41800000, v239\n"
"v_exp_f32 v240, v240\n"
"v_fract_f32 v241, v241\n"
"v_mfma_f32_32x32x16_bf16 v[32:47], a[200:203], a[32:35], v[32:47]\n"
"ds_read_b64_tr_b16 a[184:185], v143 offset:51200\n"
"ds_read_b64_tr_b16 a[186:187], v143 offset:51712\n"
"buffer_load_dwordx4 v170, s[8:11], s72 offen offset:1024 lds\n"
"v_mfma_f32_32x32x16_bf16 v[48:63], a[204:207], a[32:35], v[48:63]\n"
"s_waitcnt lgkmcnt(7)\n"
"ds_read_b64_tr_b16 a[188:189], v144 offset:51200\n"
"ds_read_b64_tr_b16 a[190:191], v144 offset:51712\n"
"v_cos_f32 v192, v241\n"
"v_sin_f32 v193, v241\n"
"v_mul_f32 v192, v192, v240\n"
"v_mul_f32 v193, v193, v240\n"
"v_mfma_f32_32x32x16_bf16 v[0:15], a[160:163], a[36:39], v[0:15]\n"
"ds_read_b128 a[44:47], v136 offset:16384\n"
"ds_read_b64_tr_b16 a[192:193], v141 offset:52224\n"
"ds_read_b64_tr_b16 a[194:195], v141 offset:52736\n"
"buffer_load_dwordx4 v171, s[8:11], s72 offen offset:2048 lds\n"
"v_mfma_f32_32x32x16_bf16 v[16:31], a[164:167], a[36:39], v[16:31]\n"
"s_lshl_b32 s80, s64, 1\n"
"ds_read_b64_tr_b16 a[196:197], v142 offset:52224\n"
"ds_read_b64_tr_b16 a[198:199], v142 offset:52736\n"
"v_lshrrev_b32 v243, 5, v253\n"
"v_add_u32 v243, s80, v243\n"
"v_lshlrev_b32 v242, 4, v243\n"
"v_cvt_f32_u32 v242, v242\n"
"v_mul_f32 v240, v242, v238\n"
"v_mfma_f32_32x32x16_bf16 v[32:47], a[168:171], a[36:39], v[32:47]\n"
"ds_read_b64_tr_b16 a[200:201], v143 offset:52224\n"
"ds_read_b64_tr_b16 a[202:203], v143 offset:52736\n"
"buffer_load_dwordx4 v172, s[8:11], s72 offen offset:3072 lds\n"
"s_add_u32 s72, s72, 0x36000\n"
"v_mfma_f32_32x32x16_bf16 v[48:63], a[172:175], a[36:39], v[48:63]\n"
"s_waitcnt lgkmcnt(7)\n"
"ds_read_b64_tr_b16 a[204:205], v144 offset:52224\n"
"ds_read_b64_tr_b16 a[206:207], v144 offset:52736\n"
"v_mul_f32 v241, v242, v239\n"
"v_exp_f32 v240, v240\n"
"v_fract_f32 v241, v241\n"
"v_cos_f32 v194, v241\n"
"s_waitcnt vmcnt(8)\n"
"s_waitcnt lgkmcnt(8)\n"
"s_barrier\n"
"v_mfma_f32_32x32x16_bf16 v[0:15], a[176:179], a[40:43], v[0:15]\n"
"s_add_u32 m0, s65, 0xbd80\n"
"ds_read_b128 a[48:51], v133 offset:32768\n"
"ds_read_b64_tr_b16 a[160:161], v141\n"
"ds_read_b64_tr_b16 a[162:163], v141 offset:512\n"
"buffer_load_dwordx4 v129, s[4:7], s68 offen offset:640 lds\n"
"v_mfma_f32_32x32x16_bf16 v[16:31], a[180:183], a[40:43], v[16:31]\n"
"s_sub_u32 s80, s66, 1\n"
"ds_read_b64_tr_b16 a[164:165], v142\n"
"ds_read_b64_tr_b16 a[166:167], v142 offset:512\n"
"v_sin_f32 v195, v241\n"
"v_mul_f32 v194, v194, v240\n"
"v_mul_f32 v195, v195, v240\n"
"v_lshlrev_b32 v242, 1, v243\n"
"v_mfma_f32_32x32x16_bf16 v[32:47], a[184:187], a[40:43], v[32:47]\n"
"s_add_u32 m0, s65, 0xc180\n"
"ds_read_b64_tr_b16 a[168:169], v143\n"
"ds_read_b64_tr_b16 a[170:171], v143 offset:512\n"
"buffer_load_dwordx4 v130, s[4:7], s69 offen offset:640 lds\n"
"v_sub_u32 v242, s80, v242\n"
"v_mfma_f32_32x32x16_bf16 v[48:63], a[188:191], a[40:43], v[48:63]\n"
"s_waitcnt lgkmcnt(7)\n"
"ds_read_b64_tr_b16 a[172:173], v144\n"
"ds_read_b64_tr_b16 a[174:175], v144 offset:512\n"
"v_max_i32 v242, 0, v242\n"
"v_lshlrev_b32 v242, 7, v242\n"
"v_cvt_f32_u32 v242, v242\n"
"v_mul_f32 v240, v242, v238\n"
"v_mul_f32 v241, v242, v239\n"
"v_mfma_f32_32x32x16_bf16 v[0:15], a[192:195], a[44:47], v[0:15]\n"
"s_add_u32 m0, s65, 0xc580\n"
"ds_read_b128 a[52:55], v134 offset:32768\n"
"ds_read_b64_tr_b16 a[176:177], v141 offset:1024\n"
"ds_read_b64_tr_b16 a[178:179], v141 offset:1536\n"
"buffer_load_dwordx4 v129, s[4:7], s70 offen offset:640 lds\n"
"v_mfma_f32_32x32x16_bf16 v[16:31], a[196:199], a[44:47], v[16:31]\n"
"ds_read_b64_tr_b16 a[180:181], v142 offset:1024\n"
"ds_read_b64_tr_b16 a[182:183], v142 offset:1536\n"
"v_exp_f32 v240, v240\n"
"v_fract_f32 v241, v241\n"
"v_cos_f32 v196, v241\n"
"v_mfma_f32_32x32x16_bf16 v[32:47], a[200:203], a[44:47], v[32:47]\n"
"s_add_u32 m0, s65, 0xc980\n"
"ds_read_b64_tr_b16 a[184:185], v143 offset:1024\n"
"ds_read_b64_tr_b16 a[186:187], v143 offset:1536\n"
"buffer_load_dwordx4 v130, s[4:7], s71 offen offset:640 lds\n"
"v_mfma_f32_32x32x16_bf16 v[48:63], a[204:207], a[44:47], v[48:63]\n"
"s_waitcnt lgkmcnt(7)\n"
"ds_read_b64_tr_b16 a[188:189], v144 offset:1024\n"
"ds_read_b64_tr_b16 a[190:191], v144 offset:1536\n"
"v_sin_f32 v197, v241\n"
"v_mul_f32 v196, v196, v240\n"
"v_mul_f32 v197, v197, v240\n"
"s_sub_u32 s80, s66, 2\n"
"v_lshlrev_b32 v242, 1, v243\n"
"v_mfma_f32_32x32x16_bf16 v[0:15], a[160:163], a[48:51], v[0:15]\n"
"s_add_u32 m0, s65, 0x20000\n"
"ds_read_b128 a[56:59], v135 offset:32768\n"
"ds_read_b64_tr_b16 a[192:193], v141 offset:2048\n"
"ds_read_b64_tr_b16 a[194:195], v141 offset:2560\n"
"buffer_load_dwordx4 v131, s[8:11], s72 offen lds\n"
"v_mfma_f32_32x32x16_bf16 v[16:31], a[164:167], a[48:51], v[16:31]\n"
"ds_read_b64_tr_b16 a[196:197], v142 offset:2048\n"
"ds_read_b64_tr_b16 a[198:199], v142 offset:2560\n"
"v_sub_u32 v242, s80, v242\n"
"v_max_i32 v242, 0, v242\n"
"v_lshlrev_b32 v242, 7, v242\n"
"v_cvt_f32_u32 v242, v242\n"
"v_mul_f32 v240, v242, v238\n"
"v_mul_f32 v241, v242, v239\n"
"v_mfma_f32_32x32x16_bf16 v[32:47], a[168:171], a[48:51], v[32:47]\n"
"ds_read_b64_tr_b16 a[200:201], v143 offset:2048\n"
"ds_read_b64_tr_b16 a[202:203], v143 offset:2560\n"
"buffer_load_dwordx4 v170, s[8:11], s72 offen offset:1024 lds\n"
"v_mfma_f32_32x32x16_bf16 v[48:63], a[172:175], a[48:51], v[48:63]\n"
"s_waitcnt lgkmcnt(7)\n"
"ds_read_b64_tr_b16 a[204:205], v144 offset:2048\n"
"ds_read_b64_tr_b16 a[206:207], v144 offset:2560\n"
"v_exp_f32 v240, v240\n"
"v_fract_f32 v241, v241\n"
"v_cos_f32 v198, v241\n"
"v_mfma_f32_32x32x16_bf16 v[0:15], a[176:179], a[52:55], v[0:15]\n"
"ds_read_b128 a[60:63], v136 offset:32768\n"
"ds_read_b64_tr_b16 a[160:161], v141 offset:3072\n"
"ds_read_b64_tr_b16 a[162:163], v141 offset:3584\n"
"buffer_load_dwordx4 v171, s[8:11], s72 offen offset:2048 lds\n"
"v_mfma_f32_32x32x16_bf16 v[16:31], a[180:183], a[52:55], v[16:31]\n"
"ds_read_b64_tr_b16 a[164:165], v142 offset:3072\n"
"ds_read_b64_tr_b16 a[166:167], v142 offset:3584\n"
"v_sin_f32 v199, v241\n"
"v_mul_f32 v198, v198, v240\n"
"v_mul_f32 v199, v199, v240\n"
"v_add_f32 v240, -1.0, v190\n"
"v_mul_f32 v241, v120, v120\n"
"v_mfma_f32_32x32x16_bf16 v[32:47], a[184:187], a[52:55], v[32:47]\n"
"ds_read_b64_tr_b16 a[168:169], v143 offset:3072\n"
"ds_read_b64_tr_b16 a[170:171], v143 offset:3584\n"
"buffer_load_dwordx4 v172, s[8:11], s72 offen offset:3072 lds\n"
"s_add_u32 s72, s72, 0x36000\n"
"v_mfma_f32_32x32x16_bf16 v[48:63], a[188:191], a[52:55], v[48:63]\n"
"s_waitcnt lgkmcnt(7)\n"
"ds_read_b64_tr_b16 a[172:173], v144 offset:3072\n"
"ds_read_b64_tr_b16 a[174:175], v144 offset:3584\n"
"v_fmac_f32 v241, v121, v121\n"
"v_rcp_f32 v241, v241\n"
"v_mul_f32 v200, v240, v120\n"
"v_fmac_f32 v200, v191, v121\n"
"v_mul_f32 v201, v191, v120\n"
"s_waitcnt vmcnt(8)\n"
"s_waitcnt lgkmcnt(8)\n"
"s_barrier\n"
"v_mfma_f32_32x32x16_bf16 v[0:15], a[192:195], a[56:59], v[0:15]\n"
"s_add_u32 m0, s65, 0xfd00\n"
"ds_read_b128 a[64:67], v133\n"
"ds_read_b64_tr_b16 a[176:177], v141 offset:16384\n"
"ds_read_b64_tr_b16 a[178:179], v141 offset:16896\n"
"buffer_load_dwordx4 v129, s[4:7], s68 offen offset:768 lds\n"
"v_mfma_f32_32x32x16_bf16 v[16:31], a[196:199], a[56:59], v[16:31]\n"
"ds_read_b64_tr_b16 a[180:181], v142 offset:16384\n"
"ds_read_b64_tr_b16 a[182:183], v142 offset:16896\n"
"v_add_f32 v241, v241, v241\n"
"v_fma_f32 v201, -v240, v121, v201\n"
"v_mul_f32 v200, v200, v241\n"
"v_mul_f32 v201, v201, v241\n"
"v_lshlrev_b32 v124, 16, v124\n"
"v_lshlrev_b32 v125, 16, v125\n"
"v_mfma_f32_32x32x16_bf16 v[32:47], a[200:203], a[56:59], v[32:47]\n"
"s_add_u32 m0, s65, 0x10100\n"
"ds_read_b64_tr_b16 a[184:185], v143 offset:16384\n"
"ds_read_b64_tr_b16 a[186:187], v143 offset:16896\n"
"buffer_load_dwordx4 v130, s[4:7], s69 offen offset:768 lds\n"
"v_lshlrev_b32 v126, 16, v126\n"
"v_mfma_f32_32x32x16_bf16 v[48:63], a[204:207], a[56:59], v[48:63]\n"
"s_waitcnt lgkmcnt(7)\n"
"ds_read_b64_tr_b16 a[188:189], v144 offset:16384\n"
"ds_read_b64_tr_b16 a[190:191], v144 offset:16896\n"
"v_mul_f32 v126, 0x3fb8aa3b, v126\n"
"v_exp_f32 v126, v126\n"
"s_nop 0\n"
"v_cvt_pk_bf16_f32 v126, v126, v126\n"
"v_and_b32 v126, 0xffff0000, v126\n"
"v_mul_f32 v238, v126, v124\n"
"v_mfma_f32_32x32x16_bf16 v[0:15], a[160:163], a[60:63], v[0:15]\n"
"s_add_u32 m0, s65, 0x10500\n"
"ds_read_b128 a[68:71], v134\n"
"ds_read_b64_tr_b16 a[192:193], v141 offset:17408\n"
"ds_read_b64_tr_b16 a[194:195], v141 offset:17920\n"
"buffer_load_dwordx4 v129, s[4:7], s70 offen offset:768 lds\n"
"v_mfma_f32_32x32x16_bf16 v[16:31], a[164:167], a[60:63], v[16:31]\n"
"ds_read_b64_tr_b16 a[196:197], v142 offset:17408\n"
"ds_read_b64_tr_b16 a[198:199], v142 offset:17920\n"
"v_mul_f32 v239, v126, v125\n"
"v_mul_f32 v238, 0x3fb8aa3b, v238\n"
"v_mul_f32 v239, 0x3e22f983, v239\n"
"v_mul_f32 v240, 1.0, v238\n"
"v_mul_f32 v241, 1.0, v239\n"
"v_mfma_f32_32x32x16_bf16 v[32:47], a[168:171], a[60:63], v[32:47]\n"
"s_add_u32 m0, s65, 0x10900\n"
"ds_read_b64_tr_b16 a[200:201], v143 offset:17408\n"
"ds_read_b64_tr_b16 a[202:203], v143 offset:17920\n"
"buffer_load_dwordx4 v130, s[4:7], s71 offen offset:768 lds\n"
"v_mfma_f32_32x32x16_bf16 v[48:63], a[172:175], a[60:63], v[48:63]\n"
"s_waitcnt lgkmcnt(7)\n"
"ds_read_b64_tr_b16 a[204:205], v144 offset:17408\n"
"ds_read_b64_tr_b16 a[206:207], v144 offset:17920\n"
"v_exp_f32 v240, v240\n"
"v_fract_f32 v241, v241\n"
"v_cos_f32 v202, v241\n"
"v_mfma_f32_32x32x16_bf16 v[0:15], a[176:179], a[64:67], v[0:15]\n"
"s_add_u32 m0, s65, 0x24000\n"
"ds_read_b128 a[72:75], v135\n"
"ds_read_b64_tr_b16 a[160:161], v141 offset:18432\n"
"ds_read_b64_tr_b16 a[162:163], v141 offset:18944\n"
"buffer_load_dwordx4 v131, s[8:11], s72 offen lds\n"
"v_mfma_f32_32x32x16_bf16 v[16:31], a[180:183], a[64:67], v[16:31]\n"
"ds_read_b64_tr_b16 a[164:165], v142 offset:18432\n"
"ds_read_b64_tr_b16 a[166:167], v142 offset:18944\n"
"v_sin_f32 v203, v241\n"
"v_mul_f32 v202, v202, v240\n"
"v_mul_f32 v203, v203, v240\n"
"v_mul_f32 v240, 0x41800000, v238\n"
"v_mul_f32 v241, 0x41800000, v239\n"
"v_mfma_f32_32x32x16_bf16 v[32:47], a[184:187], a[64:67], v[32:47]\n"
"ds_read_b64_tr_b16 a[168:169], v143 offset:18432\n"
"ds_read_b64_tr_b16 a[170:171], v143 offset:18944\n"
"buffer_load_dwordx4 v170, s[8:11], s72 offen offset:1024 lds\n"
"v_mfma_f32_32x32x16_bf16 v[48:63], a[188:191], a[64:67], v[48:63]\n"
"s_waitcnt lgkmcnt(7)\n"
"ds_read_b64_tr_b16 a[172:173], v144 offset:18432\n"
"ds_read_b64_tr_b16 a[174:175], v144 offset:18944\n"
"v_exp_f32 v240, v240\n"
"v_fract_f32 v241, v241\n"
"v_cos_f32 v204, v241\n"
"v_mfma_f32_32x32x16_bf16 v[0:15], a[192:195], a[68:71], v[0:15]\n"
"ds_read_b128 a[76:79], v136\n"
"ds_read_b64_tr_b16 a[176:177], v141 offset:19456\n"
"ds_read_b64_tr_b16 a[178:179], v141 offset:19968\n"
"buffer_load_dwordx4 v171, s[8:11], s72 offen offset:2048 lds\n"
"v_mfma_f32_32x32x16_bf16 v[16:31], a[196:199], a[68:71], v[16:31]\n"
"s_lshl_b32 s80, s64, 1\n"
"ds_read_b64_tr_b16 a[180:181], v142 offset:19456\n"
"ds_read_b64_tr_b16 a[182:183], v142 offset:19968\n"
"v_sin_f32 v205, v241\n"
"v_mul_f32 v204, v204, v240\n"
"v_mul_f32 v205, v205, v240\n"
"v_lshrrev_b32 v243, 5, v253\n"
"v_mfma_f32_32x32x16_bf16 v[32:47], a[200:203], a[68:71], v[32:47]\n"
"ds_read_b64_tr_b16 a[184:185], v143 offset:19456\n"
"ds_read_b64_tr_b16 a[186:187], v143 offset:19968\n"
"buffer_load_dwordx4 v172, s[8:11], s72 offen offset:3072 lds\n"
"s_add_u32 s72, s72, 0x36000\n"
"v_mfma_f32_32x32x16_bf16 v[48:63], a[204:207], a[68:71], v[48:63]\n"
"s_waitcnt lgkmcnt(7)\n"
"ds_read_b64_tr_b16 a[188:189], v144 offset:19456\n"
"ds_read_b64_tr_b16 a[190:191], v144 offset:19968\n"
"v_add_u32 v243, s80, v243\n"
"v_lshlrev_b32 v242, 4, v243\n"
"v_cvt_f32_u32 v242, v242\n"
"v_mul_f32 v240, v242, v238\n"
"v_mul_f32 v241, v242, v239\n"
"s_waitcnt vmcnt(8)\n"
"s_waitcnt lgkmcnt(8)\n"
"s_barrier\n"
"v_mfma_f32_32x32x16_bf16 v[0:15], a[160:163], a[72:75], v[0:15]\n"
"s_add_u32 m0, s65, 0x7c80\n"
"ds_read_b128 a[80:83], v133 offset:16384\n"
"ds_read_b64_tr_b16 a[192:193], v141 offset:32768\n"
"ds_read_b64_tr_b16 a[194:195], v141 offset:33280\n"
"buffer_load_dwordx4 v129, s[4:7], s68 offen offset:896 lds\n"
"v_mfma_f32_32x32x16_bf16 v[16:31], a[164:167], a[72:75], v[16:31]\n"
"ds_read_b64_tr_b16 a[196:197], v142 offset:32768\n"
"ds_read_b64_tr_b16 a[198:199], v142 offset:33280\n"
"v_exp_f32 v240, v240\n"
"v_fract_f32 v241, v241\n"
"v_cos_f32 v206, v241\n"
"v_mfma_f32_32x32x16_bf16 v[32:47], a[168:171], a[72:75], v[32:47]\n"
"s_add_u32 m0, s65, 0x8080\n"
"ds_read_b64_tr_b16 a[200:201], v143 offset:32768\n"
"ds_read_b64_tr_b16 a[202:203], v143 offset:33280\n"
"buffer_load_dwordx4 v130, s[4:7], s69 offen offset:896 lds\n"
"v_mfma_f32_32x32x16_bf16 v[48:63], a[172:175], a[72:75], v[48:63]\n"
"s_waitcnt lgkmcnt(7)\n"
"ds_read_b64_tr_b16 a[204:205], v144 offset:32768\n"
"ds_read_b64_tr_b16 a[206:207], v144 offset:33280\n"
"v_sin_f32 v207, v241\n"
"v_mul_f32 v206, v206, v240\n"
"v_mul_f32 v207, v207, v240\n"
"s_sub_u32 s80, s66, 1\n"
"v_lshlrev_b32 v242, 1, v243\n"
"v_mfma_f32_32x32x16_bf16 v[0:15], a[176:179], a[76:79], v[0:15]\n"
"s_add_u32 m0, s65, 0x8480\n"
"ds_read_b128 a[84:87], v134 offset:16384\n"
"ds_read_b64_tr_b16 a[160:161], v141 offset:33792\n"
"ds_read_b64_tr_b16 a[162:163], v141 offset:34304\n"
"buffer_load_dwordx4 v129, s[4:7], s70 offen offset:896 lds\n"
"v_mfma_f32_32x32x16_bf16 v[16:31], a[180:183], a[76:79], v[16:31]\n"
"ds_read_b64_tr_b16 a[164:165], v142 offset:33792\n"
"ds_read_b64_tr_b16 a[166:167], v142 offset:34304\n"
"v_sub_u32 v242, s80, v242\n"
"v_max_i32 v242, 0, v242\n"
"v_lshlrev_b32 v242, 7, v242\n"
"v_cvt_f32_u32 v242, v242\n"
"v_mul_f32 v240, v242, v238\n"
"v_mul_f32 v241, v242, v239\n"
"v_mfma_f32_32x32x16_bf16 v[32:47], a[184:187], a[76:79], v[32:47]\n"
"s_add_u32 m0, s65, 0x8880\n"
"ds_read_b64_tr_b16 a[168:169], v143 offset:33792\n"
"ds_read_b64_tr_b16 a[170:171], v143 offset:34304\n"
"buffer_load_dwordx4 v130, s[4:7], s71 offen offset:896 lds\n"
"v_mfma_f32_32x32x16_bf16 v[48:63], a[188:191], a[76:79], v[48:63]\n"
"s_waitcnt lgkmcnt(7)\n"
"ds_read_b64_tr_b16 a[172:173], v144 offset:33792\n"
"ds_read_b64_tr_b16 a[174:175], v144 offset:34304\n"
"v_exp_f32 v240, v240\n"
"v_fract_f32 v241, v241\n"
"v_cos_f32 v208, v241\n"
"v_mfma_f32_32x32x16_bf16 v[0:15], a[192:195], a[80:83], v[0:15]\n"
"s_add_u32 m0, s65, 0x18000\n"
"ds_read_b128 a[88:91], v135 offset:16384\n"
"ds_read_b64_tr_b16 a[176:177], v141 offset:34816\n"
"ds_read_b64_tr_b16 a[178:179], v141 offset:35328\n"
"buffer_load_dwordx4 v131, s[8:11], s72 offen lds\n"
"v_mfma_f32_32x32x16_bf16 v[16:31], a[196:199], a[80:83], v[16:31]\n"
"s_sub_u32 s80, s66, 2\n"
"ds_read_b64_tr_b16 a[180:181], v142 offset:34816\n"
"ds_read_b64_tr_b16 a[182:183], v142 offset:35328\n"
"v_sin_f32 v209, v241\n"
"v_mul_f32 v208, v208, v240\n"
"v_mul_f32 v209, v209, v240\n"
"v_lshlrev_b32 v242, 1, v243\n"
"v_mfma_f32_32x32x16_bf16 v[32:47], a[200:203], a[80:83], v[32:47]\n"
"ds_read_b64_tr_b16 a[184:185], v143 offset:34816\n"
"ds_read_b64_tr_b16 a[186:187], v143 offset:35328\n"
"buffer_load_dwordx4 v170, s[8:11], s72 offen offset:1024 lds\n"
"v_sub_u32 v242, s80, v242\n"
"v_mfma_f32_32x32x16_bf16 v[48:63], a[204:207], a[80:83], v[48:63]\n"
"s_waitcnt lgkmcnt(7)\n"
"ds_read_b64_tr_b16 a[188:189], v144 offset:34816\n"
"ds_read_b64_tr_b16 a[190:191], v144 offset:35328\n"
"v_max_i32 v242, 0, v242\n"
"v_lshlrev_b32 v242, 7, v242\n"
"v_cvt_f32_u32 v242, v242\n"
"v_mul_f32 v240, v242, v238\n"
"v_mul_f32 v241, v242, v239\n"
"v_mfma_f32_32x32x16_bf16 v[0:15], a[160:163], a[84:87], v[0:15]\n"
"ds_read_b128 a[92:95], v136 offset:16384\n"
"ds_read_b64_tr_b16 a[192:193], v141 offset:35840\n"
"ds_read_b64_tr_b16 a[194:195], v141 offset:36352\n"
"buffer_load_dwordx4 v171, s[8:11], s72 offen offset:2048 lds\n"
"v_mfma_f32_32x32x16_bf16 v[16:31], a[164:167], a[84:87], v[16:31]\n"
"ds_read_b64_tr_b16 a[196:197], v142 offset:35840\n"
"ds_read_b64_tr_b16 a[198:199], v142 offset:36352\n"
"v_exp_f32 v240, v240\n"
"v_fract_f32 v241, v241\n"
"v_cos_f32 v210, v241\n"
"v_mfma_f32_32x32x16_bf16 v[32:47], a[168:171], a[84:87], v[32:47]\n"
"ds_read_b64_tr_b16 a[200:201], v143 offset:35840\n"
"ds_read_b64_tr_b16 a[202:203], v143 offset:36352\n"
"buffer_load_dwordx4 v172, s[8:11], s72 offen offset:3072 lds\n"
"s_add_u32 s72, s72, 0x36000\n"
"v_mfma_f32_32x32x16_bf16 v[48:63], a[172:175], a[84:87], v[48:63]\n"
"s_waitcnt lgkmcnt(7)\n"
"ds_read_b64_tr_b16 a[204:205], v144 offset:35840\n"
"ds_read_b64_tr_b16 a[206:207], v144 offset:36352\n"
"v_sin_f32 v211, v241\n"
"v_mul_f32 v210, v210, v240\n"
"v_mul_f32 v211, v211, v240\n"
"v_add_f32 v240, -1.0, v202\n"
"v_mul_f32 v241, v124, v124\n"
"s_waitcnt vmcnt(8)\n"
"s_waitcnt lgkmcnt(8)\n"
"s_barrier\n"
"v_mfma_f32_32x32x16_bf16 v[0:15], a[176:179], a[88:91], v[0:15]\n"
"s_add_u32 m0, s65, 0xbc00\n"
"ds_read_b128 a[96:99], v133 offset:32768\n"
"ds_read_b64_tr_b16 a[160:161], v141 offset:49152\n"
"ds_read_b64_tr_b16 a[162:163], v141 offset:49664\n"
"buffer_load_dwordx4 v129, s[4:7], s68 offen offset:1024 lds\n"
"v_mfma_f32_32x32x16_bf16 v[16:31], a[180:183], a[88:91], v[16:31]\n"
"ds_read_b64_tr_b16 a[164:165], v142 offset:49152\n"
"ds_read_b64_tr_b16 a[166:167], v142 offset:49664\n"
"v_fmac_f32 v241, v125, v125\n"
"v_rcp_f32 v241, v241\n"
"v_mul_f32 v212, v240, v124\n"
"v_fmac_f32 v212, v203, v125\n"
"v_mul_f32 v213, v203, v124\n"
"v_mfma_f32_32x32x16_bf16 v[32:47], a[184:187], a[88:91], v[32:47]\n"
"s_add_u32 m0, s65, 0xc000\n"
"ds_read_b64_tr_b16 a[168:169], v143 offset:49152\n"
"ds_read_b64_tr_b16 a[170:171], v143 offset:49664\n"
"buffer_load_dwordx4 v130, s[4:7], s69 offen offset:1024 lds\n"
"v_add_f32 v241, v241, v241\n"
"v_mfma_f32_32x32x16_bf16 v[48:63], a[188:191], a[88:91], v[48:63]\n"
"s_waitcnt lgkmcnt(7)\n"
"ds_read_b64_tr_b16 a[172:173], v144 offset:49152\n"
"ds_read_b64_tr_b16 a[174:175], v144 offset:49664\n"
"v_fma_f32 v213, -v240, v125, v213\n"
"v_mul_f32 v212, v212, v241\n"
"v_mul_f32 v213, v213, v241\n"
"v_cmp_gt_u32 vcc, 32, v253\n"
"v_mov_b32 v150, 0\n"
"v_mov_b32 v151, 0\n"
"v_mfma_f32_32x32x16_bf16 v[0:15], a[192:195], a[92:95], v[0:15]\n"
"s_add_u32 m0, s65, 0xc400\n"
"ds_read_b128 a[100:103], v134 offset:32768\n"
"ds_read_b64_tr_b16 a[176:177], v141 offset:50176\n"
"ds_read_b64_tr_b16 a[178:179], v141 offset:50688\n"
"buffer_load_dwordx4 v129, s[4:7], s70 offen offset:1024 lds\n"
"v_mfma_f32_32x32x16_bf16 v[16:31], a[196:199], a[92:95], v[16:31]\n"
"ds_read_b64_tr_b16 a[180:181], v142 offset:50176\n"
"ds_read_b64_tr_b16 a[182:183], v142 offset:50688\n"
"v_cndmask_b32 v246, 0, -1.0, vcc\n"
"v_mov_b32 v168, 0\n"
"v_mov_b32 v169, 0\n"
"v_bfe_u32 v240, v253, 4, 1\n"
"v_mul_u32_u24 v240, 0x17800, v240\n"
"v_lshrrev_b32 v241, 5, v253\n"
"v_mfma_f32_32x32x16_bf16 v[32:47], a[200:203], a[92:95], v[32:47]\n"
"s_add_u32 m0, s65, 0xc800\n"
"ds_read_b64_tr_b16 a[184:185], v143 offset:50176\n"
"ds_read_b64_tr_b16 a[186:187], v143 offset:50688\n"
"buffer_load_dwordx4 v130, s[4:7], s71 offen offset:1024 lds\n"
"v_lshl_add_u32 v240, v241, 8, v240\n"
"v_mfma_f32_32x32x16_bf16 v[48:63], a[204:207], a[92:95], v[48:63]\n"
"s_waitcnt lgkmcnt(7)\n"
"ds_read_b64_tr_b16 a[188:189], v144 offset:50176\n"
"ds_read_b64_tr_b16 a[190:191], v144 offset:50688\n"
"v_and_b32 v241, 15, v253\n"
"v_lshl_add_u32 v145, v241, 4, v240\n"
"s_lshl_b32 s80, s66, 2\n"
"s_add_u32 s80, s80, s64\n"
"s_mul_i32 s80, s80, 0x2f000\n"
"s_lshl_b32 s81, s96, 11\n"
"s_add_u32 s80, s80, s81\n"
"s_add_u32 s75, s80, 0x300000\n"
"v_mfma_f32_32x32x16_bf16 v[0:15], a[160:163], a[96:99], v[0:15]\n"
"s_add_u32 m0, s65, 0x1c000\n"
"ds_read_b128 a[104:107], v135 offset:32768\n"
"ds_read_b64_tr_b16 a[192:193], v141 offset:51200\n"
"ds_read_b64_tr_b16 a[194:195], v141 offset:51712\n"
"buffer_load_dwordx4 v131, s[8:11], s72 offen lds\n"
"v_mfma_f32_32x32x16_bf16 v[16:31], a[164:167], a[96:99], v[16:31]\n"
"ds_read_b64_tr_b16 a[196:197], v142 offset:51200\n"
"ds_read_b64_tr_b16 a[198:199], v142 offset:51712\n"
"v_bfe_u32 v240, v253, 2, 1\n"
"v_lshlrev_b32 v240, 4, v240\n"
"v_and_b32 v241, 3, v253\n"
"v_add_u32 v240, v240, v241\n"
"v_bfe_u32 v241, v253, 3, 2\n"
"v_lshl_add_u32 v240, v241, 2, v240\n"
"v_mfma_f32_32x32x16_bf16 v[32:47], a[168:171], a[96:99], v[32:47]\n"
"s_lshl_b32 s80, s66, 2\n"
"ds_read_b64_tr_b16 a[200:201], v143 offset:51200\n"
"ds_read_b64_tr_b16 a[202:203], v143 offset:51712\n"
"buffer_load_dwordx4 v170, s[8:11], s72 offen offset:1024 lds\n"
"s_add_u32 s80, s80, s64\n"
"s_lshl_b32 s80, s80, 9\n"
"s_add_u32 s80, s80, 0x50000\n"
"v_lshl_add_u32 v247, v240, 4, s80\n"
"v_mfma_f32_32x32x16_bf16 v[48:63], a[172:175], a[96:99], v[48:63]\n"
"s_waitcnt lgkmcnt(7)\n"
"ds_read_b64_tr_b16 a[204:205], v144 offset:51200\n"
"ds_read_b64_tr_b16 a[206:207], v144 offset:51712\n"
"s_lshl_b32 s80, s64, 7\n"
"s_add_u32 s80, s80, 0x5000\n"
"v_lshl_add_u32 v250, v240, 2, s80\n"
"v_lshrrev_b32 v241, 5, v253\n"
"v_lshl_add_u32 v251, v241, 6, s80\n"
"v_and_b32 v241, 31, v253\n"
"v_mfma_f32_32x32x16_bf16 v[0:15], a[176:179], a[100:103], v[0:15]\n"
"ds_read_b128 a[108:111], v136 offset:32768\n"
"ds_read_b64_tr_b16 a[160:161], v141 offset:52224\n"
"ds_read_b64_tr_b16 a[162:163], v141 offset:52736\n"
"buffer_load_dwordx4 v171, s[8:11], s72 offen offset:2048 lds\n"
"v_mfma_f32_32x32x16_bf16 v[16:31], a[180:183], a[100:103], v[16:31]\n"
"ds_read_b64_tr_b16 a[164:165], v142 offset:52224\n"
"ds_read_b64_tr_b16 a[166:167], v142 offset:52736\n"
"v_lshrrev_b32 v240, 5, v253\n"
"v_lshl_add_u32 v240, v241, 1, v240\n"
"v_lshlrev_b32 v248, 4, v240\n"
"v_lshlrev_b32 v249, 4, v241\n"
"v_add_u32 v248, 0x8000, v248\n"
"v_add_u32 v249, 0xec00, v249\n"
"v_mfma_f32_32x32x16_bf16 v[32:47], a[184:187], a[100:103], v[32:47]\n"
"ds_read_b64_tr_b16 a[168:169], v143 offset:52224\n"
"ds_read_b64_tr_b16 a[170:171], v143 offset:52736\n"
"buffer_load_dwordx4 v172, s[8:11], s72 offen offset:3072 lds\n"
"s_add_u32 s72, s72, 0x36000\n"
"s_cmp_ge_u32 s64, 2\n"
"s_cselect_b32 s32, s36, s32\n"
"s_cselect_b32 s33, s37, s33\n"
"v_mfma_f32_32x32x16_bf16 v[48:63], a[188:191], a[100:103], v[48:63]\n"
"s_waitcnt lgkmcnt(7)\n"
"ds_read_b64_tr_b16 a[172:173], v144 offset:52224\n"
"ds_read_b64_tr_b16 a[174:175], v144 offset:52736\n"
"s_lshr_b32 s82, s3, 3\n"
"s_lshl_b32 s82, s82, 2\n"
"s_add_u32 s82, s82, s64\n"
"s_mul_i32 s82, s82, 5\n"
"v_min_u32 v235, 4, v253\n"
"v_add_u32 v235, s82, v235\n"
"v_lshlrev_b32 v235, 7, v235\n"
"v_min_u32 v235, L%=_end-L%=_pc-8, v235\n"
"s_getpc_b64 s[80:81]\n"
"L%=_st_ct5:\n"
"s_sub_u32 s80, s80, L%=_st_ct5-L%=_pc\n"
"s_subb_u32 s81, s81, 0\n"
"global_load_dword v235, v235, s[80:81]\n"
"s_waitcnt vmcnt(9)\n"
"s_waitcnt lgkmcnt(8)\n"
"s_barrier\n"
"v_mfma_f32_32x32x16_bf16 v[0:15], a[192:195], a[104:107], v[0:15]\n"
"s_add_u32 m0, s65, 0xfb80\n"
"ds_read_b128 a[112:115], v133\n"
"ds_read_b64_tr_b16 a[176:177], v141\n"
"ds_read_b64_tr_b16 a[178:179], v141 offset:512\n"
"buffer_load_dwordx4 v129, s[4:7], s68 offen offset:1152 lds\n"
"v_mfma_f32_32x32x16_bf16 v[16:31], a[196:199], a[104:107], v[16:31]\n"
"ds_read_b64_tr_b16 a[180:181], v142\n"
"ds_read_b64_tr_b16 a[182:183], v142 offset:512\n"
"v_mfma_f32_32x32x16_bf16 v[32:47], a[200:203], a[104:107], v[32:47]\n"
"s_add_u32 m0, s65, 0xff80\n"
"ds_read_b64_tr_b16 a[184:185], v143\n"
"ds_read_b64_tr_b16 a[186:187], v143 offset:512\n"
"buffer_load_dwordx4 v130, s[4:7], s69 offen offset:1152 lds\n"
"v_mfma_f32_32x32x16_bf16 v[48:63], a[204:207], a[104:107], v[48:63]\n"
"s_waitcnt lgkmcnt(7)\n"
"ds_read_b64_tr_b16 a[188:189], v144\n"
"ds_read_b64_tr_b16 a[190:191], v144 offset:512\n"
"v_mfma_f32_32x32x16_bf16 v[0:15], a[160:163], a[108:111], v[0:15]\n"
"s_add_u32 m0, s65, 0x10380\n"
"ds_read_b128 a[116:119], v134\n"
"ds_read_b64_tr_b16 a[192:193], v141 offset:1024\n"
"ds_read_b64_tr_b16 a[194:195], v141 offset:1536\n"
"buffer_load_dwordx4 v129, s[4:7], s70 offen offset:1152 lds\n"
"v_mfma_f32_32x32x16_bf16 v[16:31], a[164:167], a[108:111], v[16:31]\n"
"ds_read_b64_tr_b16 a[196:197], v142 offset:1024\n"
"ds_read_b64_tr_b16 a[198:199], v142 offset:1536\n"
"v_mfma_f32_32x32x16_bf16 v[32:47], a[168:171], a[108:111], v[32:47]\n"
"s_add_u32 m0, s65, 0x10780\n"
"ds_read_b64_tr_b16 a[200:201], v143 offset:1024\n"
"ds_read_b64_tr_b16 a[202:203], v143 offset:1536\n"
"buffer_load_dwordx4 v130, s[4:7], s71 offen offset:1152 lds\n"
"v_mfma_f32_32x32x16_bf16 v[48:63], a[172:175], a[108:111], v[48:63]\n"
"s_waitcnt lgkmcnt(7)\n"
"ds_read_b64_tr_b16 a[204:205], v144 offset:1024\n"
"ds_read_b64_tr_b16 a[206:207], v144 offset:1536\n"
"v_mfma_f32_32x32x16_bf16 v[0:15], a[176:179], a[112:115], v[0:15]\n"
"s_add_u32 m0, s65, 0x20000\n"
"ds_read_b128 a[120:123], v135\n"
"ds_read_b64_tr_b16 a[160:161], v141 offset:2048\n"
"ds_read_b64_tr_b16 a[162:163], v141 offset:2560\n"
"buffer_load_dwordx4 v131, s[8:11], s72 offen lds\n"
"v_mfma_f32_32x32x16_bf16 v[16:31], a[180:183], a[112:115], v[16:31]\n"
"ds_read_b64_tr_b16 a[164:165], v142 offset:2048\n"
"ds_read_b64_tr_b16 a[166:167], v142 offset:2560\n"
"v_mfma_f32_32x32x16_bf16 v[32:47], a[184:187], a[112:115], v[32:47]\n"
"ds_read_b64_tr_b16 a[168:169], v143 offset:2048\n"
"ds_read_b64_tr_b16 a[170:171], v143 offset:2560\n"
"buffer_load_dwordx4 v170, s[8:11], s72 offen offset:1024 lds\n"
"v_mfma_f32_32x32x16_bf16 v[48:63], a[188:191], a[112:115], v[48:63]\n"
"s_waitcnt lgkmcnt(7)\n"
"ds_read_b64_tr_b16 a[172:173], v144 offset:2048\n"
"ds_read_b64_tr_b16 a[174:175], v144 offset:2560\n"
"v_mfma_f32_32x32x16_bf16 v[0:15], a[192:195], a[116:119], v[0:15]\n"
"ds_read_b128 a[124:127], v136\n"
"ds_read_b64_tr_b16 a[176:177], v141 offset:3072\n"
"ds_read_b64_tr_b16 a[178:179], v141 offset:3584\n"
"buffer_load_dwordx4 v171, s[8:11], s72 offen offset:2048 lds\n"
"v_mfma_f32_32x32x16_bf16 v[16:31], a[196:199], a[116:119], v[16:31]\n"
"ds_read_b64_tr_b16 a[180:181], v142 offset:3072\n"
"ds_read_b64_tr_b16 a[182:183], v142 offset:3584\n"
"v_mfma_f32_32x32x16_bf16 v[32:47], a[200:203], a[116:119], v[32:47]\n"
"ds_read_b64_tr_b16 a[184:185], v143 offset:3072\n"
"ds_read_b64_tr_b16 a[186:187], v143 offset:3584\n"
"buffer_load_dwordx4 v172, s[8:11], s72 offen offset:3072 lds\n"
"s_add_u32 s72, s72, 0x36000\n"
"v_mfma_f32_32x32x16_bf16 v[48:63], a[204:207], a[116:119], v[48:63]\n"
"s_waitcnt lgkmcnt(7)\n"
"ds_read_b64_tr_b16 a[188:189], v144 offset:3072\n"
"ds_read_b64_tr_b16 a[190:191], v144 offset:3584\n"
"s_waitcnt vmcnt(9)\n"
"s_waitcnt lgkmcnt(8)\n"
"s_barrier\n"
"v_mfma_f32_32x32x16_bf16 v[0:15], a[160:163], a[120:123], v[0:15]\n"
"s_add_u32 m0, s65, 0x24000\n"
"ds_read_b128 a[128:131], v133 offset:16384\n"
"ds_read_b64_tr_b16 a[192:193], v141 offset:16384\n"
"ds_read_b64_tr_b16 a[194:195], v141 offset:16896\n"
"buffer_load_dwordx4 v132, s[16:19], s73 offen lds\n"
"v_mfma_f32_32x32x16_bf16 v[16:31], a[164:167], a[120:123], v[16:31]\n"
"s_lshl_b32 s81, s93, 9\n"
"ds_read_b64_tr_b16 a[196:197], v142 offset:16384\n"
"ds_read_b64_tr_b16 a[198:199], v142 offset:16896\n"
"buffer_load_dwordx4 v[104:107], v247, s[24:27], 0 offen sc1\n"
"buffer_load_dwordx4 v[108:111], v249, s[24:27], s81 offen sc1\n"
"s_lshl_b32 s81, s94, 9\n"
"buffer_load_dwordx4 v[112:115], v249, s[24:27], s81 offen sc1\n"
"s_lshl_b32 s81, s96, 10\n"
"buffer_load_dwordx4 v[116:119], v248, s[24:27], s81 offen sc1\n"
"v_mfma_f32_32x32x16_bf16 v[32:47], a[168:171], a[120:123], v[32:47]\n"
"s_mul_i32 s80, s97, 0x1400\n"
"ds_read_b64_tr_b16 a[200:201], v143 offset:16384\n"
"ds_read_b64_tr_b16 a[202:203], v143 offset:16896\n"
"v_add_u32 v130, 0x1000, v128\n"
"s_mul_i32 s81, s64, 0xa000\n"
"s_add_u32 s90, s80, s81\n"
"s_add_u32 s90, s90, 0x80000\n"
"s_bfe_u32 s1, s3, 0x10002\n"
"v_mfma_f32_32x32x16_bf16 v[48:63], a[172:175], a[120:123], v[48:63]\n"
"s_waitcnt lgkmcnt(7)\n"
"s_add_u32 m0, s65, 0x24400\n"
"ds_read_b64_tr_b16 a[204:205], v144 offset:16384\n"
"ds_read_b64_tr_b16 a[206:207], v144 offset:16896\n"
"buffer_load_dwordx4 v132, s[20:23], s73 offen lds\n"
"s_lshl_b32 s80, s98, 2\n"
"v_mfma_f32_32x32x16_bf16 v[0:15], a[176:179], a[124:127], v[0:15]\n"
"s_add_u32 s80, s80, s64\n"
"ds_read_b128 a[132:135], v134 offset:16384\n"
"ds_read_b64_tr_b16 a[160:161], v141 offset:17408\n"
"ds_read_b64_tr_b16 a[162:163], v141 offset:17920\n"
"s_lshl_b32 s80, s80, 1\n"
"s_add_u32 s80, s80, s1\n"
"s_mul_i32 s92, s80, 0x17800\n"
"s_add_u32 s92, s92, 0x300000\n"
"v_mfma_f32_32x32x16_bf16 v[16:31], a[180:183], a[124:127], v[16:31]\n"
"s_add_u32 m0, s65, 0x24780\n"
"ds_read_b64_tr_b16 a[164:165], v142 offset:17408\n"
"ds_read_b64_tr_b16 a[166:167], v142 offset:17920\n"
"buffer_load_dwordx4 v132, s[16:19], s73 offen offset:128 lds\n"
"s_mul_i32 s0, s1, 0xfffd1000\n"
"v_mfma_f32_32x32x16_bf16 v[32:47], a[184:187], a[124:127], v[32:47]\n"
"s_add_u32 s0, s0, 0x17800\n"
"ds_read_b64_tr_b16 a[168:169], v143 offset:17408\n"
"ds_read_b64_tr_b16 a[170:171], v143 offset:17920\n"
"v_mfma_f32_32x32x16_bf16 v[48:63], a[188:191], a[124:127], v[48:63]\n"
"s_waitcnt lgkmcnt(7)\n"
"ds_read_b64_tr_b16 a[172:173], v144 offset:17408\n"
"ds_read_b64_tr_b16 a[174:175], v144 offset:17920\n"
"v_mfma_f32_32x32x16_bf16 v[0:15], a[192:195], a[128:131], v[0:15]\n"
"s_add_u32 m0, s65, 0x24b80\n"
"ds_read_b128 a[136:139], v135 offset:16384\n"
"ds_read_b64_tr_b16 a[176:177], v141 offset:18432\n"
"ds_read_b64_tr_b16 a[178:179], v141 offset:18944\n"
"buffer_load_dwordx4 v132, s[20:23], s73 offen offset:128 lds\n"
"v_mfma_f32_32x32x16_bf16 v[16:31], a[196:199], a[128:131], v[16:31]\n"
"ds_read_b64_tr_b16 a[180:181], v142 offset:18432\n"
"ds_read_b64_tr_b16 a[182:183], v142 offset:18944\n"
"v_mfma_f32_32x32x16_bf16 v[32:47], a[200:203], a[128:131], v[32:47]\n"
"ds_read_b64_tr_b16 a[184:185], v143 offset:18432\n"
"ds_read_b64_tr_b16 a[186:187], v143 offset:18944\n"
"v_mfma_f32_32x32x16_bf16 v[48:63], a[204:207], a[128:131], v[48:63]\n"
"s_waitcnt lgkmcnt(7)\n"
"ds_read_b64_tr_b16 a[188:189], v144 offset:18432\n"
"ds_read_b64_tr_b16 a[190:191], v144 offset:18944\n"
"v_mfma_f32_32x32x16_bf16 v[0:15], a[160:163], a[132:135], v[0:15]\n"
"ds_read_b128 a[140:143], v136 offset:16384\n"
"ds_read_b64_tr_b16 a[192:193], v141 offset:19456\n"
"ds_read_b64_tr_b16 a[194:195], v141 offset:19968\n"
"v_mfma_f32_32x32x16_bf16 v[16:31], a[164:167], a[132:135], v[16:31]\n"
"ds_read_b64_tr_b16 a[196:197], v142 offset:19456\n"
"ds_read_b64_tr_b16 a[198:199], v142 offset:19968\n"
"v_mfma_f32_32x32x16_bf16 v[32:47], a[168:171], a[132:135], v[32:47]\n"
"ds_read_b64_tr_b16 a[200:201], v143 offset:19456\n"
"ds_read_b64_tr_b16 a[202:203], v143 offset:19968\n"
"v_mfma_f32_32x32x16_bf16 v[48:63], a[172:175], a[132:135], v[48:63]\n"
"s_waitcnt lgkmcnt(7)\n"
"ds_read_b64_tr_b16 a[204:205], v144 offset:19456\n"
"ds_read_b64_tr_b16 a[206:207], v144 offset:19968\n"
"s_waitcnt vmcnt(8)\n"
"s_waitcnt lgkmcnt(8)\n"
"s_barrier\n"
"v_mfma_f32_32x32x16_bf16 v[0:15], a[176:179], a[136:139], v[0:15]\n"
"s_add_u32 m0, s65, 0x17f00\n"
"ds_read_b128 a[144:147], v133 offset:32768\n"
"ds_read_b64_tr_b16 a[160:161], v141 offset:32768\n"
"ds_read_b64_tr_b16 a[162:163], v141 offset:33280\n"
"buffer_load_dwordx4 v132, s[16:19], s73 offen offset:256 lds\n"
"v_mfma_f32_32x32x16_bf16 v[16:31], a[180:183], a[136:139], v[16:31]\n"
"ds_read_b64_tr_b16 a[164:165], v142 offset:32768\n"
"ds_read_b64_tr_b16 a[166:167], v142 offset:33280\n"
"s_waitcnt vmcnt(7)\n"
"v_xor_b32 v240, s76, v105\n"
"v_bitop3_b32 v240, v107, s78, v240 bitop3:0xbe\n"
"s_waitcnt vmcnt(6)\n"
"v_bitop3_b32 v240, v109, s76, v240 bitop3:0xbe\n"
"v_bitop3_b32 v240, v111, s76, v240 bitop3:0xbe\n"
"s_waitcnt vmcnt(5)\n"
"v_bitop3_b32 v240, v113, s76, v240 bitop3:0xbe\n"
"v_bitop3_b32 v240, v115, s76, v240 bitop3:0xbe\n"
"s_waitcnt vmcnt(4)\n"
"v_bitop3_b32 v240, v117, s76, v240 bitop3:0xbe\n"
"v_bitop3_b32 v240, v119, s76, v240 bitop3:0xbe\n"
"v_cmp_ne_u32 vcc, 0, v240\n"
"s_nop 0\n"
"s_cbranch_vccnz L%=_stS_slow\n"
"L%=_stS_back:\n"
"v_mfma_f32_32x32x16_bf16 v[32:47], a[184:187], a[136:139], v[32:47]\n"
"ds_read_b64_tr_b16 a[168:169], v143 offset:32768\n"
"ds_read_b64_tr_b16 a[170:171], v143 offset:33280\n"
"ds_write_b32 v250, v106\n"
"ds_read_b128 v[174:177], v251\n"
"ds_read_b128 v[178:181], v251 offset:16\n"
"ds_read_b128 v[182:185], v251 offset:32\n"
"v_mfma_f32_32x32x16_bf16 v[48:63], a[188:191], a[136:139], v[48:63]\n"
"s_waitcnt lgkmcnt(11)\n"
"s_add_u32 m0, s65, 0x18300\n"
"ds_read_b64_tr_b16 a[172:173], v144 offset:32768\n"
"ds_read_b64_tr_b16 a[174:175], v144 offset:33280\n"
"buffer_load_dwordx4 v132, s[20:23], s73 offen offset:256 lds\n"
"ds_read_b128 v[186:189], v251 offset:48\n"
"v_mfma_f32_32x32x16_bf16 v[0:15], a[192:195], a[140:143], v[0:15]\n"
"ds_read_b128 a[148:151], v134 offset:32768\n"
"ds_read_b64_tr_b16 a[176:177], v141 offset:33792\n"
"ds_read_b64_tr_b16 a[178:179], v141 offset:34304\n"
"v_mul_f32 v104, v104, v246\n"
"v_cvt_pk_bf16_f32 v240, v104, v104\n"
"v_mul_f32 v146, 0xbfb8aa3b, v106\n"
"v_lshlrev_b32 v240, 16, v240\n"
"v_sub_f32 v241, v104, v240\n"
"v_mfma_f32_32x32x16_bf16 v[16:31], a[196:199], a[140:143], v[16:31]\n"
"s_add_u32 m0, s65, 0x18680\n"
"ds_read_b64_tr_b16 a[180:181], v142 offset:33792\n"
"ds_read_b64_tr_b16 a[182:183], v142 offset:34304\n"
"buffer_load_dwordx4 v132, s[16:19], s73 offen offset:384 lds\n"
"v_cvt_pk_bf16_f32 v148, v240, v241\n"
"v_mfma_f32_32x32x16_bf16 v[32:47], a[200:203], a[140:143], v[32:47]\n"
"ds_read_b64_tr_b16 a[184:185], v143 offset:33792\n"
"ds_read_b64_tr_b16 a[186:187], v143 offset:34304\n"
"v_mul_f32 v240, v106, v106\n"
"v_mov_b32 v149, v148\n"
"v_rcp_f32 v147, v240\n"
"v_mov_b32 v117, v116\n"
"v_mov_b32 v119, v118\n"
"v_mfma_f32_32x32x16_bf16 v[48:63], a[204:207], a[140:143], v[48:63]\n"
"s_waitcnt lgkmcnt(8)\n"
"ds_read_b64_tr_b16 a[188:189], v144 offset:33792\n"
"ds_read_b64_tr_b16 a[190:191], v144 offset:34304\n"
"v_permlane32_swap_b32 v116, v117\n"
"v_permlane32_swap_b32 v118, v119\n"
"v_cvt_pk_bf16_f32 v153, v116, v116\n"
"v_and_b32 v241, 0xffff0000, v153\n"
"v_sub_f32 v241, v116, v241\n"
"v_cvt_pk_bf16_f32 v152, v241, v241\n"
"v_mfma_f32_32x32x16_bf16 v[0:15], a[160:163], a[144:147], v[0:15]\n"
"s_add_u32 m0, s65, 0x18a80\n"
"ds_read_b128 a[152:155], v135 offset:32768\n"
"ds_read_b64_tr_b16 a[192:193], v141 offset:34816\n"
"ds_read_b64_tr_b16 a[194:195], v141 offset:35328\n"
"buffer_load_dwordx4 v132, s[20:23], s73 offen offset:384 lds\n"
"v_mfma_f32_32x32x16_bf16 v[16:31], a[164:167], a[144:147], v[16:31]\n"
"ds_read_b64_tr_b16 a[196:197], v142 offset:34816\n"
"ds_read_b64_tr_b16 a[198:199], v142 offset:35328\n"
"v_cvt_pk_bf16_f32 v155, v118, v118\n"
"v_and_b32 v241, 0xffff0000, v155\n"
"v_sub_f32 v241, v118, v241\n"
"v_cvt_pk_bf16_f32 v154, v241, v241\n"
"v_cvt_pk_bf16_f32 v157, v117, v117\n"
"v_and_b32 v241, 0xffff0000, v157\n"
"v_mfma_f32_32x32x16_bf16 v[32:47], a[168:171], a[144:147], v[32:47]\n"
"ds_read_b64_tr_b16 a[200:201], v143 offset:34816\n"
"ds_read_b64_tr_b16 a[202:203], v143 offset:35328\n"
"v_sub_f32 v241, v117, v241\n"
"v_cvt_pk_bf16_f32 v156, v241, v241\n"
"v_cvt_pk_bf16_f32 v159, v119, v119\n"
"v_and_b32 v241, 0xffff0000, v159\n"
"v_sub_f32 v241, v119, v241\n"
"v_cvt_pk_bf16_f32 v158, v241, v241\n"
"v_mfma_f32_32x32x16_bf16 v[48:63], a[172:175], a[144:147], v[48:63]\n"
"s_waitcnt lgkmcnt(7)\n"
"ds_read_b64_tr_b16 a[204:205], v144 offset:34816\n"
"ds_read_b64_tr_b16 a[206:207], v144 offset:35328\n"
"v_cvt_pk_bf16_f32 v161, v108, v108\n"
"v_and_b32 v241, 0xffff0000, v161\n"
"v_sub_f32 v241, v108, v241\n"
"v_cvt_pk_bf16_f32 v160, v241, v241\n"
"v_cvt_pk_bf16_f32 v163, v110, v110\n"
"v_and_b32 v241, 0xffff0000, v163\n"
"v_mfma_f32_32x32x16_bf16 v[0:15], a[176:179], a[148:151], v[0:15]\n"
"ds_read_b128 a[156:159], v136 offset:32768\n"
"ds_read_b64_tr_b16 a[160:161], v141 offset:35840\n"
"ds_read_b64_tr_b16 a[162:163], v141 offset:36352\n"
"v_sub_f32 v241, v110, v241\n"
"v_cvt_pk_bf16_f32 v162, v241, v241\n"
"v_cvt_pk_bf16_f32 v165, v112, v112\n"
"v_and_b32 v241, 0xffff0000, v165\n"
"v_sub_f32 v241, v112, v241\n"
"v_mfma_f32_32x32x16_bf16 v[16:31], a[180:183], a[148:151], v[16:31]\n"
"ds_read_b64_tr_b16 a[164:165], v142 offset:35840\n"
"ds_read_b64_tr_b16 a[166:167], v142 offset:36352\n"
"ds_read_b64_tr_b16 a[168:169], v143 offset:35840\n"
"v_cvt_pk_bf16_f32 v164, v241, v241\n"
"v_cvt_pk_bf16_f32 v167, v114, v114\n"
"v_and_b32 v241, 0xffff0000, v167\n"
"v_sub_f32 v241, v114, v241\n"
"v_cvt_pk_bf16_f32 v166, v241, v241\n"
"v_mfma_f32_32x32x16_bf16 v[32:47], a[184:187], a[148:151], v[32:47]\n"
"s_and_b32 s80, s64, 1\n"
"ds_read_b64_tr_b16 a[170:171], v143 offset:36352\n"
"ds_read_b64_tr_b16 a[172:173], v144 offset:35840\n"
"ds_read_b64_tr_b16 a[174:175], v144 offset:36352\n"
"v_lshrrev_b32 v232, 3, v253\n"
"s_mul_i32 s81, s80, 40\n"
"s_mul_i32 s82, s97, 0x50\n"
"s_add_u32 s81, s81, s82\n"
"s_lshl_b32 s80, s80, 2\n"
"v_add_u32 v233, s81, v232\n"
"v_lshrrev_b32 v232, 1, v232\n"
"v_xor_b32 v232, s80, v232\n"
"v_mfma_f32_32x32x16_bf16 v[48:63], a[188:191], a[148:151], v[48:63]\n"
"s_waitcnt lgkmcnt(9)\n"
"v_mul_u32_u24 v233, 0x500, v233\n"
"v_and_b32 v234, 7, v253\n"
"v_xor_b32 v232, v232, v234\n"
"v_lshl_add_u32 v133, v232, 4, v233\n"
"v_xor_b32 v134, 64, v133\n"
"v_add_u32 v134, 0x2400, v134\n"
"v_xor_b32 v135, 0, v133\n"
"v_add_u32 v135, 0x4800, v135\n"
"s_waitcnt vmcnt(4)\n"
"s_waitcnt lgkmcnt(0)\n"
"s_barrier\n"
"v_mfma_f32_32x32x16_bf16 v[0:15], a[192:195], a[152:155], v[0:15]\n"
"s_add_u32 m0, s65, 0x1be00\n"
"ds_read_b128 a[176:179], v137 offset:49152\n"
"buffer_load_dwordx4 v132, s[16:19], s73 offen offset:512 lds\n"
"v_mfma_f32_32x32x16_bf16 v[16:31], a[196:199], a[152:155], v[16:31]\n"
"ds_read_b128 a[180:183], v137 offset:50176\n"
"v_xor_b32 v136, 64, v133\n"
"v_add_u32 v136, 0x6c00, v136\n"
"v_xor_b32 v129, 0, v133\n"
"v_add_u32 v129, 0xa000, v129\n"
"v_mfma_f32_32x32x16_bf16 v[32:47], a[200:203], a[152:155], v[32:47]\n"
"ds_read_b128 a[184:187], v138 offset:49152\n"
"v_mfma_f32_32x32x16_bf16 v[48:63], a[204:207], a[152:155], v[48:63]\n"
"s_add_u32 m0, s65, 0x1c200\n"
"ds_read_b128 a[188:191], v138 offset:50176\n"
"buffer_load_dwordx4 v132, s[20:23], s73 offen offset:512 lds\n"
"v_mfma_f32_32x32x16_bf16 v[0:15], a[160:163], a[156:159], v[0:15]\n"
"ds_read_b128 a[192:195], v139 offset:49152\n"
"v_mfma_f32_32x32x16_bf16 v[16:31], a[164:167], a[156:159], v[16:31]\n"
"s_add_u32 m0, s65, 0x1c580\n"
"ds_read_b128 a[196:199], v139 offset:50176\n"
"buffer_load_dwordx4 v132, s[16:19], s73 offen offset:640 lds\n"
"v_mfma_f32_32x32x16_bf16 v[32:47], a[168:171], a[156:159], v[32:47]\n"
"ds_read_b128 a[200:203], v140 offset:49152\n"
"v_mfma_f32_32x32x16_bf16 v[48:63], a[172:175], a[156:159], v[48:63]\n"
"ds_read_b128 a[204:207], v140 offset:50176\n"
"v_mfma_f32_32x32x16_bf16 v[0:15], v[152:155], v[148:151], v[0:15]\n"
"v_mfma_f32_32x32x16_bf16 v[16:31], v[154:157], v[148:151], v[16:31]\n"
"v_mfma_f32_32x32x16_bf16 v[32:47], v[156:159], v[148:151], v[32:47]\n"
"v_mfma_f32_32x32x16_bf16 v[48:63], v[158:161], v[148:151], v[48:63]\n"
"s_waitcnt lgkmcnt(4)\n"
"v_mfma_f32_32x32x16_bf16 v[64:79], a[0:3], a[176:179], 0\n"
"s_add_u32 m0, s65, 0x1c980\n"
"ds_read_b128 a[160:163], v137 offset:51200\n"
"buffer_load_dwordx4 v132, s[20:23], s73 offen offset:640 lds\n"
"s_lshr_b32 s82, s3, 3\n"
"s_lshl_b32 s82, s82, 2\n"
"s_add_u32 s82, s82, s64\n"
"s_mul_i32 s82, s82, 5\n"
"v_min_u32 v251, 4, v253\n"
"v_add_u32 v251, s82, v251\n"
"v_lshlrev_b32 v251, 7, v251\n"
"v_min_u32 v251, L%=_end-L%=_pc-8, v251\n"
"s_getpc_b64 s[80:81]\n"
"L%=_st_ct6:\n"
"s_sub_u32 s80, s80, L%=_st_ct6-L%=_pc\n"
"s_subb_u32 s81, s81, 0\n"
"global_load_dword v251, v251, s[80:81]\n"
"v_mfma_f32_32x32x16_bf16 v[80:95], a[0:3], a[180:183], 0\n"
"ds_read_b128 a[164:167], v137 offset:52224\n"
"v_mul_f32 v232, v0, v146\n"
"v_mul_f32 v233, v1, v146\n"
"v_mul_f32 v234, v2, v146\n"
"v_mul_f32 v235, v3, v146\n"
"v_exp_f32 v232, v232\n"
"v_mfma_f32_32x32x16_bf16 v[64:79], a[4:7], a[184:187], v[64:79]\n"
"ds_read_b128 a[168:171], v138 offset:51200\n"
"v_exp_f32 v233, v233\n"
"v_exp_f32 v234, v234\n"
"v_exp_f32 v235, v235\n"
"v_mfma_f32_32x32x16_bf16 v[80:95], a[4:7], a[188:191], v[80:95]\n"
"s_waitcnt lgkmcnt(3)\n"
"s_add_u32 m0, s65, 0x1fd00\n"
"ds_read_b128 a[172:175], v138 offset:52224\n"
"buffer_load_dwordx4 v132, s[16:19], s73 offen offset:768 lds\n"
"v_mul_f32 v8, v0, v8\n"
"v_mfma_f32_32x32x16_bf16 v[64:79], a[8:11], a[192:195], v[64:79]\n"
"ds_read_b128 a[176:179], v139 offset:51200\n"
"ds_read_b128 a[180:183], v139 offset:52224\n"
"v_mul_f32 v9, v1, v9\n"
"v_mul_f32 v10, v2, v10\n"
"v_mul_f32 v11, v3, v11\n"
"v_fma_f32 v232, v232, v147, v147\n"
"v_fma_f32 v233, v233, v147, v147\n"
"v_mfma_f32_32x32x16_bf16 v[80:95], a[8:11], a[196:199], v[80:95]\n"
"s_add_u32 m0, s65, 0x20100\n"
"ds_read_b128 a[184:187], v140 offset:51200\n"
"buffer_load_dwordx4 v132, s[20:23], s73 offen offset:768 lds\n"
"v_fma_f32 v234, v234, v147, v147\n"
"v_mfma_f32_32x32x16_bf16 v[64:79], a[12:15], a[200:203], v[64:79]\n"
"ds_read_b128 a[188:191], v140 offset:52224\n"
"v_fma_f32 v235, v235, v147, v147\n"
"v_rcp_f32 v232, v232\n"
"v_rcp_f32 v233, v233\n"
"v_mfma_f32_32x32x16_bf16 v[80:95], a[12:15], a[204:207], v[80:95]\n"
"s_waitcnt lgkmcnt(4)\n"
"v_rcp_f32 v234, v234\n"
"v_rcp_f32 v235, v235\n"
"v_mul_f32 v8, v8, v232\n"
"v_mul_f32 v9, v9, v233\n"
"v_mul_f32 v10, v10, v234\n"
"s_waitcnt vmcnt(7)\n"
"s_waitcnt lgkmcnt(0)\n"
"s_barrier\n"
"v_mfma_f32_32x32x16_bf16 v[64:79], a[16:19], a[160:163], v[64:79]\n"
"s_add_u32 m0, s65, 0x20480\n"
"ds_read_b128 a[192:195], v137\n"
"buffer_load_dwordx4 v132, s[16:19], s73 offen offset:896 lds\n"
"v_mfma_f32_32x32x16_bf16 v[80:95], a[16:19], a[164:167], v[80:95]\n"
"ds_read_b128 a[196:199], v137 offset:1024\n"
"v_mul_f32 v11, v11, v235\n"
"v_cvt_pk_bf16_f32 v0, v8, v9\n"
"v_cvt_pk_bf16_f32 v1, v10, v11\n"
"v_mul_f32 v232, v4, v146\n"
"v_mul_f32 v233, v5, v146\n"
"v_mul_f32 v234, v6, v146\n"
"v_mfma_f32_32x32x16_bf16 v[64:79], a[20:23], a[168:171], v[64:79]\n"
"s_add_u32 m0, s65, 0x20880\n"
"ds_read_b128 a[200:203], v138\n"
"buffer_load_dwordx4 v132, s[20:23], s73 offen offset:896 lds\n"
"v_mul_f32 v235, v7, v146\n"
"v_mfma_f32_32x32x16_bf16 v[80:95], a[20:23], a[172:175], v[80:95]\n"
"ds_read_b128 a[204:207], v138 offset:1024\n"
"v_exp_f32 v232, v232\n"
"v_exp_f32 v233, v233\n"
"v_exp_f32 v234, v234\n"
"v_mfma_f32_32x32x16_bf16 v[64:79], a[24:27], a[176:179], v[64:79]\n"
"s_add_u32 m0, s65, 0x23c00\n"
"ds_read_b128 a[160:163], v139\n"
"buffer_load_dwordx4 v132, s[16:19], s73 offen offset:1024 lds\n"
"v_mfma_f32_32x32x16_bf16 v[80:95], a[24:27], a[180:183], v[80:95]\n"
"ds_read_b128 a[164:167], v139 offset:1024\n"
"v_exp_f32 v235, v235\n"
"v_mul_f32 v12, v4, v12\n"
"v_mul_f32 v13, v5, v13\n"
"v_mul_f32 v14, v6, v14\n"
"v_mul_f32 v15, v7, v15\n"
"v_mfma_f32_32x32x16_bf16 v[64:79], a[28:31], a[184:187], v[64:79]\n"
"ds_read_b128 a[168:171], v140\n"
"v_fma_f32 v232, v232, v147, v147\n"
"v_fma_f32 v233, v233, v147, v147\n"
"v_fma_f32 v234, v234, v147, v147\n"
"v_fma_f32 v235, v235, v147, v147\n"
"v_rcp_f32 v232, v232\n"
"v_mfma_f32_32x32x16_bf16 v[80:95], a[28:31], a[188:191], v[80:95]\n"
"s_waitcnt lgkmcnt(3)\n"
"s_add_u32 m0, s65, 0x24000\n"
"ds_read_b128 a[172:175], v140 offset:1024\n"
"buffer_load_dwordx4 v132, s[20:23], s73 offen offset:1024 lds\n"
"v_mfma_f32_32x32x16_bf16 v[64:79], a[32:35], a[192:195], v[64:79]\n"
"ds_read_b128 a[176:179], v137 offset:2048\n"
"v_rcp_f32 v233, v233\n"
"v_rcp_f32 v234, v234\n"
"v_rcp_f32 v235, v235\n"
"v_mfma_f32_32x32x16_bf16 v[80:95], a[32:35], a[196:199], v[80:95]\n"
"s_add_u32 m0, s65, 0x24380\n"
"ds_read_b128 a[180:183], v137 offset:3072\n"
"buffer_load_dwordx4 v132, s[16:19], s73 offen offset:1152 lds\n"
"v_mul_f32 v12, v12, v232\n"
"v_mfma_f32_32x32x16_bf16 v[64:79], a[36:39], a[200:203], v[64:79]\n"
"ds_read_b128 a[184:187], v138 offset:2048\n"
"v_mul_f32 v13, v13, v233\n"
"v_mul_f32 v14, v14, v234\n"
"v_mul_f32 v15, v15, v235\n"
"v_cvt_pk_bf16_f32 v2, v12, v13\n"
"v_cvt_pk_bf16_f32 v3, v14, v15\n"
"buffer_store_dwordx4 v[0:3], v145, s[24:27], s75 offen sc1\n"
"v_mfma_f32_32x32x16_bf16 v[80:95], a[36:39], a[204:207], v[80:95]\n"
"s_waitcnt lgkmcnt(3)\n"
"s_add_u32 m0, s65, 0x24780\n"
"ds_read_b128 a[188:191], v138 offset:3072\n"
"buffer_load_dwordx4 v132, s[20:23], s73 offen offset:1152 lds\n"
"v_mul_f32 v232, v16, v146\n"
"v_mfma_f32_32x32x16_bf16 v[64:79], a[40:43], a[160:163], v[64:79]\n"
"ds_read_b128 a[192:195], v139 offset:2048\n"
"ds_read_b128 a[196:199], v139 offset:3072\n"
"v_mul_f32 v233, v17, v146\n"
"v_mul_f32 v234, v18, v146\n"
"v_mul_f32 v235, v19, v146\n"
"v_exp_f32 v232, v232\n"
"v_mfma_f32_32x32x16_bf16 v[80:95], a[40:43], a[164:167], v[80:95]\n"
"s_lshl_b32 s99, s1, 7\n"
"s_add_u32 s99, s99, 0\n"
"s_add_u32 m0, s88, 0\n"
"ds_read_b128 a[200:203], v140 offset:2048\n"
"buffer_load_dwordx4 v133, s[32:35], s99 offen lds\n"
"v_mfma_f32_32x32x16_bf16 v[64:79], a[44:47], a[168:171], v[64:79]\n"
"ds_read_b128 a[204:207], v140 offset:3072\n"
"v_exp_f32 v233, v233\n"
"v_exp_f32 v234, v234\n"
"v_exp_f32 v235, v235\n"
"v_mfma_f32_32x32x16_bf16 v[80:95], a[44:47], a[172:175], v[80:95]\n"
"s_waitcnt lgkmcnt(4)\n"
"v_mul_f32 v24, v16, v24\n"
"v_mul_f32 v25, v17, v25\n"
"v_mul_f32 v26, v18, v26\n"
"v_mul_f32 v27, v19, v27\n"
"v_fma_f32 v232, v232, v147, v147\n"
"v_fma_f32 v233, v233, v147, v147\n"
"v_fma_f32 v234, v234, v147, v147\n"
"s_waitcnt vmcnt(11)\n"
"s_waitcnt lgkmcnt(0)\n"
"s_barrier\n"
"v_mfma_f32_32x32x16_bf16 v[64:79], a[48:51], a[176:179], v[64:79]\n"
"s_add_u32 m0, s65, 0x18000\n"
"ds_read_b128 a[160:163], v137 offset:16384\n"
"buffer_load_dwordx4 v132, s[16:19], s74 offen lds\n"
"v_mfma_f32_32x32x16_bf16 v[80:95], a[48:51], a[180:183], v[80:95]\n"
"ds_read_b128 a[164:167], v137 offset:17408\n"
"v_fma_f32 v235, v235, v147, v147\n"
"v_rcp_f32 v232, v232\n"
"v_rcp_f32 v233, v233\n"
"v_mfma_f32_32x32x16_bf16 v[64:79], a[52:55], a[184:187], v[64:79]\n"
"s_add_u32 m0, s65, 0x18400\n"
"ds_read_b128 a[168:171], v138 offset:16384\n"
"buffer_load_dwordx4 v132, s[20:23], s74 offen lds\n"
"v_mfma_f32_32x32x16_bf16 v[80:95], a[52:55], a[188:191], v[80:95]\n"
"ds_read_b128 a[172:175], v138 offset:17408\n"
"v_rcp_f32 v234, v234\n"
"v_rcp_f32 v235, v235\n"
"v_mul_f32 v24, v24, v232\n"
"v_mul_f32 v25, v25, v233\n"
"v_mfma_f32_32x32x16_bf16 v[64:79], a[56:59], a[192:195], v[64:79]\n"
"s_add_u32 m0, s65, 0x18780\n"
"ds_read_b128 a[176:179], v139 offset:16384\n"
"buffer_load_dwordx4 v132, s[16:19], s74 offen offset:128 lds\n"
"v_mul_f32 v26, v26, v234\n"
"v_mfma_f32_32x32x16_bf16 v[80:95], a[56:59], a[196:199], v[80:95]\n"
"ds_read_b128 a[180:183], v139 offset:17408\n"
"v_mul_f32 v27, v27, v235\n"
"v_cvt_pk_bf16_f32 v16, v24, v25\n"
"v_cvt_pk_bf16_f32 v17, v26, v27\n"
"v_mul_f32 v232, v20, v146\n"
"v_mul_f32 v233, v21, v146\n"
"v_mul_f32 v234, v22, v146\n"
"v_mfma_f32_32x32x16_bf16 v[64:79], a[60:63], a[200:203], v[64:79]\n"
"ds_read_b128 a[184:187], v140 offset:16384\n"
"v_mul_f32 v235, v23, v146\n"
"v_exp_f32 v232, v232\n"
"v_exp_f32 v233, v233\n"
"v_mfma_f32_32x32x16_bf16 v[80:95], a[60:63], a[204:207], v[80:95]\n"
"s_waitcnt lgkmcnt(3)\n"
"s_add_u32 m0, s65, 0x18b80\n"
"ds_read_b128 a[188:191], v140 offset:17408\n"
"buffer_load_dwordx4 v132, s[20:23], s74 offen offset:128 lds\n"
"v_mfma_f32_32x32x16_bf16 v[64:79], a[64:67], a[160:163], v[64:79]\n"
"ds_read_b128 a[192:195], v137 offset:18432\n"
"v_exp_f32 v234, v234\n"
"v_exp_f32 v235, v235\n"
"v_mul_f32 v28, v20, v28\n"
"v_mul_f32 v29, v21, v29\n"
"v_mfma_f32_32x32x16_bf16 v[80:95], a[64:67], a[164:167], v[80:95]\n"
"s_add_u32 m0, s88, 0\n"
"ds_read_b128 a[196:199], v137 offset:19456\n"
"buffer_load_dwordx4 v134, s[32:35], s99 offen offset:1024 lds\n"
"v_mul_f32 v30, v22, v30\n"
"v_mfma_f32_32x32x16_bf16 v[64:79], a[68:71], a[168:171], v[64:79]\n"
"ds_read_b128 a[200:203], v138 offset:18432\n"
"v_mul_f32 v31, v23, v31\n"
"v_fma_f32 v232, v232, v147, v147\n"
"v_fma_f32 v233, v233, v147, v147\n"
"v_fma_f32 v234, v234, v147, v147\n"
"v_fma_f32 v235, v235, v147, v147\n"
"v_mfma_f32_32x32x16_bf16 v[80:95], a[68:71], a[172:175], v[80:95]\n"
"s_waitcnt lgkmcnt(3)\n"
"ds_read_b128 a[204:207], v138 offset:19456\n"
"buffer_load_dwordx4 v135, s[32:35], s99 offen offset:2048 lds\n"
"v_mfma_f32_32x32x16_bf16 v[64:79], a[72:75], a[176:179], v[64:79]\n"
"ds_read_b128 a[160:163], v139 offset:18432\n"
"ds_read_b128 a[164:167], v139 offset:19456\n"
"v_rcp_f32 v232, v232\n"
"v_rcp_f32 v233, v233\n"
"v_mfma_f32_32x32x16_bf16 v[80:95], a[72:75], a[180:183], v[80:95]\n"
"ds_read_b128 a[168:171], v140 offset:18432\n"
"buffer_load_dwordx4 v136, s[32:35], s99 offen offset:3072 lds\n"
"v_mfma_f32_32x32x16_bf16 v[64:79], a[76:79], a[184:187], v[64:79]\n"
"ds_read_b128 a[172:175], v140 offset:19456\n"
"v_rcp_f32 v234, v234\n"
"v_rcp_f32 v235, v235\n"
"v_mul_f32 v28, v28, v232\n"
"v_mul_f32 v29, v29, v233\n"
"v_mfma_f32_32x32x16_bf16 v[80:95], a[76:79], a[188:191], v[80:95]\n"
"s_waitcnt lgkmcnt(4)\n"
"v_mul_f32 v30, v30, v234\n"
"v_mul_f32 v31, v31, v235\n"
"v_cvt_pk_bf16_f32 v18, v28, v29\n"
"v_cvt_pk_bf16_f32 v19, v30, v31\n"
"buffer_store_dwordx4 v[16:19], v145, s[24:27], s75 offen offset:512 sc1\n"
"s_waitcnt vmcnt(14)\n"
"s_waitcnt lgkmcnt(0)\n"
"s_barrier\n"
"v_mfma_f32_32x32x16_bf16 v[64:79], a[80:83], a[192:195], v[64:79]\n"
"s_add_u32 m0, s65, 0x1bf00\n"
"ds_read_b128 a[176:179], v137 offset:32768\n"
"buffer_load_dwordx4 v132, s[16:19], s74 offen offset:256 lds\n"
"v_mfma_f32_32x32x16_bf16 v[80:95], a[80:83], a[196:199], v[80:95]\n"
"ds_read_b128 a[180:183], v137 offset:33792\n"
"v_mul_f32 v232, v32, v146\n"
"v_mul_f32 v233, v33, v146\n"
"v_mul_f32 v234, v34, v146\n"
"v_mul_f32 v235, v35, v146\n"
"v_exp_f32 v232, v232\n"
"v_mfma_f32_32x32x16_bf16 v[64:79], a[84:87], a[200:203], v[64:79]\n"
"s_add_u32 m0, s65, 0x1c300\n"
"ds_read_b128 a[184:187], v138 offset:32768\n"
"buffer_load_dwordx4 v132, s[20:23], s74 offen offset:256 lds\n"
"v_mfma_f32_32x32x16_bf16 v[80:95], a[84:87], a[204:207], v[80:95]\n"
"ds_read_b128 a[188:191], v138 offset:33792\n"
"v_exp_f32 v233, v233\n"
"v_exp_f32 v234, v234\n"
"v_exp_f32 v235, v235\n"
"v_mfma_f32_32x32x16_bf16 v[64:79], a[88:91], a[160:163], v[64:79]\n"
"s_add_u32 m0, s65, 0x1c680\n"
"ds_read_b128 a[192:195], v139 offset:32768\n"
"buffer_load_dwordx4 v132, s[16:19], s74 offen offset:384 lds\n"
"v_mul_f32 v40, v32, v40\n"
"v_mfma_f32_32x32x16_bf16 v[80:95], a[88:91], a[164:167], v[80:95]\n"
"ds_read_b128 a[196:199], v139 offset:33792\n"
"v_mul_f32 v41, v33, v41\n"
"v_mul_f32 v42, v34, v42\n"
"v_mul_f32 v43, v35, v43\n"
"v_fma_f32 v232, v232, v147, v147\n"
"v_fma_f32 v233, v233, v147, v147\n"
"v_fma_f32 v234, v234, v147, v147\n"
"v_mfma_f32_32x32x16_bf16 v[64:79], a[92:95], a[168:171], v[64:79]\n"
"ds_read_b128 a[200:203], v140 offset:32768\n"
"v_fma_f32 v235, v235, v147, v147\n"
"v_rcp_f32 v232, v232\n"
"v_rcp_f32 v233, v233\n"
"v_mfma_f32_32x32x16_bf16 v[80:95], a[92:95], a[172:175], v[80:95]\n"
"s_waitcnt lgkmcnt(3)\n"
"s_add_u32 m0, s65, 0x1ca80\n"
"ds_read_b128 a[204:207], v140 offset:33792\n"
"buffer_load_dwordx4 v132, s[20:23], s74 offen offset:384 lds\n"
"v_mfma_f32_32x32x16_bf16 v[64:79], a[96:99], a[176:179], v[64:79]\n"
"ds_read_b128 a[160:163], v137 offset:34816\n"
"v_rcp_f32 v234, v234\n"
"v_rcp_f32 v235, v235\n"
"v_mul_f32 v40, v40, v232\n"
"v_mul_f32 v41, v41, v233\n"
"v_mfma_f32_32x32x16_bf16 v[80:95], a[96:99], a[180:183], v[80:95]\n"
"s_add_u32 m0, s88, 0x1000\n"
"ds_read_b128 a[164:167], v137 offset:35840\n"
"buffer_load_dwordx4 v129, s[32:35], s99 offen lds\n"
"v_mul_f32 v42, v42, v234\n"
"v_mfma_f32_32x32x16_bf16 v[64:79], a[100:103], a[184:187], v[64:79]\n"
"ds_read_b128 a[168:171], v138 offset:34816\n"
"v_mul_f32 v43, v43, v235\n"
"v_cvt_pk_bf16_f32 v32, v40, v41\n"
"v_cvt_pk_bf16_f32 v33, v42, v43\n"
"v_mul_f32 v232, v36, v146\n"
"v_mul_f32 v233, v37, v146\n"
"v_mul_f32 v234, v38, v146\n"
"v_mfma_f32_32x32x16_bf16 v[80:95], a[100:103], a[188:191], v[80:95]\n"
"s_waitcnt lgkmcnt(3)\n"
"s_lshl_b32 s99, s1, 7\n"
"s_add_u32 s99, s99, 0x100\n"
"s_add_u32 m0, s88, 0x5000\n"
"ds_read_b128 a[172:175], v138 offset:35840\n"
"buffer_load_dwordx4 v133, s[32:35], s99 offen lds\n"
"v_mfma_f32_32x32x16_bf16 v[64:79], a[104:107], a[192:195], v[64:79]\n"
"ds_read_b128 a[176:179], v139 offset:34816\n"
"ds_read_b128 a[180:183], v139 offset:35840\n"
"v_mul_f32 v235, v39, v146\n"
"v_exp_f32 v232, v232\n"
"v_exp_f32 v233, v233\n"
"v_mfma_f32_32x32x16_bf16 v[80:95], a[104:107], a[196:199], v[80:95]\n"
"ds_read_b128 a[184:187], v140 offset:34816\n"
"buffer_load_dwordx4 v134, s[32:35], s99 offen offset:1024 lds\n"
"v_mfma_f32_32x32x16_bf16 v[64:79], a[108:111], a[200:203], v[64:79]\n"
"ds_read_b128 a[188:191], v140 offset:35840\n"
"v_exp_f32 v234, v234\n"
"v_exp_f32 v235, v235\n"
"v_mul_f32 v44, v36, v44\n"
"v_mul_f32 v45, v37, v45\n"
"v_mfma_f32_32x32x16_bf16 v[80:95], a[108:111], a[204:207], v[80:95]\n"
"s_waitcnt lgkmcnt(4)\n"
"v_mul_f32 v46, v38, v46\n"
"v_mul_f32 v47, v39, v47\n"
"v_fma_f32 v232, v232, v147, v147\n"
"v_fma_f32 v233, v233, v147, v147\n"
"v_fma_f32 v234, v234, v147, v147\n"
"v_fma_f32 v235, v235, v147, v147\n"
"s_waitcnt vmcnt(16)\n"
"s_waitcnt lgkmcnt(0)\n"
"s_barrier\n"
"v_mfma_f32_32x32x16_bf16 v[64:79], a[112:115], a[160:163], v[64:79]\n"
"s_add_u32 m0, s65, 0x1fe00\n"
"ds_read_b128 a[192:195], v137 offset:49152\n"
"buffer_load_dwordx4 v132, s[16:19], s74 offen offset:512 lds\n"
"v_mfma_f32_32x32x16_bf16 v[80:95], a[112:115], a[164:167], v[80:95]\n"
"ds_read_b128 a[196:199], v137 offset:50176\n"
"v_rcp_f32 v232, v232\n"
"v_rcp_f32 v233, v233\n"
"v_rcp_f32 v234, v234\n"
"v_mfma_f32_32x32x16_bf16 v[64:79], a[116:119], a[168:171], v[64:79]\n"
"s_add_u32 m0, s65, 0x20200\n"
"ds_read_b128 a[200:203], v138 offset:49152\n"
"buffer_load_dwordx4 v132, s[20:23], s74 offen offset:512 lds\n"
"v_mfma_f32_32x32x16_bf16 v[80:95], a[116:119], a[172:175], v[80:95]\n"
"ds_read_b128 a[204:207], v138 offset:50176\n"
"v_rcp_f32 v235, v235\n"
"v_mul_f32 v44, v44, v232\n"
"v_mul_f32 v45, v45, v233\n"
"v_mul_f32 v46, v46, v234\n"
"v_mul_f32 v47, v47, v235\n"
"v_mfma_f32_32x32x16_bf16 v[64:79], a[120:123], a[176:179], v[64:79]\n"
"s_add_u32 m0, s65, 0x20580\n"
"ds_read_b128 a[160:163], v139 offset:49152\n"
"buffer_load_dwordx4 v132, s[16:19], s74 offen offset:640 lds\n"
"v_cvt_pk_bf16_f32 v34, v44, v45\n"
"v_mfma_f32_32x32x16_bf16 v[80:95], a[120:123], a[180:183], v[80:95]\n"
"ds_read_b128 a[164:167], v139 offset:50176\n"
"v_cvt_pk_bf16_f32 v35, v46, v47\n"
"buffer_store_dwordx4 v[32:35], v145, s[24:27], s75 offen offset:1024 sc1\n"
"v_mfma_f32_32x32x16_bf16 v[64:79], a[124:127], a[184:187], v[64:79]\n"
"ds_read_b128 a[168:171], v140 offset:49152\n"
"v_mul_f32 v232, v48, v146\n"
"v_mul_f32 v233, v49, v146\n"
"v_mul_f32 v234, v50, v146\n"
"v_mul_f32 v235, v51, v146\n"
"v_exp_f32 v232, v232\n"
"v_mfma_f32_32x32x16_bf16 v[80:95], a[124:127], a[188:191], v[80:95]\n"
"s_add_u32 m0, s65, 0x20980\n"
"ds_read_b128 a[172:175], v140 offset:50176\n"
"buffer_load_dwordx4 v132, s[20:23], s74 offen offset:640 lds\n"
"v_mfma_f32_32x32x16_bf16 v[64:79], v[148:151], v[160:163], v[64:79]\n"
"v_exp_f32 v233, v233\n"
"v_exp_f32 v234, v234\n"
"v_exp_f32 v235, v235\n"
"v_mul_f32 v56, v48, v56\n"
"v_mfma_f32_32x32x16_bf16 v[80:95], v[148:151], v[162:165], v[80:95]\n"
"s_waitcnt lgkmcnt(4)\n"
"v_mul_f32 v57, v49, v57\n"
"v_mul_f32 v58, v50, v58\n"
"v_mul_f32 v59, v51, v59\n"
"v_fma_f32 v232, v232, v147, v147\n"
"v_fma_f32 v233, v233, v147, v147\n"
"v_fma_f32 v234, v234, v147, v147\n"
"v_fma_f32 v235, v235, v147, v147\n"
"v_mfma_f32_32x32x16_bf16 v[64:79], a[128:131], a[192:195], v[64:79]\n"
"ds_read_b128 a[176:179], v137 offset:51200\n"
"v_rcp_f32 v232, v232\n"
"v_rcp_f32 v233, v233\n"
"v_rcp_f32 v234, v234\n"
"v_mfma_f32_32x32x16_bf16 v[80:95], a[128:131], a[196:199], v[80:95]\n"
"s_add_u32 m0, s88, 0x5000\n"
"ds_read_b128 a[180:183], v137 offset:52224\n"
"buffer_load_dwordx4 v135, s[32:35], s99 offen offset:2048 lds\n"
"v_mfma_f32_32x32x16_bf16 v[64:79], a[132:135], a[200:203], v[64:79]\n"
"ds_read_b128 a[184:187], v138 offset:51200\n"
"v_rcp_f32 v235, v235\n"
"v_mul_f32 v56, v56, v232\n"
"v_mul_f32 v57, v57, v233\n"
"v_mul_f32 v58, v58, v234\n"
"v_mul_f32 v59, v59, v235\n"
"v_mfma_f32_32x32x16_bf16 v[80:95], a[132:135], a[204:207], v[80:95]\n"
"s_waitcnt lgkmcnt(3)\n"
"ds_read_b128 a[188:191], v138 offset:52224\n"
"buffer_load_dwordx4 v136, s[32:35], s99 offen offset:3072 lds\n"
"v_cvt_pk_bf16_f32 v48, v56, v57\n"
"v_mfma_f32_32x32x16_bf16 v[64:79], a[136:139], a[160:163], v[64:79]\n"
"ds_read_b128 a[192:195], v139 offset:51200\n"
"ds_read_b128 a[196:199], v139 offset:52224\n"
"v_cvt_pk_bf16_f32 v49, v58, v59\n"
"v_mul_f32 v232, v52, v146\n"
"v_mul_f32 v233, v53, v146\n"
"v_mul_f32 v234, v54, v146\n"
"v_mul_f32 v235, v55, v146\n"
"v_mfma_f32_32x32x16_bf16 v[80:95], a[136:139], a[164:167], v[80:95]\n"
"s_add_u32 m0, s88, 0x6000\n"
"ds_read_b128 a[200:203], v140 offset:51200\n"
"buffer_load_dwordx4 v129, s[32:35], s99 offen lds\n"
"v_mfma_f32_32x32x16_bf16 v[64:79], a[140:143], a[168:171], v[64:79]\n"
"ds_read_b128 a[204:207], v140 offset:52224\n"
"v_exp_f32 v232, v232\n"
"v_exp_f32 v233, v233\n"
"v_exp_f32 v234, v234\n"
"v_mfma_f32_32x32x16_bf16 v[80:95], a[140:143], a[172:175], v[80:95]\n"
"s_waitcnt lgkmcnt(4)\n"
"v_exp_f32 v235, v235\n"
"v_mul_f32 v60, v52, v60\n"
"v_mul_f32 v61, v53, v61\n"
"v_mul_f32 v62, v54, v62\n"
"v_mul_f32 v63, v55, v63\n"
"v_fma_f32 v232, v232, v147, v147\n"
"s_waitcnt vmcnt(19)\n"
"s_waitcnt lgkmcnt(0)\n"
"s_barrier\n"
"v_mfma_f32_32x32x16_bf16 v[64:79], a[144:147], a[176:179], v[64:79]\n"
"s_add_u32 m0, s65, 0x23d00\n"
"ds_read_b128 a[160:163], v137\n"
"buffer_load_dwordx4 v132, s[16:19], s74 offen offset:768 lds\n"
"v_mfma_f32_32x32x16_bf16 v[80:95], a[144:147], a[180:183], v[80:95]\n"
"s_mov_b32 s87, 0x1000\n"
"ds_read_b128 a[164:167], v137 offset:1024\n"
"buffer_load_dwordx4 v[244:247], v128, s[24:27], s87 offen sc1\n"
"buffer_load_dwordx4 v[8:11], v128, s[24:27], s87 offen offset:1024 sc1\n"
"v_mfma_f32_32x32x16_bf16 v[64:79], a[148:151], a[184:187], v[64:79]\n"
"ds_read_b128 a[168:171], v138\n"
"v_fma_f32 v233, v233, v147, v147\n"
"v_fma_f32 v234, v234, v147, v147\n"
"v_fma_f32 v235, v235, v147, v147\n"
"v_rcp_f32 v232, v232\n"
"v_mfma_f32_32x32x16_bf16 v[80:95], a[148:151], a[188:191], v[80:95]\n"
"s_add_u32 m0, s65, 0x24100\n"
"ds_read_b128 a[172:175], v138 offset:1024\n"
"buffer_load_dwordx4 v132, s[20:23], s74 offen offset:768 lds\n"
"v_mfma_f32_32x32x16_bf16 v[64:79], a[152:155], a[192:195], v[64:79]\n"
"ds_read_b128 a[176:179], v139\n"
"v_rcp_f32 v233, v233\n"
"v_rcp_f32 v234, v234\n"
"v_rcp_f32 v235, v235\n"
"v_mfma_f32_32x32x16_bf16 v[80:95], a[152:155], a[196:199], v[80:95]\n"
"s_add_u32 m0, s65, 0x24480\n"
"ds_read_b128 a[180:183], v139 offset:1024\n"
"buffer_load_dwordx4 v132, s[16:19], s74 offen offset:896 lds\n"
"v_mul_f32 v60, v60, v232\n"
"v_mfma_f32_32x32x16_bf16 v[64:79], a[156:159], a[200:203], v[64:79]\n"
"ds_read_b128 a[184:187], v140\n"
"v_mul_f32 v61, v61, v233\n"
"v_mul_f32 v62, v62, v234\n"
"v_mul_f32 v63, v63, v235\n"
"v_cvt_pk_bf16_f32 v50, v60, v61\n"
"v_cvt_pk_bf16_f32 v51, v62, v63\n"
"buffer_store_dwordx4 v[48:51], v145, s[24:27], s75 offen offset:1536 sc1\n"
"v_mfma_f32_32x32x16_bf16 v[80:95], a[156:159], a[204:207], v[80:95]\n"
"s_waitcnt lgkmcnt(3)\n"
"ds_read_b128 a[188:191], v140 offset:1024\n"
"v_mfma_f32_32x32x16_bf16 v[96:111], a[0:3], a[160:163], 0\n"
"s_add_u32 m0, s65, 0x24880\n"
"ds_read_b128 a[192:195], v137 offset:2048\n"
"buffer_load_dwordx4 v132, s[20:23], s74 offen offset:896 lds\n"
"v_mfma_f32_32x32x16_bf16 v[112:127], a[0:3], a[164:167], 0\n"
"ds_read_b128 a[196:199], v137 offset:3072\n"
"v_mul_f32 v64, v64, v174\n"
"s_nop 2\n"
"v_mul_f32 v80, v80, v174\n"
"v_mul_f32 v65, v65, v175\n"
"v_mul_f32 v81, v81, v175\n"
"v_fma_f32 v238, v190, v64, v65\n"
"v_fma_f32 v239, v190, v80, v81\n"
"v_fma_f32 v238, -v191, v80, v238\n"
"v_fmac_f32 v239, v191, v64\n"
"v_mul_f32 v66, v66, v176\n"
"v_mul_f32 v82, v82, v176\n"
"v_fma_f32 v236, v190, v238, v66\n"
"v_fma_f32 v237, v190, v239, v82\n"
"v_fma_f32 v236, -v191, v239, v236\n"
"v_fmac_f32 v237, v191, v238\n"
"v_mfma_f32_32x32x16_bf16 v[96:111], a[4:7], a[168:171], v[96:111]\n"
"ds_read_b128 a[200:203], v138 offset:2048\n"
"v_mul_f32 v67, v67, v177\n"
"v_mul_f32 v83, v83, v177\n"
"v_fma_f32 v238, v190, v236, v67\n"
"v_fma_f32 v239, v190, v237, v83\n"
"v_fma_f32 v238, -v191, v237, v238\n"
"v_fmac_f32 v239, v191, v236\n"
"v_mul_f32 v68, v68, v178\n"
"v_mul_f32 v84, v84, v178\n"
"v_fma_f32 v236, v190, v238, v68\n"
"v_fma_f32 v237, v190, v239, v84\n"
"v_fma_f32 v236, -v191, v239, v236\n"
"v_fmac_f32 v237, v191, v238\n"
"v_mul_f32 v69, v69, v179\n"
"v_mul_f32 v85, v85, v179\n"
"v_mfma_f32_32x32x16_bf16 v[112:127], a[4:7], a[172:175], v[112:127]\n"
"s_waitcnt lgkmcnt(3)\n"
"ds_read_b128 a[204:207], v138 offset:3072\n"
"v_fma_f32 v238, v190, v236, v69\n"
"v_fma_f32 v239, v190, v237, v85\n"
"v_fma_f32 v238, -v191, v237, v238\n"
"v_fmac_f32 v239, v191, v236\n"
"v_mul_f32 v70, v70, v180\n"
"v_mul_f32 v86, v86, v180\n"
"v_fma_f32 v236, v190, v238, v70\n"
"v_fma_f32 v237, v190, v239, v86\n"
"v_fma_f32 v236, -v191, v239, v236\n"
"v_fmac_f32 v237, v191, v238\n"
"v_mul_f32 v71, v71, v181\n"
"v_mul_f32 v87, v87, v181\n"
"v_fma_f32 v238, v190, v236, v71\n"
"v_fma_f32 v239, v190, v237, v87\n"
"v_mfma_f32_32x32x16_bf16 v[96:111], a[8:11], a[176:179], v[96:111]\n"
"ds_read_b128 a[160:163], v139 offset:2048\n"
"ds_read_b128 a[164:167], v139 offset:3072\n"
"v_fma_f32 v238, -v191, v237, v238\n"
"v_fmac_f32 v239, v191, v236\n"
"v_mul_f32 v72, v72, v182\n"
"v_mul_f32 v88, v88, v182\n"
"v_fma_f32 v236, v190, v238, v72\n"
"v_fma_f32 v237, v190, v239, v88\n"
"v_fma_f32 v236, -v191, v239, v236\n"
"v_fmac_f32 v237, v191, v238\n"
"v_mul_f32 v73, v73, v183\n"
"v_mul_f32 v89, v89, v183\n"
"v_fma_f32 v238, v190, v236, v73\n"
"v_fma_f32 v239, v190, v237, v89\n"
"v_fma_f32 v238, -v191, v237, v238\n"
"v_mfma_f32_32x32x16_bf16 v[112:127], a[8:11], a[180:183], v[112:127]\n"
"ds_read_b128 a[168:171], v140 offset:2048\n"
"v_fmac_f32 v239, v191, v236\n"
"v_mul_f32 v74, v74, v184\n"
"v_mul_f32 v90, v90, v184\n"
"v_fma_f32 v236, v190, v238, v74\n"
"v_fma_f32 v237, v190, v239, v90\n"
"v_fma_f32 v236, -v191, v239, v236\n"
"v_fmac_f32 v237, v191, v238\n"
"v_mul_f32 v75, v75, v185\n"
"v_mul_f32 v91, v91, v185\n"
"v_fma_f32 v238, v190, v236, v75\n"
"v_fma_f32 v239, v190, v237, v91\n"
"v_fma_f32 v238, -v191, v237, v238\n"
"v_fmac_f32 v239, v191, v236\n"
"v_mul_f32 v76, v76, v186\n"
"v_mfma_f32_32x32x16_bf16 v[96:111], a[12:15], a[184:187], v[96:111]\n"
"ds_read_b128 a[172:175], v140 offset:3072\n"
"v_mul_f32 v92, v92, v186\n"
"v_fma_f32 v236, v190, v238, v76\n"
"v_fma_f32 v237, v190, v239, v92\n"
"v_fma_f32 v236, -v191, v239, v236\n"
"v_fmac_f32 v237, v191, v238\n"
"v_mul_f32 v77, v77, v187\n"
"v_mul_f32 v93, v93, v187\n"
"v_fma_f32 v238, v190, v236, v77\n"
"v_fma_f32 v239, v190, v237, v93\n"
"v_fma_f32 v238, -v191, v237, v238\n"
"v_fmac_f32 v239, v191, v236\n"
"v_mul_f32 v78, v78, v188\n"
"v_mul_f32 v94, v94, v188\n"
"v_fma_f32 v236, v190, v238, v78\n"
"v_mfma_f32_32x32x16_bf16 v[112:127], a[12:15], a[188:191], v[112:127]\n"
"s_waitcnt lgkmcnt(4)\n"
"v_fma_f32 v237, v190, v239, v94\n"
"v_fma_f32 v236, -v191, v239, v236\n"
"v_fmac_f32 v237, v191, v238\n"
"v_mul_f32 v79, v79, v189\n"
"v_mul_f32 v95, v95, v189\n"
"v_fma_f32 v238, v190, v236, v79\n"
"v_fma_f32 v239, v190, v237, v95\n"
"v_fma_f32 v238, -v191, v237, v238\n"
"v_fmac_f32 v239, v191, v236\n"
"ds_write_b64 v214, v[238:239] offset:1792\n"
"s_waitcnt vmcnt(18)\n"
"s_waitcnt lgkmcnt(0)\n"
"s_barrier\n"
"v_mfma_f32_32x32x16_bf16 v[96:111], a[16:19], a[192:195], v[96:111]\n"
"s_add_u32 m0, s65, 0x17c00\n"
"ds_read_b128 a[176:179], v137 offset:16384\n"
"buffer_load_dwordx4 v132, s[16:19], s74 offen offset:1024 lds\n"
"ds_read_b64 v[16:17], v214\n"
"ds_read_b64 v[18:19], v214 offset:256\n"
"v_mfma_f32_32x32x16_bf16 v[112:127], a[16:19], a[196:199], v[112:127]\n"
"ds_read_b128 a[180:183], v137 offset:17408\n"
"ds_read_b64 v[20:21], v214 offset:512\n"
"ds_read_b64 v[22:23], v214 offset:768\n"
"ds_read_b64 v[24:25], v214 offset:1024\n"
"ds_read_b64 v[26:27], v214 offset:1280\n"
"ds_read_b64 v[28:29], v214 offset:1536\n"
"s_waitcnt lgkmcnt(6)\n"
"v_fmac_f32 v18, v192, v16\n"
"v_fmac_f32 v19, v192, v17\n"
"v_fma_f32 v18, -v193, v17, v18\n"
"v_fmac_f32 v19, v193, v16\n"
"s_waitcnt lgkmcnt(4)\n"
"v_fmac_f32 v20, v192, v18\n"
"v_fmac_f32 v21, v192, v19\n"
"v_fma_f32 v20, -v193, v19, v20\n"
"v_fmac_f32 v21, v193, v18\n"
"s_waitcnt lgkmcnt(3)\n"
"v_fmac_f32 v22, v192, v20\n"
"v_mfma_f32_32x32x16_bf16 v[96:111], a[20:23], a[200:203], v[96:111]\n"
"ds_read_b128 a[184:187], v138 offset:16384\n"
"v_fmac_f32 v23, v192, v21\n"
"v_fma_f32 v22, -v193, v21, v22\n"
"v_fmac_f32 v23, v193, v20\n"
"s_waitcnt lgkmcnt(3)\n"
"v_fmac_f32 v24, v192, v22\n"
"v_fmac_f32 v25, v192, v23\n"
"v_fma_f32 v24, -v193, v23, v24\n"
"v_fmac_f32 v25, v193, v22\n"
"s_waitcnt lgkmcnt(2)\n"
"v_fmac_f32 v26, v192, v24\n"
"v_fmac_f32 v27, v192, v25\n"
"v_fma_f32 v26, -v193, v25, v26\n"
"v_fmac_f32 v27, v193, v24\n"
"s_waitcnt lgkmcnt(1)\n"
"v_fmac_f32 v28, v192, v26\n"
"v_fmac_f32 v29, v192, v27\n"
"v_fma_f32 v28, -v193, v27, v28\n"
"v_mfma_f32_32x32x16_bf16 v[112:127], a[20:23], a[204:207], v[112:127]\n"
"s_add_u32 m0, s65, 0x18000\n"
"ds_read_b128 a[188:191], v138 offset:17408\n"
"buffer_load_dwordx4 v132, s[20:23], s74 offen offset:1024 lds\n"
"v_fmac_f32 v29, v193, v26\n"
"v_mov_b32 v240, v28\n"
"v_mov_b32 v241, v29\n"
"v_fma_f32 v16, v192, v28, v238\n"
"v_fma_f32 v18, v192, v29, v239\n"
"v_fma_f32 v16, -v193, v29, v16\n"
"v_fmac_f32 v18, v193, v28\n"
"v_mov_b32 v17, s76\n"
"v_mov_b32 v19, s78\n"
"s_lshl_b32 s80, s93, 9\n"
"s_add_u32 s80, s80, 0x20000\n"
"buffer_store_dwordx4 v[16:19], v216, s[24:27], s80 offen sc1\n"
"v_mfma_f32_32x32x16_bf16 v[96:111], a[24:27], a[160:163], v[96:111]\n"
"ds_read_b128 a[192:195], v139 offset:16384\n"
"v_mul_f32 v242, v200, v64\n"
"v_fma_f32 v242, -v201, v80, v242\n"
"v_mul_f32 v80, v200, v80\n"
"v_fmac_f32 v80, v201, v64\n"
"v_mul_f32 v64, v200, v65\n"
"v_fma_f32 v64, -v201, v81, v64\n"
"v_mul_f32 v81, v200, v81\n"
"v_fmac_f32 v81, v201, v65\n"
"v_mul_f32 v65, v200, v66\n"
"v_fma_f32 v65, -v201, v82, v65\n"
"v_mul_f32 v82, v200, v82\n"
"v_fmac_f32 v82, v201, v66\n"
"v_mul_f32 v66, v200, v67\n"
"v_fma_f32 v66, -v201, v83, v66\n"
"v_mfma_f32_32x32x16_bf16 v[112:127], a[24:27], a[164:167], v[112:127]\n"
"s_add_u32 m0, s65, 0x18380\n"
"ds_read_b128 a[196:199], v139 offset:17408\n"
"buffer_load_dwordx4 v132, s[16:19], s74 offen offset:1152 lds\n"
"v_mul_f32 v83, v200, v83\n"
"v_fmac_f32 v83, v201, v67\n"
"v_mul_f32 v67, v200, v68\n"
"v_fma_f32 v67, -v201, v84, v67\n"
"v_mul_f32 v84, v200, v84\n"
"v_fmac_f32 v84, v201, v68\n"
"v_mul_f32 v68, v200, v69\n"
"v_fma_f32 v68, -v201, v85, v68\n"
"v_mul_f32 v85, v200, v85\n"
"v_mfma_f32_32x32x16_bf16 v[96:111], a[28:31], a[168:171], v[96:111]\n"
"ds_read_b128 a[200:203], v140 offset:16384\n"
"v_fmac_f32 v85, v201, v69\n"
"v_mul_f32 v69, v200, v70\n"
"v_fma_f32 v69, -v201, v86, v69\n"
"v_mul_f32 v86, v200, v86\n"
"v_fmac_f32 v86, v201, v70\n"
"v_mul_f32 v70, v200, v71\n"
"v_fma_f32 v70, -v201, v87, v70\n"
"v_mul_f32 v87, v200, v87\n"
"v_fmac_f32 v87, v201, v71\n"
"v_mul_f32 v71, v200, v72\n"
"v_fma_f32 v71, -v201, v88, v71\n"
"v_mul_f32 v88, v200, v88\n"
"v_fmac_f32 v88, v201, v72\n"
"v_mul_f32 v72, v200, v73\n"
"v_mfma_f32_32x32x16_bf16 v[112:127], a[28:31], a[172:175], v[112:127]\n"
"s_waitcnt lgkmcnt(3)\n"
"ds_read_b128 a[204:207], v140 offset:17408\n"
"v_fma_f32 v72, -v201, v89, v72\n"
"v_mul_f32 v89, v200, v89\n"
"v_fmac_f32 v89, v201, v73\n"
"v_mul_f32 v73, v200, v74\n"
"v_fma_f32 v73, -v201, v90, v73\n"
"v_mul_f32 v90, v200, v90\n"
"v_fmac_f32 v90, v201, v74\n"
"v_mul_f32 v74, v200, v75\n"
"v_fma_f32 v74, -v201, v91, v74\n"
"v_mul_f32 v91, v200, v91\n"
"v_fmac_f32 v91, v201, v75\n"
"v_mul_f32 v75, v200, v76\n"
"v_fma_f32 v75, -v201, v92, v75\n"
"v_mul_f32 v92, v200, v92\n"
"v_mfma_f32_32x32x16_bf16 v[96:111], a[32:35], a[176:179], v[96:111]\n"
"s_add_u32 m0, s65, 0x18780\n"
"ds_read_b128 a[160:163], v137 offset:18432\n"
"buffer_load_dwordx4 v132, s[20:23], s74 offen offset:1152 lds\n"
"v_fmac_f32 v92, v201, v76\n"
"v_mfma_f32_32x32x16_bf16 v[112:127], a[32:35], a[180:183], v[112:127]\n"
"ds_read_b128 a[164:167], v137 offset:19456\n"
"v_mul_f32 v76, v200, v77\n"
"v_fma_f32 v76, -v201, v93, v76\n"
"v_mul_f32 v93, v200, v93\n"
"v_fmac_f32 v93, v201, v77\n"
"v_mul_f32 v77, v200, v78\n"
"v_fma_f32 v77, -v201, v94, v77\n"
"v_mfma_f32_32x32x16_bf16 v[96:111], a[36:39], a[184:187], v[96:111]\n"
"ds_read_b128 a[168:171], v138 offset:18432\n"
"v_mul_f32 v94, v200, v94\n"
"v_fmac_f32 v94, v201, v78\n"
"v_mul_f32 v78, v200, v79\n"
"v_fma_f32 v78, -v201, v95, v78\n"
"v_mul_f32 v95, v200, v95\n"
"v_fmac_f32 v95, v201, v79\n"
"v_mfma_f32_32x32x16_bf16 v[112:127], a[36:39], a[188:191], v[112:127]\n"
"s_waitcnt lgkmcnt(3)\n"
"ds_read_b128 a[172:175], v138 offset:19456\n"
"v_mfma_f32_32x32x16_bf16 v[96:111], a[40:43], a[192:195], v[96:111]\n"
"ds_read_b128 a[176:179], v139 offset:18432\n"
"ds_read_b128 a[180:183], v139 offset:19456\n"
"s_waitcnt vmcnt(10)\n"
"v_cmp_ne_u64 s[80:81], s[76:77], v[244:245]\n"
"v_cmp_ne_u64 s[82:83], s[76:77], v[246:247]\n"
"s_waitcnt vmcnt(9)\n"
"v_cmp_ne_u64 s[84:85], s[76:77], v[8:9]\n"
"v_cmp_ne_u64 vcc, s[76:77], v[10:11]\n"
"s_or_b64 s[80:81], s[80:81], s[82:83]\n"
"s_or_b64 s[84:85], s[84:85], vcc\n"
"s_and_b32 s84, s84, 0xffffff\n"
"s_mov_b32 s85, 0\n"
"s_or_b64 s[80:81], s[80:81], s[84:85]\n"
"s_cbranch_scc1 L%=_f0S_slow\n"
"L%=_f0S_back:\n"
"v_mfma_f32_32x32x16_bf16 v[112:127], a[40:43], a[196:199], v[112:127]\n"
"ds_read_b128 a[184:187], v140 offset:18432\n"
"v_mfma_f32_32x32x16_bf16 v[96:111], a[44:47], a[200:203], v[96:111]\n"
"ds_read_b128 a[188:191], v140 offset:19456\n"
"s_waitcnt vmcnt(6)\n"
"s_add_u32 s80, s76, 1\n"
"v_mov_b32 v230, s80\n"
"s_lshl_b32 s80, s66, 2\n"
"s_add_u32 s80, s80, s64\n"
"s_lshl_b32 s80, s80, 4\n"
"s_add_u32 s80, s80, s67\n"
"s_lshl_b32 s80, s80, 3\n"
"s_add_u32 s80, s80, 0x60000\n"
"buffer_store_dwordx2 v[230:231], v229, s[24:27], s80 offen sc1\n"
"v_mfma_f32_32x32x16_bf16 v[112:127], a[44:47], a[204:207], v[112:127]\n"
"s_waitcnt lgkmcnt(4)\n"
"s_waitcnt lgkmcnt(0)\n"
"s_barrier\n"
"v_mfma_f32_32x32x16_bf16 v[96:111], a[48:51], a[160:163], v[96:111]\n"
"s_mul_i32 s99, s1, 0x6e000\n"
"s_add_u32 s99, s99, s90\n"
"s_add_u32 s99, s99, 0\n"
"s_add_u32 m0, s88, 0xa000\n"
"ds_read_b128 a[192:195], v137 offset:32768\n"
"buffer_load_dwordx4 v128, s[24:27], s99 offen sc1 lds\n"
"v_mfma_f32_32x32x16_bf16 v[112:127], a[48:51], a[164:167], v[112:127]\n"
"ds_read_b128 a[196:199], v137 offset:33792\n"
"v_mfma_f32_32x32x16_bf16 v[96:111], a[52:55], a[168:171], v[96:111]\n"
"ds_read_b128 a[200:203], v138 offset:32768\n"
"buffer_load_dwordx4 v128, s[24:27], s99 offen offset:1024 sc1 lds\n"
"v_mfma_f32_32x32x16_bf16 v[112:127], a[52:55], a[172:175], v[112:127]\n"
"ds_read_b128 a[204:207], v138 offset:33792\n"
"v_mfma_f32_32x32x16_bf16 v[96:111], a[56:59], a[176:179], v[96:111]\n"
"ds_read_b128 a[160:163], v139 offset:32768\n"
"buffer_load_dwordx4 v128, s[24:27], s99 offen offset:2048 sc1 lds\n"
"v_mfma_f32_32x32x16_bf16 v[112:127], a[56:59], a[180:183], v[112:127]\n"
"ds_read_b128 a[164:167], v139 offset:33792\n"
"v_mfma_f32_32x32x16_bf16 v[96:111], a[60:63], a[184:187], v[96:111]\n"
"ds_read_b128 a[168:171], v140 offset:32768\n"
"buffer_load_dwordx4 v128, s[24:27], s99 offen offset:3072 sc1 lds\n"
"v_mfma_f32_32x32x16_bf16 v[112:127], a[60:63], a[188:191], v[112:127]\n"
"s_waitcnt lgkmcnt(3)\n"
"ds_read_b128 a[172:175], v140 offset:33792\n"
"v_mfma_f32_32x32x16_bf16 v[96:111], a[64:67], a[192:195], v[96:111]\n"
"s_add_u32 m0, s88, 0xb000\n"
"ds_read_b128 a[176:179], v137 offset:34816\n"
"buffer_load_dwordx4 v130, s[24:27], s99 offen sc1 lds\n"
"v_mfma_f32_32x32x16_bf16 v[112:127], a[64:67], a[196:199], v[112:127]\n"
"ds_read_b128 a[180:183], v137 offset:35840\n"
"v_mfma_f32_32x32x16_bf16 v[96:111], a[68:71], a[200:203], v[96:111]\n"
"s_mul_i32 s99, s1, 0x6e000\n"
"s_add_u32 s99, s99, s90\n"
"s_add_u32 s99, s99, 0x28000\n"
"s_add_u32 m0, s88, 0xf000\n"
"ds_read_b128 a[184:187], v138 offset:34816\n"
"buffer_load_dwordx4 v128, s[24:27], s99 offen sc1 lds\n"
"v_mfma_f32_32x32x16_bf16 v[112:127], a[68:71], a[204:207], v[112:127]\n"
"s_waitcnt lgkmcnt(3)\n"
"ds_read_b128 a[188:191], v138 offset:35840\n"
"v_mfma_f32_32x32x16_bf16 v[96:111], a[72:75], a[160:163], v[96:111]\n"
"s_lshl_b32 s80, s98, 2\n"
"ds_read_b128 a[192:195], v139 offset:34816\n"
"ds_read_b128 a[196:199], v139 offset:35840\n"
"buffer_load_dwordx4 v128, s[24:27], s99 offen offset:1024 sc1 lds\n"
"s_add_u32 s80, s80, s64\n"
"s_lshl_b32 s80, s80, 7\n"
"s_add_u32 s80, s80, 0x60000\n"
"v_and_b32 v244, 15, v253\n"
"v_lshl_add_u32 v244, v244, 3, s80\n"
"v_mfma_f32_32x32x16_bf16 v[112:127], a[72:75], a[164:167], v[112:127]\n"
"ds_read_b128 a[200:203], v140 offset:34816\n"
"buffer_load_dwordx2 v[246:247], v244, s[24:27], 0 offen sc1\n"
"v_mfma_f32_32x32x16_bf16 v[96:111], a[76:79], a[168:171], v[96:111]\n"
"ds_read_b128 a[204:207], v140 offset:35840\n"
"buffer_load_dwordx4 v128, s[24:27], s99 offen offset:2048 sc1 lds\n"
"v_mfma_f32_32x32x16_bf16 v[112:127], a[76:79], a[172:175], v[112:127]\n"
"s_waitcnt lgkmcnt(4)\n"
"s_waitcnt vmcnt(15)\n"
"s_waitcnt lgkmcnt(0)\n"
"s_barrier\n"
"v_mfma_f32_32x32x16_bf16 v[96:111], a[80:83], a[176:179], v[96:111]\n"
"ds_read_b128 a[160:163], v137 offset:49152\n"
"buffer_load_dwordx4 v128, s[24:27], s99 offen offset:3072 sc1 lds\n"
"v_mfma_f32_32x32x16_bf16 v[112:127], a[80:83], a[180:183], v[112:127]\n"
"ds_read_b128 a[164:167], v137 offset:50176\n"
"v_mfma_f32_32x32x16_bf16 v[96:111], a[84:87], a[184:187], v[96:111]\n"
"s_add_u32 m0, s88, 0x10000\n"
"ds_read_b128 a[168:171], v138 offset:49152\n"
"buffer_load_dwordx4 v130, s[24:27], s99 offen sc1 lds\n"
"s_lshl_b32 s68, s93, 9\n"
"s_add_u32 s68, s68, 0x20000\n"
"buffer_load_dwordx4 v[16:19], v217, s[24:27], s68 offen sc1\n"
"v_mfma_f32_32x32x16_bf16 v[112:127], a[84:87], a[188:191], v[112:127]\n"
"ds_read_b128 a[172:175], v138 offset:50176\n"
"buffer_load_dwordx4 v[20:23], v218, s[24:27], s68 offen sc1\n"
"v_mfma_f32_32x32x16_bf16 v[96:111], a[88:91], a[192:195], v[96:111]\n"
"s_mul_i32 s99, s1, 0x6e000\n"
"s_add_u32 s99, s99, s90\n"
"s_add_u32 s99, s99, 0x50000\n"
"s_mul_i32 s79, s1, 0xfffc4000\n"
"s_add_u32 s79, s79, 0x14a000\n"
"s_cmp_ge_u32 s64, 3\n"
"s_cselect_b32 s79, s79, 0\n"
"s_add_u32 s99, s99, s79\n"
"s_add_u32 m0, s88, 0x1e000\n"
"ds_read_b128 a[176:179], v139 offset:49152\n"
"buffer_load_dwordx4 v128, s[24:27], s99 offen sc1 lds\n"
"v_mfma_f32_32x32x16_bf16 v[112:127], a[88:91], a[196:199], v[112:127]\n"
"ds_read_b128 a[180:183], v139 offset:50176\n"
"v_mfma_f32_32x32x16_bf16 v[96:111], a[92:95], a[200:203], v[96:111]\n"
"ds_read_b128 a[184:187], v140 offset:49152\n"
"v_mfma_f32_32x32x16_bf16 v[112:127], a[92:95], a[204:207], v[112:127]\n"
"s_waitcnt lgkmcnt(3)\n"
"ds_read_b128 a[188:191], v140 offset:50176\n"
"buffer_load_dwordx4 v128, s[24:27], s99 offen offset:1024 sc1 lds\n"
"v_mfma_f32_32x32x16_bf16 v[96:111], a[96:99], a[160:163], v[96:111]\n"
"ds_read_b128 a[192:195], v137 offset:51200\n"
"v_mfma_f32_32x32x16_bf16 v[112:127], a[96:99], a[164:167], v[112:127]\n"
"ds_read_b128 a[196:199], v137 offset:52224\n"
"buffer_load_dwordx4 v128, s[24:27], s99 offen offset:2048 sc1 lds\n"
"v_mfma_f32_32x32x16_bf16 v[96:111], a[100:103], a[168:171], v[96:111]\n"
"ds_read_b128 a[200:203], v138 offset:51200\n"
"v_mfma_f32_32x32x16_bf16 v[112:127], a[100:103], a[172:175], v[112:127]\n"
"s_waitcnt lgkmcnt(3)\n"
"ds_read_b128 a[204:207], v138 offset:52224\n"
"buffer_load_dwordx4 v128, s[24:27], s99 offen offset:3072 sc1 lds\n"
"v_mfma_f32_32x32x16_bf16 v[96:111], a[104:107], a[176:179], v[96:111]\n"
"ds_read_b128 a[160:163], v139 offset:51200\n"
"ds_read_b128 a[164:167], v139 offset:52224\n"
"v_mfma_f32_32x32x16_bf16 v[112:127], a[104:107], a[180:183], v[112:127]\n"
"s_add_u32 m0, s88, 0x1f000\n"
"ds_read_b128 a[168:171], v140 offset:51200\n"
"buffer_load_dwordx4 v130, s[24:27], s99 offen sc1 lds\n"
"v_mfma_f32_32x32x16_bf16 v[96:111], a[108:111], a[184:187], v[96:111]\n"
"ds_read_b128 a[172:175], v140 offset:52224\n"
"v_mfma_f32_32x32x16_bf16 v[112:127], a[108:111], a[188:191], v[112:127]\n"
"s_waitcnt lgkmcnt(4)\n"
"s_add_u32 s80, s76, 1\n"
"s_waitcnt vmcnt(10)\n"
"v_subrev_u32 v245, s80, v246\n"
"v_cmp_lt_u32 vcc, 2, v245\n"
"v_cmp_ne_u32 s[82:83], s77, v247\n"
"s_nop 0\n"
"s_or_b32 s82, s82, vcc_lo\n"
"s_and_b32 s82, s82, 0x7ff\n"
"s_cbranch_scc1 L%=_f2aS_slow\n"
"L%=_f2aS_back:\n"
"s_waitcnt lgkmcnt(0)\n"
"s_barrier\n"
"v_mfma_f32_32x32x16_bf16 v[96:111], a[112:115], a[192:195], v[96:111]\n"
"s_mul_i32 s99, s1, 0x32000\n"
"s_add_u32 s99, s99, s90\n"
"s_add_u32 s99, s99, 0x1c2000\n"
"s_add_u32 m0, s88, 0x23000\n"
"ds_read_b128 a[176:179], v137\n"
"buffer_load_dwordx4 v128, s[24:27], s99 offen sc1 lds\n"
"v_mfma_f32_32x32x16_bf16 v[112:127], a[112:115], a[196:199], v[112:127]\n"
"s_lshr_b32 s82, s3, 3\n"
"ds_read_b128 a[180:183], v137 offset:1024\n"
"s_lshl_b32 s82, s82, 2\n"
"s_add_u32 s82, s82, s64\n"
"s_mul_i32 s82, s82, 5\n"
"v_min_u32 v251, 4, v253\n"
"v_add_u32 v251, s82, v251\n"
"v_lshlrev_b32 v251, 7, v251\n"
"v_min_u32 v251, L%=_end-L%=_pc-8, v251\n"
"s_getpc_b64 s[80:81]\n"
"L%=_st_ct7:\n"
"s_sub_u32 s80, s80, L%=_st_ct7-L%=_pc\n"
"s_subb_u32 s81, s81, 0\n"
"global_load_dword v251, v251, s[80:81]\n"
"v_mfma_f32_32x32x16_bf16 v[96:111], a[116:119], a[200:203], v[96:111]\n"
"ds_read_b128 a[184:187], v138\n"
"buffer_load_dwordx4 v128, s[24:27], s99 offen offset:1024 sc1 lds\n"
"v_mfma_f32_32x32x16_bf16 v[112:127], a[116:119], a[204:207], v[112:127]\n"
"ds_read_b128 a[188:191], v138 offset:1024\n"
"v_mfma_f32_32x32x16_bf16 v[96:111], a[120:123], a[160:163], v[96:111]\n"
"ds_read_b128 a[192:195], v139\n"
"buffer_load_dwordx4 v128, s[24:27], s99 offen offset:2048 sc1 lds\n"
"v_mfma_f32_32x32x16_bf16 v[112:127], a[120:123], a[164:167], v[112:127]\n"
"ds_read_b128 a[196:199], v139 offset:1024\n"
"v_mfma_f32_32x32x16_bf16 v[96:111], a[124:127], a[168:171], v[96:111]\n"
"ds_read_b128 a[200:203], v140\n"
"buffer_load_dwordx4 v128, s[24:27], s99 offen offset:3072 sc1 lds\n"
"s_waitcnt vmcnt(11)\n"
"v_xor_b32 v24, v17, v219\n"
"v_bitop3_b32 v24, v19, v220, v24 bitop3:0xbe\n"
"s_waitcnt vmcnt(10)\n"
"v_bitop3_b32 v24, v21, v221, v24 bitop3:0xbe\n"
"v_bitop3_b32 v24, v23, v222, v24 bitop3:0xbe\n"
"v_cmp_ne_u32 vcc, 0, v24\n"
"s_nop 0\n"
"s_cbranch_vccnz L%=_tS0_slow\n"
"L%=_tS0_back:\n"
"v_mfma_f32_32x32x16_bf16 v[112:127], a[124:127], a[172:175], v[112:127]\n"
"ds_read_b128 a[204:207], v140 offset:1024\n"
"v_mul_f32 v236, v196, v16\n"
"v_mul_f32 v237, v196, v18\n"
"v_fma_f32 v236, -v197, v18, v236\n"
"v_fmac_f32 v237, v197, v16\n"
"v_fmac_f32 v236, v198, v20\n"
"v_fmac_f32 v237, v198, v22\n"
"v_mfma_f32_32x32x16_bf16 v[96:111], v[148:151], v[164:167], v[96:111]\n"
"v_fma_f32 v236, -v199, v22, v236\n"
"v_fmac_f32 v237, v199, v20\n"
"ds_write_b64 v214, v[236:237] offset:3840\n"
"v_mfma_f32_32x32x16_bf16 v[112:127], v[148:151], v[166:169], v[112:127]\n"
"s_waitcnt lgkmcnt(5)\n"
"v_mfma_f32_32x32x16_bf16 v[96:111], a[128:131], a[176:179], v[96:111]\n"
"s_add_u32 m0, s88, 0x24000\n"
"ds_read_b128 a[160:163], v137 offset:2048\n"
"buffer_load_dwordx4 v130, s[24:27], s99 offen sc1 lds\n"
"v_mfma_f32_32x32x16_bf16 v[112:127], a[128:131], a[180:183], v[112:127]\n"
"ds_read_b128 a[164:167], v137 offset:3072\n"
"v_mfma_f32_32x32x16_bf16 v[96:111], a[132:135], a[184:187], v[96:111]\n"
"s_mul_i32 s89, s1, 0x2c00\n"
"s_add_u32 s89, s89, s92\n"
"s_add_u32 s89, s89, 0\n"
"s_add_u32 s74, s89, s0\n"
"ds_read_b128 a[168:171], v138 offset:2048\n"
"buffer_load_dwordx4 a[208:211], v128, s[24:27], s89 offen sc1\n"
"v_mfma_f32_32x32x16_bf16 v[112:127], a[132:135], a[188:191], v[112:127]\n"
"s_waitcnt lgkmcnt(4)\n"
"ds_read_b128 a[172:175], v138 offset:3072\n"
"v_mfma_f32_32x32x16_bf16 v[96:111], a[136:139], a[192:195], v[96:111]\n"
"ds_read_b128 a[176:179], v139 offset:2048\n"
"ds_read_b128 a[180:183], v139 offset:3072\n"
"buffer_load_dwordx4 a[212:215], v128, s[24:27], s74 offen sc1\n"
"v_mfma_f32_32x32x16_bf16 v[112:127], a[136:139], a[196:199], v[112:127]\n"
"ds_read_b128 a[184:187], v140 offset:2048\n"
"v_mfma_f32_32x32x16_bf16 v[96:111], a[140:143], a[200:203], v[96:111]\n"
"ds_read_b128 a[188:191], v140 offset:3072\n"
"buffer_load_dwordx4 a[216:219], v128, s[24:27], s89 offen offset:1024 sc1\n"
"v_mfma_f32_32x32x16_bf16 v[112:127], a[140:143], a[204:207], v[112:127]\n"
"s_waitcnt lgkmcnt(4)\n"
"s_waitcnt lgkmcnt(0)\n"
"s_barrier\n"
"v_mfma_f32_32x32x16_bf16 v[96:111], a[144:147], a[160:163], v[96:111]\n"
"ds_read_b128 a[92:95], v128 offset:40960\n"
"ds_read_b128 a[96:99], v128 offset:41984\n"
"buffer_load_dwordx4 a[220:223], v128, s[24:27], s74 offen offset:1024 sc1\n"
"v_mfma_f32_32x32x16_bf16 v[112:127], a[144:147], a[164:167], v[112:127]\n"
"ds_read_b128 a[100:103], v128 offset:43008\n"
"buffer_load_dwordx4 a[224:227], v128, s[24:27], s89 offen offset:2048 sc1\n"
"v_mfma_f32_32x32x16_bf16 v[96:111], a[148:151], a[168:171], v[96:111]\n"
"ds_read_b128 a[104:107], v128 offset:44032\n"
"buffer_load_dwordx4 a[228:231], v128, s[24:27], s74 offen offset:2048 sc1\n"
"v_mfma_f32_32x32x16_bf16 v[112:127], a[148:151], a[172:175], v[112:127]\n"
"ds_read_b128 a[108:111], v128 offset:45056\n"
"buffer_load_dwordx4 a[232:235], v128, s[24:27], s89 offen offset:3072 sc1\n"
"v_mfma_f32_32x32x16_bf16 v[96:111], a[152:155], a[176:179], v[96:111]\n"
"ds_read_b128 a[112:115], v128 offset:46080\n"
"ds_read_b128 a[116:119], v128 offset:47104\n"
"v_mfma_f32_32x32x16_bf16 v[112:127], a[152:155], a[180:183], v[112:127]\n"
"ds_read_b128 a[120:123], v128 offset:48128\n"
"v_mfma_f32_32x32x16_bf16 v[96:111], a[156:159], a[184:187], v[96:111]\n"
"ds_read_b128 a[124:127], v128 offset:49152\n"
"v_mfma_f32_32x32x16_bf16 v[112:127], a[156:159], a[188:191], v[112:127]\n"
"ds_read_b128 a[128:131], v128 offset:50176\n"
"L%=_P2S:\n"
"s_waitcnt vmcnt(5) lgkmcnt(5)\n"
"v_mfma_f32_16x16x32_bf16 v[0:3], a[208:211], a[92:95], 0\n"
"ds_read_b128 a[132:135], v128 offset:51200\n"
"v_add_u32 v137, 0x10000, v128\n"
"v_add_u32 v138, 0x20000, v128\n"
"ds_read_b64 v[152:153], v215 offset:3840\n"
"ds_read_b64 v[154:155], v215 offset:4096\n"
"ds_read_b64 v[156:157], v215 offset:4352\n"
"ds_read_b64 v[158:159], v215 offset:4608\n"
"ds_read_b64 v[160:161], v215 offset:4864\n"
"ds_read_b64 v[162:163], v215 offset:5120\n"
"ds_read_b64 v[164:165], v215 offset:5376\n"
"ds_read_b64 v[166:167], v215 offset:5632\n"
"s_waitcnt lgkmcnt(6)\n"
"v_add_f32 v152, v152, v154\n"
"v_add_f32 v153, v153, v155\n"
"s_waitcnt lgkmcnt(4)\n"
"v_add_f32 v156, v156, v158\n"
"v_mfma_f32_16x16x32_bf16 v[20:23], a[212:215], a[92:95], 0\n"
"buffer_load_dwordx4 a[236:239], v128, s[24:27], s74 offen offset:3072 sc1\n"
"v_mul_f32 v96, v96, v174\n"
"v_mul_f32 v112, v112, v174\n"
"v_mul_f32 v97, v97, v175\n"
"v_mul_f32 v113, v113, v175\n"
"v_fma_f32 v246, v202, v96, v97\n"
"v_fma_f32 v247, v202, v112, v113\n"
"v_fma_f32 v246, -v203, v112, v246\n"
"v_fmac_f32 v247, v203, v96\n"
"v_mul_f32 v98, v98, v176\n"
"v_mul_f32 v114, v114, v176\n"
"v_mfma_f32_16x16x32_bf16 v[4:7], a[208:211], a[96:99], 0\n"
"ds_read_b128 a[136:139], v128 offset:52224\n"
"v_fma_f32 v244, v202, v246, v98\n"
"v_fma_f32 v245, v202, v247, v114\n"
"v_fma_f32 v244, -v203, v247, v244\n"
"v_fmac_f32 v245, v203, v246\n"
"v_mul_f32 v99, v99, v177\n"
"v_mul_f32 v115, v115, v177\n"
"v_fma_f32 v246, v202, v244, v99\n"
"v_fma_f32 v247, v202, v245, v115\n"
"v_fma_f32 v246, -v203, v245, v246\n"
"v_fmac_f32 v247, v203, v244\n"
"v_mul_f32 v100, v100, v178\n"
"v_mul_f32 v116, v116, v178\n"
"v_fma_f32 v244, v202, v246, v100\n"
"v_mfma_f32_16x16x32_bf16 v[24:27], a[212:215], a[96:99], 0\n"
"s_mul_i32 s89, s1, 0x2c00\n"
"s_add_u32 s89, s89, s92\n"
"s_add_u32 s89, s89, 0x1000\n"
"s_add_u32 s74, s89, s0\n"
"buffer_load_dwordx4 a[240:243], v128, s[24:27], s89 offen sc1\n"
"v_fma_f32 v245, v202, v247, v116\n"
"v_fma_f32 v244, -v203, v247, v244\n"
"v_fmac_f32 v245, v203, v246\n"
"v_mul_f32 v101, v101, v179\n"
"v_mul_f32 v117, v117, v179\n"
"v_fma_f32 v246, v202, v244, v101\n"
"v_mfma_f32_16x16x32_bf16 v[8:11], a[208:211], a[100:103], 0\n"
"ds_read_b128 a[140:143], v128 offset:53248\n"
"v_fma_f32 v247, v202, v245, v117\n"
"v_fma_f32 v246, -v203, v245, v246\n"
"v_fmac_f32 v247, v203, v244\n"
"v_mul_f32 v102, v102, v180\n"
"v_mul_f32 v118, v118, v180\n"
"v_fma_f32 v244, v202, v246, v102\n"
"v_fma_f32 v245, v202, v247, v118\n"
"v_fma_f32 v244, -v203, v247, v244\n"
"v_fmac_f32 v245, v203, v246\n"
"v_mul_f32 v103, v103, v181\n"
"v_mul_f32 v119, v119, v181\n"
"v_fma_f32 v246, v202, v244, v103\n"
"v_fma_f32 v247, v202, v245, v119\n"
"v_mfma_f32_16x16x32_bf16 v[28:31], a[212:215], a[100:103], 0\n"
"buffer_load_dwordx4 a[244:247], v128, s[24:27], s74 offen sc1\n"
"v_fma_f32 v246, -v203, v245, v246\n"
"v_fmac_f32 v247, v203, v244\n"
"v_mul_f32 v104, v104, v182\n"
"v_mul_f32 v120, v120, v182\n"
"v_fma_f32 v244, v202, v246, v104\n"
"v_fma_f32 v245, v202, v247, v120\n"
"v_fma_f32 v244, -v203, v247, v244\n"
"v_fmac_f32 v245, v203, v246\n"
"v_mul_f32 v105, v105, v183\n"
"v_mul_f32 v121, v121, v183\n"
"v_mfma_f32_16x16x32_bf16 v[12:15], a[208:211], a[104:107], 0\n"
"ds_read_b128 a[144:147], v128 offset:54272\n"
"v_fma_f32 v246, v202, v244, v105\n"
"v_fma_f32 v247, v202, v245, v121\n"
"v_fma_f32 v246, -v203, v245, v246\n"
"v_fmac_f32 v247, v203, v244\n"
"v_mul_f32 v106, v106, v184\n"
"v_mul_f32 v122, v122, v184\n"
"v_fma_f32 v244, v202, v246, v106\n"
"v_fma_f32 v245, v202, v247, v122\n"
"v_fma_f32 v244, -v203, v247, v244\n"
"v_fmac_f32 v245, v203, v246\n"
"v_mul_f32 v107, v107, v185\n"
"v_mul_f32 v123, v123, v185\n"
"v_fma_f32 v246, v202, v244, v107\n"
"v_mfma_f32_16x16x32_bf16 v[32:35], a[212:215], a[104:107], 0\n"
"buffer_load_dwordx4 a[248:251], v128, s[24:27], s89 offen offset:1024 sc1\n"
"v_fma_f32 v247, v202, v245, v123\n"
"v_fma_f32 v246, -v203, v245, v246\n"
"v_fmac_f32 v247, v203, v244\n"
"v_mul_f32 v108, v108, v186\n"
"v_mul_f32 v124, v124, v186\n"
"v_fma_f32 v244, v202, v246, v108\n"
"v_fma_f32 v245, v202, v247, v124\n"
"v_fma_f32 v244, -v203, v247, v244\n"
"v_fmac_f32 v245, v203, v246\n"
"v_mul_f32 v109, v109, v187\n"
"v_mfma_f32_16x16x32_bf16 v[16:19], a[208:211], a[108:111], 0\n"
"ds_read_b128 a[148:151], v128 offset:55296\n"
"v_mul_f32 v125, v125, v187\n"
"v_fma_f32 v246, v202, v244, v109\n"
"v_fma_f32 v247, v202, v245, v125\n"
"v_fma_f32 v246, -v203, v245, v246\n"
"v_fmac_f32 v247, v203, v244\n"
"v_mul_f32 v110, v110, v188\n"
"v_mul_f32 v126, v126, v188\n"
"v_fma_f32 v244, v202, v246, v110\n"
"v_fma_f32 v245, v202, v247, v126\n"
"v_fma_f32 v244, -v203, v247, v244\n"
"v_fmac_f32 v245, v203, v246\n"
"v_mul_f32 v111, v111, v189\n"
"v_mul_f32 v127, v127, v189\n"
"v_mfma_f32_16x16x32_bf16 v[36:39], a[212:215], a[108:111], 0\n"
"s_waitcnt vmcnt(7)\n"
"v_fma_f32 v246, v202, v244, v111\n"
"v_fma_f32 v247, v202, v245, v127\n"
"v_fma_f32 v246, -v203, v245, v246\n"
"v_fmac_f32 v247, v203, v244\n"
"ds_write_b64 v214, v[246:247] offset:7936\n"
"v_add_f32 v157, v157, v159\n"
"s_waitcnt lgkmcnt(7)\n"
"v_add_f32 v160, v160, v162\n"
"v_add_f32 v161, v161, v163\n"
"s_waitcnt lgkmcnt(5)\n"
"v_add_f32 v164, v164, v166\n"
"v_add_f32 v165, v165, v167\n"
"v_add_f32 v152, v152, v156\n"
"v_add_f32 v153, v153, v157\n"
"v_mfma_f32_16x16x32_bf16 v[0:3], a[216:219], a[112:115], v[0:3]\n"
"s_lshr_b32 s80, s93, 1\n"
"ds_read_b128 a[92:95], v128 offset:56320\n"
"v_add_f32 v160, v160, v164\n"
"v_add_f32 v161, v161, v165\n"
"v_add_f32 v152, v152, v160\n"
"v_add_f32 v153, v153, v161\n"
"v_fmac_f32 v240, v194, v152\n"
"v_fmac_f32 v241, v194, v153\n"
"v_fma_f32 v240, -v195, v153, v240\n"
"v_fmac_f32 v241, v195, v152\n"
"v_mul_f32 v236, v200, v240\n"
"v_mul_f32 v237, v200, v241\n"
"v_fma_f32 v236, -v201, v241, v236\n"
"v_fmac_f32 v237, v201, v240\n"
"s_lshl_b32 s80, s80, 2\n"
"s_and_b32 s81, s93, 1\n"
"s_add_u32 s80, s80, s81\n"
"s_add_u32 s80, s80, 54\n"
"s_lshl_b32 s80, s80, 10\n"
"s_lshl_b32 s81, s66, 2\n"
"s_add_u32 s81, s81, s64\n"
"s_mul_i32 s81, s81, 0x2f000\n"
"s_add_u32 s80, s80, s81\n"
"s_add_u32 s70, s80, 0x300000\n"
"v_mfma_f32_16x16x32_bf16 v[20:23], a[220:223], a[112:115], v[20:23]\n"
"buffer_load_dwordx4 a[252:255], v128, s[24:27], s74 offen offset:1024 sc1\n"
"v_fmac_f32 v242, v190, v236\n"
"v_fmac_f32 v80, v190, v237\n"
"v_fma_f32 v242, -v191, v237, v242\n"
"v_fmac_f32 v80, v191, v236\n"
"v_fmac_f32 v64, v190, v242\n"
"v_fmac_f32 v81, v190, v80\n"
"v_fma_f32 v64, -v191, v80, v64\n"
"v_fmac_f32 v81, v191, v242\n"
"v_fmac_f32 v65, v190, v64\n"
"v_fmac_f32 v82, v190, v81\n"
"v_mfma_f32_16x16x32_bf16 v[4:7], a[216:219], a[116:119], v[4:7]\n"
"ds_read_b128 a[96:99], v128 offset:57344\n"
"v_fma_f32 v65, -v191, v81, v65\n"
"v_fmac_f32 v82, v191, v64\n"
"v_fmac_f32 v66, v190, v65\n"
"v_fmac_f32 v83, v190, v82\n"
"v_fma_f32 v66, -v191, v82, v66\n"
"v_fmac_f32 v83, v191, v65\n"
"v_fmac_f32 v67, v190, v66\n"
"v_fmac_f32 v84, v190, v83\n"
"v_fma_f32 v67, -v191, v83, v67\n"
"v_fmac_f32 v84, v191, v66\n"
"v_fmac_f32 v68, v190, v67\n"
"v_fmac_f32 v85, v190, v84\n"
"v_fma_f32 v68, -v191, v84, v68\n"
"v_mfma_f32_16x16x32_bf16 v[24:27], a[220:223], a[116:119], v[24:27]\n"
"buffer_load_dwordx4 a[0:3], v128, s[24:27], s89 offen offset:2048 sc1\n"
"v_fmac_f32 v85, v191, v67\n"
"v_fmac_f32 v69, v190, v68\n"
"v_fmac_f32 v86, v190, v85\n"
"v_fma_f32 v69, -v191, v85, v69\n"
"v_fmac_f32 v86, v191, v68\n"
"v_fmac_f32 v70, v190, v69\n"
"v_fmac_f32 v87, v190, v86\n"
"v_fma_f32 v70, -v191, v86, v70\n"
"v_fmac_f32 v87, v191, v69\n"
"v_cvt_pk_bf16_f32 v152, v242, v64\n"
"v_mfma_f32_16x16x32_bf16 v[8:11], a[216:219], a[120:123], v[8:11]\n"
"ds_read_b128 a[100:103], v128 offset:58368\n"
"v_cvt_pk_bf16_f32 v153, v65, v66\n"
"v_cvt_pk_bf16_f32 v154, v67, v68\n"
"v_cvt_pk_bf16_f32 v155, v69, v70\n"
"ds_write_b128 v227, v[152:155]\n"
"ds_read_b64_tr_b16 v[156:157], v228\n"
"ds_read_b64_tr_b16 v[158:159], v228 offset:128\n"
"v_cvt_pk_bf16_f32 v160, -v80, -v81\n"
"v_cvt_pk_bf16_f32 v161, -v82, -v83\n"
"v_cvt_pk_bf16_f32 v162, -v84, -v85\n"
"v_mfma_f32_16x16x32_bf16 v[28:31], a[220:223], a[120:123], v[28:31]\n"
"buffer_load_dwordx4 a[4:7], v128, s[24:27], s74 offen offset:2048 sc1\n"
"v_cvt_pk_bf16_f32 v163, -v86, -v87\n"
"ds_write_b128 v227, v[160:163]\n"
"ds_read_b64_tr_b16 v[164:165], v228\n"
"ds_read_b64_tr_b16 v[166:167], v228 offset:128\n"
"s_waitcnt lgkmcnt(3)\n"
"buffer_store_dwordx4 v[156:159], v128, s[24:27], s70 offen sc1\n"
"v_mfma_f32_16x16x32_bf16 v[12:15], a[216:219], a[124:127], v[12:15]\n"
"ds_read_b128 a[104:107], v128 offset:59392\n"
"s_waitcnt lgkmcnt(1)\n"
"buffer_store_dwordx4 v[164:167], v128, s[24:27], s70 offen offset:2048 sc1\n"
"s_add_u32 s70, s70, 0x17800\n"
"v_fmac_f32 v71, v190, v70\n"
"v_fmac_f32 v88, v190, v87\n"
"v_fma_f32 v71, -v191, v87, v71\n"
"v_fmac_f32 v88, v191, v70\n"
"v_fmac_f32 v72, v190, v71\n"
"v_fmac_f32 v89, v190, v88\n"
"v_mfma_f32_16x16x32_bf16 v[32:35], a[220:223], a[124:127], v[32:35]\n"
"buffer_load_dwordx4 a[8:11], v128, s[24:27], s89 offen offset:3072 sc1\n"
"v_fma_f32 v72, -v191, v88, v72\n"
"v_fmac_f32 v89, v191, v71\n"
"v_fmac_f32 v73, v190, v72\n"
"v_fmac_f32 v90, v190, v89\n"
"v_fma_f32 v73, -v191, v89, v73\n"
"v_fmac_f32 v90, v191, v72\n"
"v_fmac_f32 v74, v190, v73\n"
"v_fmac_f32 v91, v190, v90\n"
"v_fma_f32 v74, -v191, v90, v74\n"
"v_fmac_f32 v91, v191, v73\n"
"v_mfma_f32_16x16x32_bf16 v[16:19], a[216:219], a[128:131], v[16:19]\n"
"ds_read_b128 a[108:111], v128 offset:60416\n"
"v_fmac_f32 v75, v190, v74\n"
"v_fmac_f32 v92, v190, v91\n"
"v_fma_f32 v75, -v191, v91, v75\n"
"v_fmac_f32 v92, v191, v74\n"
"v_fmac_f32 v76, v190, v75\n"
"v_fmac_f32 v93, v190, v92\n"
"v_fma_f32 v76, -v191, v92, v76\n"
"v_fmac_f32 v93, v191, v75\n"
"v_fmac_f32 v77, v190, v76\n"
"v_fmac_f32 v94, v190, v93\n"
"v_fma_f32 v77, -v191, v93, v77\n"
"v_fmac_f32 v94, v191, v76\n"
"v_fmac_f32 v78, v190, v77\n"
"v_mfma_f32_16x16x32_bf16 v[36:39], a[220:223], a[128:131], v[36:39]\n"
"v_fmac_f32 v95, v190, v94\n"
"v_fma_f32 v78, -v191, v94, v78\n"
"v_fmac_f32 v95, v191, v77\n"
"v_cvt_pk_bf16_f32 v152, v71, v72\n"
"v_cvt_pk_bf16_f32 v153, v73, v74\n"
"v_cvt_pk_bf16_f32 v154, v75, v76\n"
"v_cvt_pk_bf16_f32 v155, v77, v78\n"
"ds_write_b128 v227, v[152:155]\n"
"ds_read_b64_tr_b16 v[156:157], v228\n"
"ds_read_b64_tr_b16 v[158:159], v228 offset:128\n"
"s_waitcnt lgkmcnt(0)\n"
"s_barrier\n"
"s_waitcnt vmcnt(12)\n"
"v_mfma_f32_16x16x32_bf16 v[0:3], a[224:227], a[132:135], v[0:3]\n"
"s_lshl_b32 s80, s98, 2\n"
"ds_read_b128 a[112:115], v128 offset:61440\n"
"s_add_u32 s80, s80, s64\n"
"s_lshl_b32 s80, s80, 7\n"
"s_add_u32 s80, s80, 0x62000\n"
"v_and_b32 v140, 15, v253\n"
"v_lshl_add_u32 v140, v140, 3, s80\n"
"v_bfe_u32 v141, v253, 4, 1\n"
"v_lshlrev_b32 v141, 13, v141\n"
"v_sub_u32 v140, v140, v141\n"
"s_waitcnt vmcnt(11)\n"
"v_mfma_f32_16x16x32_bf16 v[20:23], a[228:231], a[132:135], v[20:23]\n"
"buffer_load_dwordx4 a[12:15], v128, s[24:27], s74 offen offset:3072 sc1\n"
"buffer_load_dwordx2 v[142:143], v140, s[24:27], 0 offen sc1\n"
"ds_read_b64 v[40:41], v214 offset:6144\n"
"ds_read_b64 v[42:43], v214 offset:6400\n"
"ds_read_b64 v[44:45], v214 offset:6656\n"
"ds_read_b64 v[46:47], v214 offset:6912\n"
"ds_read_b64 v[48:49], v214 offset:7168\n"
"ds_read_b64 v[50:51], v214 offset:7424\n"
"ds_read_b64 v[52:53], v214 offset:7680\n"
"s_waitcnt lgkmcnt(5)\n"
"v_fmac_f32 v42, v204, v40\n"
"v_mfma_f32_16x16x32_bf16 v[4:7], a[224:227], a[136:139], v[4:7]\n"
"ds_read_b128 a[116:119], v128 offset:62464\n"
"v_fmac_f32 v43, v204, v41\n"
"v_fma_f32 v42, -v205, v41, v42\n"
"v_fmac_f32 v43, v205, v40\n"
"s_waitcnt lgkmcnt(5)\n"
"v_fmac_f32 v44, v204, v42\n"
"v_fmac_f32 v45, v204, v43\n"
"v_fma_f32 v44, -v205, v43, v44\n"
"v_fmac_f32 v45, v205, v42\n"
"s_waitcnt lgkmcnt(4)\n"
"v_fmac_f32 v46, v204, v44\n"
"v_fmac_f32 v47, v204, v45\n"
"v_fma_f32 v46, -v205, v45, v46\n"
"v_fmac_f32 v47, v205, v44\n"
"s_waitcnt lgkmcnt(3)\n"
"v_fmac_f32 v48, v204, v46\n"
"v_fmac_f32 v49, v204, v47\n"
"v_mfma_f32_16x16x32_bf16 v[24:27], a[228:231], a[136:139], v[24:27]\n"
"s_mul_i32 s89, s1, 0x2c00\n"
"s_add_u32 s89, s89, s92\n"
"s_add_u32 s89, s89, 0x2000\n"
"s_add_u32 s74, s89, s0\n"
"buffer_load_dwordx4 a[16:19], v128, s[24:27], s89 offen sc1\n"
"v_fma_f32 v48, -v205, v47, v48\n"
"v_fmac_f32 v49, v205, v46\n"
"s_waitcnt lgkmcnt(2)\n"
"v_fmac_f32 v50, v204, v48\n"
"v_fmac_f32 v51, v204, v49\n"
"v_fma_f32 v50, -v205, v49, v50\n"
"v_fmac_f32 v51, v205, v48\n"
"v_mfma_f32_16x16x32_bf16 v[8:11], a[224:227], a[140:143], v[8:11]\n"
"s_lshl_b32 s80, s94, 9\n"
"ds_read_b128 a[120:123], v128 offset:63488\n"
"s_waitcnt lgkmcnt(2)\n"
"v_fmac_f32 v52, v204, v50\n"
"v_fmac_f32 v53, v204, v51\n"
"v_fma_f32 v52, -v205, v51, v52\n"
"v_fmac_f32 v53, v205, v50\n"
"v_mov_b32 v248, v52\n"
"v_mov_b32 v249, v53\n"
"v_fma_f32 v40, v204, v52, v246\n"
"v_fma_f32 v42, v204, v53, v247\n"
"v_fma_f32 v40, -v205, v53, v40\n"
"v_fmac_f32 v42, v205, v52\n"
"v_mov_b32 v41, s76\n"
"v_mov_b32 v43, s78\n"
"s_add_u32 s80, s80, 0x20000\n"
"buffer_store_dwordx4 v[40:43], v216, s[60:63], s80 offen sc1\n"
"v_mfma_f32_16x16x32_bf16 v[28:31], a[228:231], a[140:143], v[28:31]\n"
"buffer_load_dwordx4 a[20:23], v128, s[24:27], s74 offen sc1\n"
"v_cvt_pk_bf16_f32 v160, -v88, -v89\n"
"v_cvt_pk_bf16_f32 v161, -v90, -v91\n"
"v_cvt_pk_bf16_f32 v162, -v92, -v93\n"
"v_cvt_pk_bf16_f32 v163, -v94, -v95\n"
"ds_write_b128 v227, v[160:163]\n"
"ds_read_b64_tr_b16 v[164:165], v228\n"
"v_mfma_f32_16x16x32_bf16 v[12:15], a[224:227], a[144:147], v[12:15]\n"
"ds_read_b128 a[124:127], v128 offset:64512\n"
"ds_read_b64_tr_b16 v[166:167], v228 offset:128\n"
"buffer_store_dwordx4 v[156:159], v128, s[24:27], s70 offen sc1\n"
"s_waitcnt lgkmcnt(0)\n"
"buffer_store_dwordx4 v[164:167], v128, s[24:27], s70 offen offset:2048 sc1\n"
"v_mfma_f32_16x16x32_bf16 v[32:35], a[228:231], a[144:147], v[32:35]\n"
"v_mfma_f32_16x16x32_bf16 v[16:19], a[224:227], a[148:151], v[16:19]\n"
"ds_read_b128 a[128:131], v137\n"
"v_mfma_f32_16x16x32_bf16 v[36:39], a[228:231], a[148:151], v[36:39]\n"
"s_waitcnt vmcnt(16)\n"
"v_mfma_f32_16x16x32_bf16 v[0:3], a[232:235], a[92:95], v[0:3]\n"
"ds_read_b128 a[132:135], v137 offset:1024\n"
"v_mfma_f32_16x16x32_bf16 v[20:23], a[236:239], a[92:95], v[20:23]\n"
"buffer_load_dwordx4 a[24:27], v128, s[24:27], s89 offen offset:1024 sc1\n"
"v_mfma_f32_16x16x32_bf16 v[4:7], a[232:235], a[96:99], v[4:7]\n"
"ds_read_b128 a[136:139], v137 offset:2048\n"
"v_mfma_f32_16x16x32_bf16 v[24:27], a[236:239], a[96:99], v[24:27]\n"
"v_mfma_f32_16x16x32_bf16 v[8:11], a[232:235], a[100:103], v[8:11]\n"
"ds_read_b128 a[140:143], v137 offset:3072\n"
"v_mfma_f32_16x16x32_bf16 v[28:31], a[236:239], a[100:103], v[28:31]\n"
"buffer_load_dwordx4 a[28:31], v128, s[24:27], s74 offen offset:1024 sc1\n"
"v_mfma_f32_16x16x32_bf16 v[12:15], a[232:235], a[104:107], v[12:15]\n"
"ds_read_b128 a[144:147], v137 offset:4096\n"
"v_mfma_f32_16x16x32_bf16 v[32:35], a[236:239], a[104:107], v[32:35]\n"
"v_mfma_f32_16x16x32_bf16 v[16:19], a[232:235], a[108:111], v[16:19]\n"
"ds_read_b128 a[148:151], v137 offset:5120\n"
"v_mfma_f32_16x16x32_bf16 v[36:39], a[236:239], a[108:111], v[36:39]\n"
"s_waitcnt vmcnt(16) lgkmcnt(5)\n"
"v_mfma_f32_16x16x32_bf16 v[0:3], a[240:243], a[112:115], v[0:3]\n"
"ds_read_b128 a[92:95], v137 offset:6144\n"
"v_mul_f32 v250, v212, v96\n"
"v_fma_f32 v250, -v213, v112, v250\n"
"v_mul_f32 v112, v212, v112\n"
"v_fmac_f32 v112, v213, v96\n"
"v_mul_f32 v96, v212, v97\n"
"v_fma_f32 v96, -v213, v113, v96\n"
"v_mul_f32 v113, v212, v113\n"
"v_fmac_f32 v113, v213, v97\n"
"v_mul_f32 v97, v212, v98\n"
"v_fma_f32 v97, -v213, v114, v97\n"
"v_mul_f32 v114, v212, v114\n"
"v_fmac_f32 v114, v213, v98\n"
"v_mul_f32 v98, v212, v99\n"
"v_mfma_f32_16x16x32_bf16 v[20:23], a[244:247], a[112:115], v[20:23]\n"
"s_mul_i32 s99, s1, 0x6e000\n"
"s_add_u32 s99, s99, s90\n"
"s_add_u32 s99, s99, 0xdc000\n"
"s_add_u32 m0, s88, 0x19000\n"
"s_nop 0\n"
"buffer_load_dwordx4 v128, s[24:27], s99 offen sc1 lds\n"
"v_fma_f32 v98, -v213, v115, v98\n"
"v_mul_f32 v115, v212, v115\n"
"v_fmac_f32 v115, v213, v99\n"
"v_mul_f32 v99, v212, v100\n"
"v_fma_f32 v99, -v213, v116, v99\n"
"v_mul_f32 v116, v212, v116\n"
"v_mfma_f32_16x16x32_bf16 v[4:7], a[240:243], a[116:119], v[4:7]\n"
"ds_read_b128 a[96:99], v137 offset:7168\n"
"buffer_load_dwordx4 v128, s[24:27], s99 offen offset:1024 sc1 lds\n"
"v_fmac_f32 v116, v213, v100\n"
"v_mul_f32 v100, v212, v101\n"
"v_fma_f32 v100, -v213, v117, v100\n"
"v_mul_f32 v117, v212, v117\n"
"v_fmac_f32 v117, v213, v101\n"
"v_mul_f32 v101, v212, v102\n"
"v_fma_f32 v101, -v213, v118, v101\n"
"v_mul_f32 v118, v212, v118\n"
"v_mfma_f32_16x16x32_bf16 v[24:27], a[244:247], a[116:119], v[24:27]\n"
"buffer_load_dwordx4 v128, s[24:27], s99 offen offset:2048 sc1 lds\n"
"v_fmac_f32 v118, v213, v102\n"
"v_mul_f32 v102, v212, v103\n"
"v_fma_f32 v102, -v213, v119, v102\n"
"v_mul_f32 v119, v212, v119\n"
"v_fmac_f32 v119, v213, v103\n"
"v_mul_f32 v103, v212, v104\n"
"v_fma_f32 v103, -v213, v120, v103\n"
"v_mul_f32 v120, v212, v120\n"
"v_fmac_f32 v120, v213, v104\n"
"v_mfma_f32_16x16x32_bf16 v[8:11], a[240:243], a[120:123], v[8:11]\n"
"ds_read_b128 a[100:103], v137 offset:8192\n"
"buffer_load_dwordx4 v128, s[24:27], s99 offen offset:3072 sc1 lds\n"
"v_mul_f32 v104, v212, v105\n"
"v_fma_f32 v104, -v213, v121, v104\n"
"v_mul_f32 v121, v212, v121\n"
"v_fmac_f32 v121, v213, v105\n"
"v_mul_f32 v105, v212, v106\n"
"v_fma_f32 v105, -v213, v122, v105\n"
"v_mul_f32 v122, v212, v122\n"
"v_fmac_f32 v122, v213, v106\n"
"v_mfma_f32_16x16x32_bf16 v[28:31], a[244:247], a[120:123], v[28:31]\n"
"s_add_u32 m0, s88, 0x1a000\n"
"s_nop 0\n"
"buffer_load_dwordx4 v130, s[24:27], s99 offen sc1 lds\n"
"v_mul_f32 v106, v212, v107\n"
"v_fma_f32 v106, -v213, v123, v106\n"
"v_mul_f32 v123, v212, v123\n"
"v_fmac_f32 v123, v213, v107\n"
"v_mul_f32 v107, v212, v108\n"
"v_fma_f32 v107, -v213, v124, v107\n"
"v_mul_f32 v124, v212, v124\n"
"v_fmac_f32 v124, v213, v108\n"
"v_mul_f32 v108, v212, v109\n"
"v_mfma_f32_16x16x32_bf16 v[12:15], a[240:243], a[124:127], v[12:15]\n"
"s_add_u32 s80, s76, 1\n"
"ds_read_b128 a[104:107], v137 offset:9216\n"
"buffer_load_dwordx4 a[32:35], v128, s[24:27], s89 offen offset:2048 sc1\n"
"s_waitcnt vmcnt(13)\n"
"v_subrev_u32 v141, s80, v142\n"
"v_cmp_lt_u32 vcc, 2, v141\n"
"v_cmp_ne_u32 s[82:83], s77, v143\n"
"s_nop 0\n"
"s_or_b32 s82, s82, vcc_lo\n"
"s_and_b32 s82, s82, 0xf80000ff\n"
"s_cbranch_scc1 L%=_f2cS_slow\n"
"L%=_f2cS_back:\n"
"v_and_b32 v232, 15, v253\n"
"v_lshrrev_b32 v233, 3, v232\n"
"v_mfma_f32_16x16x32_bf16 v[32:35], a[244:247], a[124:127], v[32:35]\n"
"buffer_load_dwordx4 a[36:39], v128, s[24:27], s74 offen offset:2048 sc1\n"
"v_lshlrev_b32 v233, 10, v233\n"
"v_and_b32 v234, 7, v253\n"
"v_lshl_add_u32 v233, v234, 7, v233\n"
"v_lshrrev_b32 v232, 1, v232\n"
"v_lshrrev_b32 v234, 4, v253\n"
"v_xor_b32 v232, v232, v234\n"
"v_lshl_add_u32 v174, v232, 4, v233\n"
"v_xor_b32 v177, 64, v174\n"
"v_add_u32 v175, 0x10000, v174\n"
"v_add_u32 v176, 0x20000, v174\n"
"v_mfma_f32_16x16x32_bf16 v[16:19], a[240:243], a[128:131], v[16:19]\n"
"s_mov_b32 s87, 0x1000\n"
"ds_read_b128 a[108:111], v137 offset:10240\n"
"v_add_u32 v178, 0x10000, v177\n"
"v_add_u32 v179, 0x20000, v177\n"
"buffer_load_dwordx4 v[56:59], v128, s[24:27], s87 offen sc1\n"
"buffer_load_dwordx4 v[60:63], v128, s[24:27], s87 offen offset:1024 sc1\n"
"v_fma_f32 v108, -v213, v125, v108\n"
"v_mul_f32 v125, v212, v125\n"
"v_fmac_f32 v125, v213, v109\n"
"v_mfma_f32_16x16x32_bf16 v[36:39], a[244:247], a[128:131], v[36:39]\n"
"s_waitcnt lgkmcnt(5)\n"
"v_mul_f32 v109, v212, v110\n"
"v_fma_f32 v109, -v213, v126, v109\n"
"v_mul_f32 v126, v212, v126\n"
"v_fmac_f32 v126, v213, v110\n"
"v_mul_f32 v110, v212, v111\n"
"v_fma_f32 v110, -v213, v127, v110\n"
"v_mul_f32 v127, v212, v127\n"
"v_fmac_f32 v127, v213, v111\n"
"v_mfma_f32_16x16x32_bf16 v[0:3], a[248:251], a[132:135], v[0:3]\n"
"ds_read_b128 a[112:115], v137 offset:11264\n"
"v_mfma_f32_16x16x32_bf16 v[20:23], a[252:255], a[132:135], v[20:23]\n"
"s_mul_i32 s89, s1, 0x1400\n"
"s_add_u32 s89, s89, s92\n"
"s_add_u32 s89, s89, 0xb000\n"
"s_add_u32 s74, s89, s0\n"
"buffer_load_dwordx4 a[40:43], v128, s[24:27], s89 offen sc1\n"
"v_mfma_f32_16x16x32_bf16 v[4:7], a[248:251], a[136:139], v[4:7]\n"
"ds_read_b128 a[116:119], v137 offset:12288\n"
"s_waitcnt vmcnt(12)\n"
"s_add_u32 s80, s76, 2\n"
"v_mov_b32 v230, s80\n"
"s_lshl_b32 s80, s66, 2\n"
"s_add_u32 s80, s80, s64\n"
"s_lshl_b32 s80, s80, 4\n"
"s_add_u32 s80, s80, s67\n"
"s_lshl_b32 s80, s80, 3\n"
"s_add_u32 s80, s80, 0x62000\n"
"buffer_store_dwordx2 v[230:231], v229, s[24:27], s80 offen sc1\n"
"v_mfma_f32_16x16x32_bf16 v[24:27], a[252:255], a[136:139], v[24:27]\n"
"v_mfma_f32_16x16x32_bf16 v[8:11], a[248:251], a[140:143], v[8:11]\n"
"ds_read_b128 a[120:123], v137 offset:13312\n"
"v_mfma_f32_16x16x32_bf16 v[28:31], a[252:255], a[140:143], v[28:31]\n"
"buffer_load_dwordx4 a[44:47], v128, s[24:27], s74 offen sc1\n"
"v_mfma_f32_16x16x32_bf16 v[12:15], a[248:251], a[144:147], v[12:15]\n"
"ds_read_b128 a[124:127], v137 offset:14336\n"
"v_mfma_f32_16x16x32_bf16 v[32:35], a[252:255], a[144:147], v[32:35]\n"
"v_mfma_f32_16x16x32_bf16 v[16:19], a[248:251], a[148:151], v[16:19]\n"
"ds_read_b128 a[128:131], v137 offset:15360\n"
"v_mfma_f32_16x16x32_bf16 v[36:39], a[252:255], a[148:151], v[36:39]\n"
"s_waitcnt lgkmcnt(5)\n"
"s_barrier\n"
"v_mfma_f32_16x16x32_bf16 v[0:3], a[0:3], a[92:95], v[0:3]\n"
"ds_read_b128 a[132:135], v137 offset:57344\n"
"v_mfma_f32_16x16x32_bf16 v[20:23], a[4:7], a[92:95], v[20:23]\n"
"s_mul_i32 s89, s1, 0x1400\n"
"s_add_u32 s89, s89, s92\n"
"s_add_u32 s89, s89, 0xb400\n"
"s_add_u32 s74, s89, s0\n"
"buffer_load_dwordx4 a[48:51], v128, s[24:27], s89 offen sc1\n"
"v_mfma_f32_16x16x32_bf16 v[4:7], a[0:3], a[96:99], v[4:7]\n"
"ds_read_b128 a[136:139], v137 offset:58368\n"
"v_mfma_f32_16x16x32_bf16 v[24:27], a[4:7], a[96:99], v[24:27]\n"
"v_mfma_f32_16x16x32_bf16 v[8:11], a[0:3], a[100:103], v[8:11]\n"
"ds_read_b128 a[140:143], v137 offset:59392\n"
"v_mfma_f32_16x16x32_bf16 v[28:31], a[4:7], a[100:103], v[28:31]\n"
"buffer_load_dwordx4 a[52:55], v128, s[24:27], s74 offen sc1\n"
"v_mfma_f32_16x16x32_bf16 v[12:15], a[0:3], a[104:107], v[12:15]\n"
"ds_read_b128 a[144:147], v137 offset:60416\n"
"v_mfma_f32_16x16x32_bf16 v[32:35], a[4:7], a[104:107], v[32:35]\n"
"v_mfma_f32_16x16x32_bf16 v[16:19], a[0:3], a[108:111], v[16:19]\n"
"ds_read_b128 a[148:151], v137 offset:61440\n"
"v_mfma_f32_16x16x32_bf16 v[36:39], a[4:7], a[108:111], v[36:39]\n"
"s_waitcnt lgkmcnt(5)\n"
"v_mfma_f32_16x16x32_bf16 v[0:3], a[8:11], a[112:115], v[0:3]\n"
"s_lshl_b32 s69, s94, 9\n"
"ds_read_b128 a[92:95], v137 offset:62464\n"
"s_add_u32 s69, s69, 0x20000\n"
"buffer_load_dwordx4 v[40:43], v217, s[60:63], s69 offen sc1\n"
"v_mfma_f32_16x16x32_bf16 v[20:23], a[12:15], a[112:115], v[20:23]\n"
"buffer_load_dwordx4 a[56:59], v128, s[24:27], s89 offen offset:1024 sc1\n"
"buffer_load_dwordx4 v[44:47], v218, s[60:63], s69 offen sc1\n"
"v_mfma_f32_16x16x32_bf16 v[4:7], a[8:11], a[116:119], v[4:7]\n"
"ds_read_b128 a[96:99], v137 offset:63488\n"
"v_mfma_f32_16x16x32_bf16 v[24:27], a[12:15], a[116:119], v[24:27]\n"
"v_mfma_f32_16x16x32_bf16 v[8:11], a[8:11], a[120:123], v[8:11]\n"
"ds_read_b128 a[100:103], v137 offset:64512\n"
"v_mfma_f32_16x16x32_bf16 v[28:31], a[12:15], a[120:123], v[28:31]\n"
"buffer_load_dwordx4 a[60:63], v128, s[24:27], s74 offen offset:1024 sc1\n"
"v_mfma_f32_16x16x32_bf16 v[12:15], a[8:11], a[124:127], v[12:15]\n"
"ds_read_b128 a[104:107], v138\n"
"v_mfma_f32_16x16x32_bf16 v[32:35], a[12:15], a[124:127], v[32:35]\n"
"v_mfma_f32_16x16x32_bf16 v[16:19], a[8:11], a[128:131], v[16:19]\n"
"ds_read_b128 a[108:111], v138 offset:1024\n"
"v_mfma_f32_16x16x32_bf16 v[36:39], a[12:15], a[128:131], v[36:39]\n"
"s_waitcnt lgkmcnt(5)\n"
"v_mfma_f32_16x16x32_bf16 v[0:3], a[16:19], a[132:135], v[0:3]\n"
"ds_read_b128 a[112:115], v138 offset:2048\n"
"v_mfma_f32_16x16x32_bf16 v[20:23], a[20:23], a[132:135], v[20:23]\n"
"s_mul_i32 s99, s1, 0x6e000\n"
"s_add_u32 s99, s99, s90\n"
"s_add_u32 s99, s99, 0x104000\n"
"s_add_u32 m0, s88, 0xa000\n"
"s_nop 0\n"
"buffer_load_dwordx4 v128, s[24:27], s99 offen sc1 lds\n"
"v_mfma_f32_16x16x32_bf16 v[4:7], a[16:19], a[136:139], v[4:7]\n"
"ds_read_b128 a[116:119], v138 offset:3072\n"
"buffer_load_dwordx4 v128, s[24:27], s99 offen offset:1024 sc1 lds\n"
"v_mfma_f32_16x16x32_bf16 v[24:27], a[20:23], a[136:139], v[24:27]\n"
"buffer_load_dwordx4 v128, s[24:27], s99 offen offset:2048 sc1 lds\n"
"v_mfma_f32_16x16x32_bf16 v[8:11], a[16:19], a[140:143], v[8:11]\n"
"ds_read_b128 a[120:123], v138 offset:4096\n"
"v_mfma_f32_16x16x32_bf16 v[28:31], a[20:23], a[140:143], v[28:31]\n"
"buffer_load_dwordx4 a[64:67], v128, s[24:27], s89 offen offset:2048 sc1\n"
"v_mfma_f32_16x16x32_bf16 v[12:15], a[16:19], a[144:147], v[12:15]\n"
"ds_read_b128 a[124:127], v138 offset:5120\n"
"v_mfma_f32_16x16x32_bf16 v[32:35], a[20:23], a[144:147], v[32:35]\n"
"buffer_load_dwordx4 a[68:71], v128, s[24:27], s74 offen offset:2048 sc1\n"
"v_mfma_f32_16x16x32_bf16 v[16:19], a[16:19], a[148:151], v[16:19]\n"
"ds_read_b128 a[128:131], v138 offset:6144\n"
"v_mfma_f32_16x16x32_bf16 v[36:39], a[20:23], a[148:151], v[36:39]\n"
"s_waitcnt vmcnt(23) lgkmcnt(5)\n"
"v_mfma_f32_16x16x32_bf16 v[0:3], a[24:27], a[92:95], v[0:3]\n"
"ds_read_b128 a[132:135], v138 offset:7168\n"
"s_waitcnt vmcnt(15)\n"
"v_cmp_ne_u64 s[80:81], s[76:77], v[56:57]\n"
"v_cmp_ne_u64 s[82:83], s[76:77], v[58:59]\n"
"s_waitcnt vmcnt(14)\n"
"v_cmp_ne_u64 s[84:85], s[76:77], v[60:61]\n"
"v_cmp_ne_u64 vcc, s[76:77], v[62:63]\n"
"s_or_b64 s[80:81], s[80:81], s[82:83]\n"
"s_or_b64 s[84:85], s[84:85], vcc\n"
"s_or_b64 s[80:81], s[80:81], s[84:85]\n"
"s_or_b32 s80, s80, s81\n"
"s_cmp_eq_u32 s3, 0\n"
"s_cselect_b32 s81, -1, 0\n"
"s_and_b32 s80, s80, s81\n"
"s_cbranch_scc1 L%=_f0PS_slow\n"
"L%=_f0PS_back:\n"
"v_mfma_f32_16x16x32_bf16 v[20:23], a[28:31], a[92:95], v[20:23]\n"
"s_add_u32 m0, s88, 0xa000\n"
"s_nop 0\n"
"buffer_load_dwordx4 v128, s[24:27], s99 offen offset:3072 sc1 lds\n"
"s_or_b32 s80, s3, s64\n"
"s_cselect_b32 s80, 0x7000000, 0\n"
"v_mov_b32 v230, s76\n"
"buffer_store_dwordx2 v[230:231], v229, s[24:27], s80 offen sc1\n"
"s_lshr_b32 s82, s3, 3\n"
"s_lshl_b32 s82, s82, 2\n"
"s_add_u32 s82, s82, s64\n"
"s_mul_i32 s82, s82, 5\n"
"v_min_u32 v251, 4, v253\n"
"v_add_u32 v251, s82, v251\n"
"v_lshlrev_b32 v251, 7, v251\n"
"v_min_u32 v251, L%=_end-L%=_pc-8, v251\n"
"s_getpc_b64 s[80:81]\n"
"L%=_st_ct9:\n"
"s_sub_u32 s80, s80, L%=_st_ct9-L%=_pc\n"
"s_subb_u32 s81, s81, 0\n"
"global_load_dword v251, v251, s[80:81]\n"
"v_mfma_f32_16x16x32_bf16 v[4:7], a[24:27], a[96:99], v[4:7]\n"
"ds_read_b128 a[136:139], v138 offset:8192\n"
"v_mfma_f32_16x16x32_bf16 v[24:27], a[28:31], a[96:99], v[24:27]\n"
"s_add_u32 m0, s88, 0xb000\n"
"s_nop 0\n"
"buffer_load_dwordx4 v130, s[24:27], s99 offen sc1 lds\n"
"v_mfma_f32_16x16x32_bf16 v[8:11], a[24:27], a[100:103], v[8:11]\n"
"ds_read_b128 a[140:143], v138 offset:9216\n"
"v_mfma_f32_16x16x32_bf16 v[28:31], a[28:31], a[100:103], v[28:31]\n"
"buffer_load_dwordx4 a[72:75], v128, s[24:27], s89 offen offset:3072 sc1\n"
"s_waitcnt vmcnt(13)\n"
"v_xor_b32 v48, v41, v223\n"
"v_bitop3_b32 v48, v43, v224, v48 bitop3:0xbe\n"
"s_waitcnt vmcnt(11)\n"
"v_bitop3_b32 v48, v45, v225, v48 bitop3:0xbe\n"
"v_bitop3_b32 v48, v47, v226, v48 bitop3:0xbe\n"
"v_cmp_ne_u32 vcc, 0, v48\n"
"s_nop 0\n"
"s_cbranch_vccnz L%=_tS1_slow\n"
"L%=_tS1_back:\n"
"v_mul_f32 v244, v208, v40\n"
"v_mul_f32 v245, v208, v42\n"
"v_mfma_f32_16x16x32_bf16 v[12:15], a[24:27], a[104:107], v[12:15]\n"
"ds_read_b128 a[144:147], v138 offset:10240\n"
"v_fma_f32 v244, -v209, v42, v244\n"
"v_fmac_f32 v245, v209, v40\n"
"v_fmac_f32 v244, v210, v44\n"
"v_fmac_f32 v245, v210, v46\n"
"v_fma_f32 v244, -v211, v46, v244\n"
"v_fmac_f32 v245, v211, v44\n"
"ds_write_b64 v214, v[244:245] offset:9984\n"
"v_mfma_f32_16x16x32_bf16 v[32:35], a[28:31], a[104:107], v[32:35]\n"
"buffer_load_dwordx4 a[76:79], v128, s[24:27], s74 offen offset:3072 sc1\n"
"v_mfma_f32_16x16x32_bf16 v[16:19], a[24:27], a[108:111], v[16:19]\n"
"ds_read_b128 a[148:151], v138 offset:11264\n"
"v_mfma_f32_16x16x32_bf16 v[36:39], a[28:31], a[108:111], v[36:39]\n"
"s_waitcnt lgkmcnt(6)\n"
"s_waitcnt lgkmcnt(1)\n"
"s_barrier\n"
"v_mfma_f32_16x16x32_bf16 v[0:3], a[32:35], a[112:115], v[0:3]\n"
"ds_read_b128 a[92:95], v138 offset:12288\n"
"ds_read_b64 v[40:41], v215 offset:9984\n"
"ds_read_b64 v[42:43], v215 offset:10240\n"
"ds_read_b64 v[44:45], v215 offset:10496\n"
"ds_read_b64 v[46:47], v215 offset:10752\n"
"v_mfma_f32_16x16x32_bf16 v[20:23], a[36:39], a[112:115], v[20:23]\n"
"s_mul_i32 s99, s1, 0x6e000\n"
"s_add_u32 s99, s99, s90\n"
"s_add_u32 s99, s99, 0x12c000\n"
"s_cmp_ge_u32 s64, 3\n"
"s_cselect_b32 s99, 0x7000000, s99\n"
"s_add_u32 m0, s88, 0xf000\n"
"s_nop 0\n"
"buffer_load_dwordx4 v128, s[24:27], s99 offen sc1 lds\n"
"ds_read_b64 v[48:49], v215 offset:11008\n"
"ds_read_b64 v[50:51], v215 offset:11264\n"
"v_mfma_f32_16x16x32_bf16 v[4:7], a[32:35], a[116:119], v[4:7]\n"
"ds_read_b128 a[96:99], v138 offset:13312\n"
"buffer_load_dwordx4 v128, s[24:27], s99 offen offset:1024 sc1 lds\n"
"ds_read_b64 v[52:53], v215 offset:11520\n"
"ds_read_b64 v[54:55], v215 offset:11776\n"
"s_waitcnt lgkmcnt(7)\n"
"v_add_f32 v40, v40, v42\n"
"v_add_f32 v41, v41, v43\n"
"s_waitcnt lgkmcnt(5)\n"
"v_add_f32 v44, v44, v46\n"
"v_add_f32 v45, v45, v47\n"
"v_mfma_f32_16x16x32_bf16 v[24:27], a[36:39], a[116:119], v[24:27]\n"
"buffer_load_dwordx4 v128, s[24:27], s99 offen offset:2048 sc1 lds\n"
"s_waitcnt lgkmcnt(3)\n"
"v_add_f32 v48, v48, v50\n"
"v_add_f32 v49, v49, v51\n"
"s_waitcnt lgkmcnt(0)\n"
"v_add_f32 v52, v52, v54\n"
"v_add_f32 v53, v53, v55\n"
"v_add_f32 v40, v40, v44\n"
"v_add_f32 v41, v41, v45\n"
"v_add_f32 v48, v48, v52\n"
"v_mfma_f32_16x16x32_bf16 v[8:11], a[32:35], a[120:123], v[8:11]\n"
"ds_read_b128 a[100:103], v138 offset:14336\n"
"v_add_f32 v49, v49, v53\n"
"v_add_f32 v40, v40, v48\n"
"v_add_f32 v41, v41, v49\n"
"v_fmac_f32 v248, v206, v40\n"
"v_fmac_f32 v249, v206, v41\n"
"v_fma_f32 v248, -v207, v41, v248\n"
"v_fmac_f32 v249, v207, v40\n"
"v_mul_f32 v244, v212, v248\n"
"v_mul_f32 v245, v212, v249\n"
"v_fma_f32 v244, -v213, v249, v244\n"
"v_fmac_f32 v245, v213, v248\n"
"v_mfma_f32_16x16x32_bf16 v[28:31], a[36:39], a[120:123], v[28:31]\n"
"s_mul_i32 s89, s1, 0x1000\n"
"s_add_u32 s89, s89, s92\n"
"s_add_u32 s89, s89, 0xd800\n"
"s_add_u32 s74, s89, s0\n"
"buffer_load_dwordx4 a[80:83], v128, s[24:27], s89 offen sc1\n"
"s_lshr_b32 s80, s94, 1\n"
"s_lshl_b32 s80, s80, 2\n"
"s_and_b32 s81, s94, 1\n"
"s_add_u32 s80, s80, s81\n"
"s_add_u32 s80, s80, 54\n"
"s_lshl_b32 s80, s80, 10\n"
"s_lshl_b32 s81, s66, 2\n"
"s_add_u32 s81, s81, s64\n"
"s_mul_i32 s81, s81, 0x2f000\n"
"s_add_u32 s80, s80, s81\n"
"s_add_u32 s71, s80, 0x300000\n"
"v_mfma_f32_16x16x32_bf16 v[12:15], a[32:35], a[124:127], v[12:15]\n"
"ds_read_b128 a[104:107], v138 offset:15360\n"
"v_fmac_f32 v250, v202, v244\n"
"v_fmac_f32 v112, v202, v245\n"
"v_fma_f32 v250, -v203, v245, v250\n"
"v_fmac_f32 v112, v203, v244\n"
"v_fmac_f32 v96, v202, v250\n"
"v_fmac_f32 v113, v202, v112\n"
"v_fma_f32 v96, -v203, v112, v96\n"
"v_fmac_f32 v113, v203, v250\n"
"v_fmac_f32 v97, v202, v96\n"
"v_fmac_f32 v114, v202, v113\n"
"v_fma_f32 v97, -v203, v113, v97\n"
"v_mfma_f32_16x16x32_bf16 v[32:35], a[36:39], a[124:127], v[32:35]\n"
"buffer_load_dwordx4 a[84:87], v128, s[24:27], s74 offen sc1\n"
"v_fmac_f32 v114, v203, v96\n"
"v_fmac_f32 v98, v202, v97\n"
"v_fmac_f32 v115, v202, v114\n"
"v_fma_f32 v98, -v203, v114, v98\n"
"v_fmac_f32 v115, v203, v97\n"
"v_fmac_f32 v99, v202, v98\n"
"v_fmac_f32 v116, v202, v115\n"
"v_fma_f32 v99, -v203, v115, v99\n"
"v_mfma_f32_16x16x32_bf16 v[16:19], a[32:35], a[128:131], v[16:19]\n"
"ds_read_b128 a[108:111], v138 offset:16384\n"
"v_fmac_f32 v116, v203, v98\n"
"v_fmac_f32 v100, v202, v99\n"
"v_fmac_f32 v117, v202, v116\n"
"v_fma_f32 v100, -v203, v116, v100\n"
"v_fmac_f32 v117, v203, v99\n"
"v_fmac_f32 v101, v202, v100\n"
"v_fmac_f32 v118, v202, v117\n"
"v_fma_f32 v101, -v203, v117, v101\n"
"v_fmac_f32 v118, v203, v100\n"
"v_fmac_f32 v102, v202, v101\n"
"v_fmac_f32 v119, v202, v118\n"
"v_mfma_f32_16x16x32_bf16 v[36:39], a[36:39], a[128:131], v[36:39]\n"
"v_fma_f32 v102, -v203, v118, v102\n"
"v_fmac_f32 v119, v203, v101\n"
"v_cvt_pk_bf16_f32 v40, v250, v96\n"
"v_cvt_pk_bf16_f32 v41, v97, v98\n"
"v_cvt_pk_bf16_f32 v42, v99, v100\n"
"v_cvt_pk_bf16_f32 v43, v101, v102\n"
"ds_write_b128 v227, v[40:43]\n"
"ds_read_b64_tr_b16 v[44:45], v228\n"
"v_mfma_f32_16x16x32_bf16 v[0:3], a[40:43], a[132:135], v[0:3]\n"
"ds_read_b128 a[112:115], v138 offset:17408\n"
"ds_read_b64_tr_b16 v[46:47], v228 offset:128\n"
"v_cvt_pk_bf16_f32 v48, -v112, -v113\n"
"v_cvt_pk_bf16_f32 v49, -v114, -v115\n"
"v_cvt_pk_bf16_f32 v50, -v116, -v117\n"
"v_cvt_pk_bf16_f32 v51, -v118, -v119\n"
"ds_write_b128 v227, v[48:51]\n"
"ds_read_b64_tr_b16 v[52:53], v228\n"
"v_mfma_f32_16x16x32_bf16 v[20:23], a[44:47], a[132:135], v[20:23]\n"
"buffer_load_dwordx4 v128, s[24:27], s99 offen offset:3072 sc1 lds\n"
"ds_read_b64_tr_b16 v[54:55], v228 offset:128\n"
"s_waitcnt lgkmcnt(3)\n"
"buffer_store_dwordx4 v[44:47], v128, s[60:63], s71 offen sc1\n"
"s_waitcnt lgkmcnt(0)\n"
"buffer_store_dwordx4 v[52:55], v128, s[60:63], s71 offen offset:2048 sc1\n"
"v_mfma_f32_16x16x32_bf16 v[4:7], a[40:43], a[136:139], v[4:7]\n"
"s_add_u32 s71, s71, 0x17800\n"
"ds_read_b128 a[116:119], v138 offset:18432\n"
"v_fmac_f32 v103, v202, v102\n"
"v_fmac_f32 v120, v202, v119\n"
"v_fma_f32 v103, -v203, v119, v103\n"
"v_fmac_f32 v120, v203, v102\n"
"v_fmac_f32 v104, v202, v103\n"
"v_fmac_f32 v121, v202, v120\n"
"v_fma_f32 v104, -v203, v120, v104\n"
"v_fmac_f32 v121, v203, v103\n"
"v_fmac_f32 v105, v202, v104\n"
"v_fmac_f32 v122, v202, v121\n"
"v_mfma_f32_16x16x32_bf16 v[24:27], a[44:47], a[136:139], v[24:27]\n"
"s_add_u32 m0, s88, 0x10000\n"
"s_nop 0\n"
"buffer_load_dwordx4 v130, s[24:27], s99 offen sc1 lds\n"
"v_fma_f32 v105, -v203, v121, v105\n"
"v_fmac_f32 v122, v203, v104\n"
"v_fmac_f32 v106, v202, v105\n"
"v_fmac_f32 v123, v202, v122\n"
"v_fma_f32 v106, -v203, v122, v106\n"
"v_fmac_f32 v123, v203, v105\n"
"v_fmac_f32 v107, v202, v106\n"
"v_mfma_f32_16x16x32_bf16 v[8:11], a[40:43], a[140:143], v[8:11]\n"
"ds_read_b128 a[120:123], v138 offset:19456\n"
"v_fmac_f32 v124, v202, v123\n"
"v_fma_f32 v107, -v203, v123, v107\n"
"v_fmac_f32 v124, v203, v106\n"
"v_fmac_f32 v108, v202, v107\n"
"v_fmac_f32 v125, v202, v124\n"
"v_fma_f32 v108, -v203, v124, v108\n"
"v_fmac_f32 v125, v203, v107\n"
"v_fmac_f32 v109, v202, v108\n"
"v_fmac_f32 v126, v202, v125\n"
"v_fma_f32 v109, -v203, v125, v109\n"
"v_fmac_f32 v126, v203, v108\n"
"v_mfma_f32_16x16x32_bf16 v[28:31], a[44:47], a[140:143], v[28:31]\n"
"buffer_load_dwordx4 a[88:91], v128, s[24:27], s89 offen offset:1024 sc1\n"
"v_fmac_f32 v110, v202, v109\n"
"v_fmac_f32 v127, v202, v126\n"
"v_fma_f32 v110, -v203, v126, v110\n"
"v_fmac_f32 v127, v203, v109\n"
"v_cvt_pk_bf16_f32 v40, v103, v104\n"
"v_cvt_pk_bf16_f32 v41, v105, v106\n"
"v_cvt_pk_bf16_f32 v42, v107, v108\n"
"v_cvt_pk_bf16_f32 v43, v109, v110\n"
"ds_write_b128 v227, v[40:43]\n"
"v_mfma_f32_16x16x32_bf16 v[12:15], a[40:43], a[144:147], v[12:15]\n"
"ds_read_b128 a[124:127], v138 offset:20480\n"
"ds_read_b64_tr_b16 v[44:45], v228\n"
"ds_read_b64_tr_b16 v[46:47], v228 offset:128\n"
"v_cvt_pk_bf16_f32 v48, -v120, -v121\n"
"v_cvt_pk_bf16_f32 v49, -v122, -v123\n"
"v_cvt_pk_bf16_f32 v50, -v124, -v125\n"
"v_cvt_pk_bf16_f32 v51, -v126, -v127\n"
"ds_write_b128 v227, v[48:51]\n"
"v_mfma_f32_16x16x32_bf16 v[32:35], a[44:47], a[144:147], v[32:35]\n"
"buffer_load_dwordx4 a[152:155], v128, s[24:27], s74 offen offset:1024 sc1\n"
"ds_read_b64_tr_b16 v[52:53], v228\n"
"ds_read_b64_tr_b16 v[54:55], v228 offset:128\n"
"s_waitcnt lgkmcnt(3)\n"
"buffer_store_dwordx4 v[44:47], v128, s[60:63], s71 offen sc1\n"
"s_waitcnt lgkmcnt(0)\n"
"buffer_store_dwordx4 v[52:55], v128, s[60:63], s71 offen offset:2048 sc1\n"
"v_mfma_f32_16x16x32_bf16 v[16:19], a[40:43], a[148:151], v[16:19]\n"
"ds_read_b128 a[128:131], v138 offset:21504\n"
"v_mfma_f32_16x16x32_bf16 v[36:39], a[44:47], a[148:151], v[36:39]\n"
"v_mfma_f32_16x16x32_bf16 v[0:3], a[48:51], a[92:95], v[0:3]\n"
"s_lshl_b32 s80, s98, 2\n"
"ds_read_b128 a[132:135], v138 offset:22528\n"
"v_bfe_u32 v188, v253, 4, 1\n"
"v_lshlrev_b32 v188, 4, v188\n"
"v_lshrrev_b32 v131, 5, v253\n"
"v_lshl_add_u32 v188, v131, 2, v188\n"
"s_add_u32 s80, s80, s64\n"
"s_lshl_b32 s80, s80, 2\n"
"s_add_u32 s80, s80, s1\n"
"s_lshl_b32 s80, s80, 3\n"
"v_add_u32 v188, s80, v188\n"
"v_and_b32 v131, 15, v253\n"
"v_mfma_f32_16x16x32_bf16 v[20:23], a[52:55], a[92:95], v[20:23]\n"
"s_mul_i32 s80, s97, 0x50\n"
"buffer_load_dwordx4 a[156:159], v128, s[24:27], s89 offen offset:2048 sc1\n"
"v_add_lshl_u32 v131, v131, s80, 1\n"
"v_mul_u32_u24 v189, 0x500, v188\n"
"v_add_u32 v189, v189, v131\n"
"v_lshlrev_b32 v227, 1, v189\n"
"v_lshlrev_b32 v188, 4, v188\n"
"v_add_u32 v228, 0x1400, v227\n"
"v_add_u32 v188, 0x50000, v188\n"
"v_mfma_f32_16x16x32_bf16 v[4:7], a[48:51], a[96:99], v[4:7]\n"
"ds_read_b128 a[136:139], v138 offset:23552\n"
"v_add_u32 v243, 0, v253\n"
"v_mul_u32_u24 v247, 0x199a, v243\n"
"v_lshrrev_b32 v247, 16, v247\n"
"s_waitcnt vmcnt(17)\n"
"v_mul_u32_u24 v251, 10, v247\n"
"v_sub_u32 v243, v243, v251\n"
"v_bfe_u32 v251, v247, 2, 1\n"
"v_lshlrev_b32 v251, 4, v251\n"
"v_and_b32 v240, 3, v247\n"
"v_add_u32 v251, v251, v240\n"
"v_lshrrev_b32 v240, 3, v247\n"
"v_lshl_add_u32 v251, v240, 2, v251\n"
"v_mfma_f32_16x16x32_bf16 v[24:27], a[52:55], a[96:99], v[24:27]\n"
"v_mul_u32_u24 v251, 0x500, v251\n"
"v_lshl_add_u32 v240, v243, 4, v251\n"
"v_add_u32 v243, 64, v253\n"
"v_mul_u32_u24 v247, 0x199a, v243\n"
"v_lshrrev_b32 v247, 16, v247\n"
"v_mul_u32_u24 v251, 10, v247\n"
"v_sub_u32 v243, v243, v251\n"
"v_bfe_u32 v251, v247, 2, 1\n"
"v_lshlrev_b32 v251, 4, v251\n"
"v_and_b32 v241, 3, v247\n"
"v_add_u32 v251, v251, v241\n"
"v_lshrrev_b32 v241, 3, v247\n"
"v_mfma_f32_16x16x32_bf16 v[8:11], a[48:51], a[100:103], v[8:11]\n"
"ds_read_b128 a[140:143], v138 offset:24576\n"
"v_lshl_add_u32 v251, v241, 2, v251\n"
"v_mul_u32_u24 v251, 0x500, v251\n"
"v_lshl_add_u32 v241, v243, 4, v251\n"
"v_add_u32 v243, 0x80, v253\n"
"v_mul_u32_u24 v247, 0x199a, v243\n"
"v_lshrrev_b32 v247, 16, v247\n"
"v_mul_u32_u24 v251, 10, v247\n"
"v_sub_u32 v243, v243, v251\n"
"v_bfe_u32 v251, v247, 2, 1\n"
"v_lshlrev_b32 v251, 4, v251\n"
"v_and_b32 v242, 3, v247\n"
"v_mfma_f32_16x16x32_bf16 v[28:31], a[52:55], a[100:103], v[28:31]\n"
"s_lshl_b32 s80, s98, 2\n"
"buffer_load_dwordx4 a[160:163], v128, s[24:27], s74 offen offset:2048 sc1\n"
"v_add_u32 v251, v251, v242\n"
"v_lshrrev_b32 v242, 3, v247\n"
"v_lshl_add_u32 v251, v242, 2, v251\n"
"v_mul_u32_u24 v251, 0x500, v251\n"
"v_lshl_add_u32 v242, v243, 4, v251\n"
"v_cmp_gt_u32 vcc, 32, v253\n"
"v_mov_b32 v243, 0x40000000\n"
"s_add_u32 s80, s80, s64\n"
"s_lshl_b32 s80, s80, 2\n"
"s_add_u32 s80, s80, s1\n"
"s_mul_i32 s70, s80, 0x2800\n"
"s_mul_i32 s80, s97, 0xa0\n"
"s_add_u32 s70, s70, s80\n"
"v_mfma_f32_16x16x32_bf16 v[12:15], a[48:51], a[104:107], v[12:15]\n"
"ds_read_b128 a[144:147], v138 offset:25600\n"
"v_cndmask_b32 v242, v243, v242, vcc\n"
"v_mfma_f32_16x16x32_bf16 v[32:35], a[52:55], a[104:107], v[32:35]\n"
"v_mfma_f32_16x16x32_bf16 v[16:19], a[48:51], a[108:111], v[16:19]\n"
"ds_read_b128 a[148:151], v138 offset:26624\n"
"v_mfma_f32_16x16x32_bf16 v[36:39], a[52:55], a[108:111], v[36:39]\n"
"s_waitcnt lgkmcnt(5)\n"
"v_mfma_f32_16x16x32_bf16 v[0:3], a[56:59], a[112:115], v[0:3]\n"
"ds_read_b128 a[92:95], v138 offset:27648\n"
"v_mfma_f32_16x16x32_bf16 v[20:23], a[60:63], a[112:115], v[20:23]\n"
"buffer_load_dwordx4 a[164:167], v128, s[24:27], s89 offen offset:3072 sc1\n"
"v_mfma_f32_16x16x32_bf16 v[4:7], a[56:59], a[116:119], v[4:7]\n"
"ds_read_b128 a[96:99], v138 offset:28672\n"
"v_mfma_f32_16x16x32_bf16 v[24:27], a[60:63], a[116:119], v[24:27]\n"
"v_mfma_f32_16x16x32_bf16 v[8:11], a[56:59], a[120:123], v[8:11]\n"
"ds_read_b128 a[100:103], v138 offset:29696\n"
"v_mfma_f32_16x16x32_bf16 v[28:31], a[60:63], a[120:123], v[28:31]\n"
"buffer_load_dwordx4 a[168:171], v128, s[24:27], s74 offen offset:3072 sc1\n"
"v_mfma_f32_16x16x32_bf16 v[12:15], a[56:59], a[124:127], v[12:15]\n"
"ds_read_b128 a[104:107], v138 offset:30720\n"
"v_mfma_f32_16x16x32_bf16 v[32:35], a[60:63], a[124:127], v[32:35]\n"
"v_mfma_f32_16x16x32_bf16 v[16:19], a[56:59], a[128:131], v[16:19]\n"
"ds_read_b128 a[108:111], v138 offset:31744\n"
"v_mfma_f32_16x16x32_bf16 v[36:39], a[60:63], a[128:131], v[36:39]\n"
"s_waitcnt lgkmcnt(5)\n"
"s_barrier\n"
"v_mfma_f32_16x16x32_bf16 v[0:3], a[64:67], a[132:135], v[0:3]\n"
"ds_read_b128 a[112:115], v174\n"
"v_mfma_f32_16x16x32_bf16 v[20:23], a[68:71], a[132:135], v[20:23]\n"
"s_lshl_b32 s99, s1, 7\n"
"s_add_u32 s99, s99, 0x200\n"
"s_add_u32 m0, s88, 0x1e000\n"
"s_nop 0\n"
"buffer_load_dwordx4 v133, s[32:35], s99 offen lds\n"
"v_mfma_f32_16x16x32_bf16 v[4:7], a[64:67], a[136:139], v[4:7]\n"
"ds_read_b128 a[116:119], v174 offset:2048\n"
"buffer_load_dwordx4 v134, s[32:35], s99 offen offset:1024 lds\n"
"v_mfma_f32_16x16x32_bf16 v[24:27], a[68:71], a[136:139], v[24:27]\n"
"buffer_load_dwordx4 v135, s[32:35], s99 offen offset:2048 lds\n"
"s_add_u32 m0, s65, 0x14000\n"
"s_nop 0\n"
"buffer_load_dwordx4 v240, s[4:7], s70 offen lds\n"
"v_mfma_f32_16x16x32_bf16 v[8:11], a[64:67], a[140:143], v[8:11]\n"
"s_add_u32 m0, s65, 0x14400\n"
"ds_read_b128 a[120:123], v174 offset:4096\n"
"s_nop 0\n"
"buffer_load_dwordx4 v241, s[4:7], s70 offen lds\n"
"v_mfma_f32_16x16x32_bf16 v[28:31], a[68:71], a[140:143], v[28:31]\n"
"s_mul_i32 s89, s1, 0x1000\n"
"s_add_u32 s89, s89, s92\n"
"s_add_u32 s89, s89, 0xf800\n"
"s_add_u32 s74, s89, s0\n"
"buffer_load_dwordx4 a[172:175], v128, s[24:27], s89 offen sc1\n"
"v_mfma_f32_16x16x32_bf16 v[12:15], a[64:67], a[144:147], v[12:15]\n"
"s_add_u32 m0, s65, 0x14800\n"
"ds_read_b128 a[124:127], v174 offset:6144\n"
"s_nop 0\n"
"buffer_load_dwordx4 v242, s[4:7], s70 offen lds\n"
"v_mfma_f32_16x16x32_bf16 v[32:35], a[68:71], a[144:147], v[32:35]\n"
"buffer_load_dwordx4 a[176:179], v128, s[24:27], s74 offen sc1\n"
"s_waitcnt vmcnt(12)\n"
"s_add_u32 s80, s76, 3\n"
"v_mov_b32 v230, s80\n"
"s_lshl_b32 s80, s66, 2\n"
"s_add_u32 s80, s80, s64\n"
"s_lshl_b32 s80, s80, 4\n"
"s_add_u32 s80, s80, s67\n"
"s_lshl_b32 s80, s80, 3\n"
"s_add_u32 s80, s80, 0x64000\n"
"buffer_store_dwordx2 v[230:231], v229, s[24:27], s80 offen sc1\n"
"v_mfma_f32_16x16x32_bf16 v[16:19], a[64:67], a[148:151], v[16:19]\n"
"ds_read_b128 a[128:131], v174 offset:8192\n"
"buffer_load_dwordx4 v[180:183], v188, s[24:27], 0 offen sc1\n"
"buffer_load_dwordx4 v[184:187], v188, s[24:27], 0 offen offset:16 sc1\n"
"v_mfma_f32_16x16x32_bf16 v[36:39], a[68:71], a[148:151], v[36:39]\n"
"s_waitcnt lgkmcnt(5)\n"
"buffer_load_dwordx4 v[40:43], v188, s[24:27], 0 offen offset:32 sc1\n"
"buffer_load_dwordx4 v[44:47], v188, s[24:27], 0 offen offset:48 sc1\n"
"v_mfma_f32_16x16x32_bf16 v[0:3], a[72:75], a[92:95], v[0:3]\n"
"ds_read_b128 a[132:135], v177\n"
"buffer_load_ushort v139, v131, s[56:59], 0 offen\n"
"buffer_load_ushort v132, v131, s[56:59], 0 offen offset:32\n"
"v_mfma_f32_16x16x32_bf16 v[20:23], a[76:79], a[92:95], v[20:23]\n"
"s_add_u32 m0, s88, 0x1e000\n"
"s_nop 0\n"
"buffer_load_dwordx4 v136, s[32:35], s99 offen offset:3072 lds\n"
"buffer_load_ushort v172, v131, s[56:59], 0 offen offset:64\n"
"v_mfma_f32_16x16x32_bf16 v[4:7], a[72:75], a[96:99], v[4:7]\n"
"ds_read_b128 a[136:139], v177 offset:2048\n"
"buffer_load_ushort v173, v131, s[56:59], 0 offen offset:96\n"
"buffer_load_ushort v226, v131, s[56:59], 0 offen offset:128\n"
"v_mfma_f32_16x16x32_bf16 v[24:27], a[76:79], a[96:99], v[24:27]\n"
"s_add_u32 m0, s88, 0x1f000\n"
"s_nop 0\n"
"buffer_load_dwordx4 v129, s[32:35], s99 offen lds\n"
"v_lshrrev_b32 v243, 2, v253\n"
"v_mfma_f32_16x16x32_bf16 v[8:11], a[72:75], a[100:103], v[8:11]\n"
"ds_read_b128 a[140:143], v177 offset:4096\n"
"v_mul_u32_u24 v243, 0xa0, v243\n"
"v_and_b32 v247, 3, v253\n"
"v_lshl_add_u32 v243, v247, 3, v243\n"
"v_add_u32 v243, s65, v243\n"
"v_mfma_f32_16x16x32_bf16 v[28:31], a[76:79], a[100:103], v[28:31]\n"
"buffer_load_dwordx4 a[180:183], v128, s[24:27], s89 offen offset:1024 sc1\n"
"v_add_u32 v243, 0x14000, v243\n"
"v_mfma_f32_16x16x32_bf16 v[12:15], a[72:75], a[104:107], v[12:15]\n"
"ds_read_b128 a[144:147], v177 offset:6144\n"
"v_mfma_f32_16x16x32_bf16 v[32:35], a[76:79], a[104:107], v[32:35]\n"
"buffer_load_dwordx4 a[184:187], v128, s[24:27], s74 offen offset:1024 sc1\n"
"v_mfma_f32_16x16x32_bf16 v[16:19], a[72:75], a[108:111], v[16:19]\n"
"ds_read_b128 a[148:151], v177 offset:8192\n"
"v_mfma_f32_16x16x32_bf16 v[36:39], a[76:79], a[108:111], v[36:39]\n"
"s_waitcnt lgkmcnt(5)\n"
"v_mfma_f32_16x16x32_bf16 v[0:3], a[80:83], a[112:115], v[0:3]\n"
"s_lshl_b32 s80, s98, 2\n"
"ds_read_b128 a[92:95], v174 offset:10240\n"
"s_add_u32 s80, s80, s64\n"
"s_lshl_b32 s80, s80, 7\n"
"s_add_u32 s80, s80, 0x64000\n"
"v_and_b32 v140, 15, v253\n"
"v_lshl_add_u32 v140, v140, 3, s80\n"
"v_bfe_u32 v141, v253, 4, 1\n"
"v_lshlrev_b32 v141, 13, v141\n"
"v_sub_u32 v140, v140, v141\n"
"v_mfma_f32_16x16x32_bf16 v[20:23], a[84:87], a[112:115], v[20:23]\n"
"buffer_load_dwordx4 a[188:191], v128, s[24:27], s89 offen offset:2048 sc1\n"
"buffer_load_dwordx2 v[142:143], v140, s[24:27], 0 offen sc1\n"
"v_mfma_f32_16x16x32_bf16 v[4:7], a[80:83], a[116:119], v[4:7]\n"
"ds_read_b128 a[96:99], v174 offset:12288\n"
"v_mfma_f32_16x16x32_bf16 v[24:27], a[84:87], a[116:119], v[24:27]\n"
"v_mfma_f32_16x16x32_bf16 v[8:11], a[80:83], a[120:123], v[8:11]\n"
"ds_read_b128 a[100:103], v174 offset:14336\n"
"v_mfma_f32_16x16x32_bf16 v[28:31], a[84:87], a[120:123], v[28:31]\n"
"buffer_load_dwordx4 a[192:195], v128, s[24:27], s74 offen offset:2048 sc1\n"
"v_mfma_f32_16x16x32_bf16 v[12:15], a[80:83], a[124:127], v[12:15]\n"
"ds_read_b128 a[104:107], v174 offset:16384\n"
"v_mfma_f32_16x16x32_bf16 v[32:35], a[84:87], a[124:127], v[32:35]\n"
"v_mfma_f32_16x16x32_bf16 v[16:19], a[80:83], a[128:131], v[16:19]\n"
"ds_read_b128 a[108:111], v174 offset:18432\n"
"v_mfma_f32_16x16x32_bf16 v[36:39], a[84:87], a[128:131], v[36:39]\n"
"s_waitcnt lgkmcnt(5)\n"
"v_mfma_f32_16x16x32_bf16 v[0:3], a[88:91], a[132:135], v[0:3]\n"
"ds_read_b128 a[112:115], v177 offset:10240\n"
"v_mfma_f32_16x16x32_bf16 v[20:23], a[152:155], a[132:135], v[20:23]\n"
"buffer_load_dwordx4 a[196:199], v128, s[24:27], s89 offen offset:3072 sc1\n"
"v_mfma_f32_16x16x32_bf16 v[4:7], a[88:91], a[136:139], v[4:7]\n"
"ds_read_b128 a[116:119], v177 offset:12288\n"
"v_mfma_f32_16x16x32_bf16 v[24:27], a[152:155], a[136:139], v[24:27]\n"
"v_mfma_f32_16x16x32_bf16 v[8:11], a[88:91], a[140:143], v[8:11]\n"
"ds_read_b128 a[120:123], v177 offset:14336\n"
"v_mfma_f32_16x16x32_bf16 v[28:31], a[152:155], a[140:143], v[28:31]\n"
"buffer_load_dwordx4 a[200:203], v128, s[24:27], s74 offen offset:3072 sc1\n"
"v_mfma_f32_16x16x32_bf16 v[12:15], a[88:91], a[144:147], v[12:15]\n"
"ds_read_b128 a[124:127], v177 offset:16384\n"
"v_mfma_f32_16x16x32_bf16 v[32:35], a[152:155], a[144:147], v[32:35]\n"
"v_mfma_f32_16x16x32_bf16 v[16:19], a[88:91], a[148:151], v[16:19]\n"
"ds_read_b128 a[128:131], v177 offset:18432\n"
"v_mfma_f32_16x16x32_bf16 v[36:39], a[152:155], a[148:151], v[36:39]\n"
"s_waitcnt lgkmcnt(5)\n"
"s_barrier\n"
"s_waitcnt vmcnt(30)\n"
"v_mfma_f32_16x16x32_bf16 v[0:3], a[156:159], a[92:95], v[0:3]\n"
"ds_read_b128 a[132:135], v174 offset:20480\n"
"s_waitcnt vmcnt(29)\n"
"v_mfma_f32_16x16x32_bf16 v[20:23], a[160:163], a[92:95], v[20:23]\n"
"s_lshl_b32 s99, s1, 7\n"
"s_add_u32 s99, s99, 0x300\n"
"s_add_u32 m0, s88, 0x23000\n"
"s_nop 0\n"
"buffer_load_dwordx4 v133, s[32:35], s99 offen lds\n"
"v_mfma_f32_16x16x32_bf16 v[4:7], a[156:159], a[96:99], v[4:7]\n"
"ds_read_b128 a[136:139], v174 offset:22528\n"
"s_waitcnt vmcnt(21)\n"
"ds_read_b64_tr_b16 v[48:49], v243\n"
"ds_read_b64_tr_b16 v[50:51], v243 offset:32\n"
"ds_read_b64_tr_b16 v[52:53], v243 offset:64\n"
"ds_read_b64_tr_b16 v[54:55], v243 offset:96\n"
"v_mfma_f32_16x16x32_bf16 v[24:27], a[160:163], a[96:99], v[24:27]\n"
"buffer_load_dwordx4 v134, s[32:35], s99 offen offset:1024 lds\n"
"ds_read_b64_tr_b16 v[56:57], v243 offset:128\n"
"v_mfma_f32_16x16x32_bf16 v[8:11], a[156:159], a[100:103], v[8:11]\n"
"ds_read_b128 a[140:143], v174 offset:24576\n"
"s_waitcnt vmcnt(19)\n"
"v_mul_f32 v180, v180, v182\n"
"s_waitcnt vmcnt(18)\n"
"v_mul_f32 v184, v184, v186\n"
"s_waitcnt vmcnt(17)\n"
"v_mul_f32 v40, v40, v42\n"
"s_waitcnt vmcnt(16)\n"
"v_mul_f32 v44, v44, v46\n"
"v_mfma_f32_16x16x32_bf16 v[28:31], a[160:163], a[100:103], v[28:31]\n"
"buffer_load_dwordx4 v135, s[32:35], s99 offen offset:2048 lds\n"
"s_waitcnt vmcnt(16)\n"
"v_lshlrev_b32 v139, 16, v139\n"
"v_mfma_f32_16x16x32_bf16 v[12:15], a[156:159], a[104:107], v[12:15]\n"
"ds_read_b128 a[144:147], v174 offset:26624\n"
"s_waitcnt vmcnt(15)\n"
"v_lshlrev_b32 v132, 16, v132\n"
"s_waitcnt vmcnt(13)\n"
"v_lshlrev_b32 v172, 16, v172\n"
"s_waitcnt vmcnt(12)\n"
"v_lshlrev_b32 v173, 16, v173\n"
"s_waitcnt vmcnt(11)\n"
"v_lshlrev_b32 v226, 16, v226\n"
"v_mfma_f32_16x16x32_bf16 v[32:35], a[160:163], a[104:107], v[32:35]\n"
"s_waitcnt lgkmcnt(6)\n"
"v_lshlrev_b32 v152, 16, v48\n"
"v_and_b32 v153, 0xffff0000, v48\n"
"v_lshlrev_b32 v154, 16, v49\n"
"v_and_b32 v155, 0xffff0000, v49\n"
"v_fma_f32 v240, v152, v182, -v180\n"
"v_mfma_f32_16x16x32_bf16 v[16:19], a[156:159], a[108:111], v[16:19]\n"
"ds_read_b128 a[148:151], v174 offset:28672\n"
"v_fma_f32 v241, v153, v186, -v184\n"
"v_fma_f32 v242, v154, v42, -v40\n"
"v_fma_f32 v243, v155, v46, -v44\n"
"v_fmac_f32 v152, v240, v139\n"
"v_mfma_f32_16x16x32_bf16 v[36:39], a[160:163], a[108:111], v[36:39]\n"
"v_fmac_f32 v153, v241, v139\n"
"v_fmac_f32 v154, v242, v139\n"
"v_fmac_f32 v155, v243, v139\n"
"s_waitcnt lgkmcnt(6)\n"
"v_lshlrev_b32 v156, 16, v50\n"
"v_and_b32 v157, 0xffff0000, v50\n"
"v_mfma_f32_16x16x32_bf16 v[0:3], a[164:167], a[112:115], v[0:3]\n"
"ds_read_b128 a[92:95], v177 offset:20480\n"
"v_lshlrev_b32 v158, 16, v51\n"
"v_and_b32 v159, 0xffff0000, v51\n"
"v_fma_f32 v240, v156, v182, -v180\n"
"v_fma_f32 v241, v157, v186, -v184\n"
"v_mfma_f32_16x16x32_bf16 v[20:23], a[168:171], a[112:115], v[20:23]\n"
"buffer_load_dwordx4 v136, s[32:35], s99 offen offset:3072 lds\n"
"v_fma_f32 v242, v158, v42, -v40\n"
"v_mfma_f32_16x16x32_bf16 v[4:7], a[164:167], a[116:119], v[4:7]\n"
"s_add_u32 s80, s76, 2\n"
"ds_read_b128 a[96:99], v177 offset:22528\n"
"s_waitcnt vmcnt(7)\n"
"v_subrev_u32 v141, s80, v142\n"
"v_cmp_lt_u32 vcc, 1, v141\n"
"v_cmp_ne_u32 s[82:83], s77, v143\n"
"s_nop 0\n"
"s_or_b32 s82, s82, vcc_lo\n"
"s_and_b32 s82, s82, 0xffff07ff\n"
"s_cbranch_scc1 L%=_f2dS_slow\n"
"L%=_f2dS_back:\n"
"v_mfma_f32_16x16x32_bf16 v[24:27], a[168:171], a[116:119], v[24:27]\n"
"v_fma_f32 v243, v159, v46, -v44\n"
"v_fmac_f32 v156, v240, v132\n"
"v_fmac_f32 v157, v241, v132\n"
"v_fmac_f32 v158, v242, v132\n"
"v_fmac_f32 v159, v243, v132\n"
"v_mfma_f32_16x16x32_bf16 v[8:11], a[164:167], a[120:123], v[8:11]\n"
"ds_read_b128 a[100:103], v177 offset:24576\n"
"s_waitcnt lgkmcnt(8)\n"
"v_lshlrev_b32 v160, 16, v52\n"
"v_and_b32 v161, 0xffff0000, v52\n"
"v_lshlrev_b32 v162, 16, v53\n"
"v_and_b32 v163, 0xffff0000, v53\n"
"v_mfma_f32_16x16x32_bf16 v[28:31], a[168:171], a[120:123], v[28:31]\n"
"s_add_u32 m0, s88, 0x24000\n"
"s_nop 0\n"
"buffer_load_dwordx4 v129, s[32:35], s99 offen lds\n"
"v_fma_f32 v240, v160, v182, -v180\n"
"v_mfma_f32_16x16x32_bf16 v[12:15], a[164:167], a[124:127], v[12:15]\n"
"ds_read_b128 a[104:107], v177 offset:26624\n"
"v_fma_f32 v241, v161, v186, -v184\n"
"v_fma_f32 v242, v162, v42, -v40\n"
"v_fma_f32 v243, v163, v46, -v44\n"
"v_fmac_f32 v160, v240, v172\n"
"v_mfma_f32_16x16x32_bf16 v[32:35], a[168:171], a[124:127], v[32:35]\n"
"v_fmac_f32 v161, v241, v172\n"
"v_fmac_f32 v162, v242, v172\n"
"v_fmac_f32 v163, v243, v172\n"
"s_waitcnt lgkmcnt(8)\n"
"v_lshlrev_b32 v164, 16, v54\n"
"v_and_b32 v165, 0xffff0000, v54\n"
"v_mfma_f32_16x16x32_bf16 v[16:19], a[164:167], a[128:131], v[16:19]\n"
"ds_read_b128 a[108:111], v177 offset:28672\n"
"v_lshlrev_b32 v166, 16, v55\n"
"v_and_b32 v167, 0xffff0000, v55\n"
"v_fma_f32 v240, v164, v182, -v180\n"
"v_fma_f32 v241, v165, v186, -v184\n"
"v_mfma_f32_16x16x32_bf16 v[36:39], a[168:171], a[128:131], v[36:39]\n"
"s_waitcnt lgkmcnt(5)\n"
"v_fma_f32 v242, v166, v42, -v40\n"
"v_fma_f32 v243, v167, v46, -v44\n"
"v_fmac_f32 v164, v240, v173\n"
"v_fmac_f32 v165, v241, v173\n"
"v_fmac_f32 v166, v242, v173\n"
"v_mfma_f32_16x16x32_bf16 v[0:3], a[172:175], a[132:135], v[0:3]\n"
"ds_read_b128 a[112:115], v174 offset:30720\n"
"v_fmac_f32 v167, v243, v173\n"
"v_lshlrev_b32 v168, 16, v56\n"
"v_and_b32 v169, 0xffff0000, v56\n"
"v_lshlrev_b32 v170, 16, v57\n"
"v_mfma_f32_16x16x32_bf16 v[20:23], a[176:179], a[132:135], v[20:23]\n"
"s_mul_i32 s74, s1, 0x2c00\n"
"s_add_u32 s74, s74, s92\n"
"s_add_u32 s74, s74, 0x5800\n"
"s_add_u32 s74, s74, s0\n"
"buffer_load_dwordx4 a[204:207], v128, s[24:27], s74 offen sc1\n"
"v_mfma_f32_16x16x32_bf16 v[4:7], a[172:175], a[136:139], v[4:7]\n"
"ds_read_b128 a[116:119], v174 offset:32768\n"
"v_and_b32 v171, 0xffff0000, v57\n"
"v_fma_f32 v240, v168, v182, -v180\n"
"v_fma_f32 v241, v169, v186, -v184\n"
"v_fma_f32 v242, v170, v42, -v40\n"
"v_mfma_f32_16x16x32_bf16 v[24:27], a[176:179], a[136:139], v[24:27]\n"
"buffer_load_dwordx4 a[208:211], v128, s[24:27], s74 offen offset:1024 sc1\n"
"v_fma_f32 v243, v171, v46, -v44\n"
"v_fmac_f32 v168, v240, v226\n"
"v_mfma_f32_16x16x32_bf16 v[8:11], a[172:175], a[140:143], v[8:11]\n"
"ds_read_b128 a[120:123], v174 offset:34816\n"
"v_fmac_f32 v169, v241, v226\n"
"v_fmac_f32 v170, v242, v226\n"
"v_fmac_f32 v171, v243, v226\n"
"v_mfma_f32_16x16x32_bf16 v[28:31], a[176:179], a[140:143], v[28:31]\n"
"buffer_load_dwordx4 a[212:215], v128, s[24:27], s74 offen offset:2048 sc1\n"
"v_mfma_f32_16x16x32_bf16 v[12:15], a[172:175], a[144:147], v[12:15]\n"
"ds_read_b128 a[124:127], v174 offset:36864\n"
"v_mfma_f32_16x16x32_bf16 v[32:35], a[176:179], a[144:147], v[32:35]\n"
"buffer_load_dwordx4 a[216:219], v128, s[24:27], s74 offen offset:3072 sc1\n"
"v_mfma_f32_16x16x32_bf16 v[16:19], a[172:175], a[148:151], v[16:19]\n"
"ds_read_b128 a[128:131], v174 offset:38912\n"
"v_mfma_f32_16x16x32_bf16 v[36:39], a[176:179], a[148:151], v[36:39]\n"
"s_waitcnt lgkmcnt(5)\n"
"v_mfma_f32_16x16x32_bf16 v[0:3], a[180:183], a[92:95], v[0:3]\n"
"ds_read_b128 a[132:135], v177 offset:30720\n"
"v_mfma_f32_16x16x32_bf16 v[20:23], a[184:187], a[92:95], v[20:23]\n"
"s_mul_i32 s74, s1, 0x2c00\n"
"s_add_u32 s74, s74, s92\n"
"s_add_u32 s74, s74, 0x6800\n"
"s_add_u32 s74, s74, s0\n"
"buffer_load_dwordx4 a[220:223], v128, s[24:27], s74 offen sc1\n"
"v_mfma_f32_16x16x32_bf16 v[4:7], a[180:183], a[96:99], v[4:7]\n"
"ds_read_b128 a[136:139], v177 offset:32768\n"
"v_mfma_f32_16x16x32_bf16 v[24:27], a[184:187], a[96:99], v[24:27]\n"
"buffer_load_dwordx4 a[224:227], v128, s[24:27], s74 offen offset:1024 sc1\n"
"v_mfma_f32_16x16x32_bf16 v[8:11], a[180:183], a[100:103], v[8:11]\n"
"ds_read_b128 a[140:143], v177 offset:34816\n"
"v_mfma_f32_16x16x32_bf16 v[28:31], a[184:187], a[100:103], v[28:31]\n"
"buffer_load_dwordx4 a[228:231], v128, s[24:27], s74 offen offset:2048 sc1\n"
"v_mfma_f32_16x16x32_bf16 v[12:15], a[180:183], a[104:107], v[12:15]\n"
"ds_read_b128 a[144:147], v177 offset:36864\n"
"v_mfma_f32_16x16x32_bf16 v[32:35], a[184:187], a[104:107], v[32:35]\n"
"buffer_load_dwordx4 a[232:235], v128, s[24:27], s74 offen offset:3072 sc1\n"
"v_mfma_f32_16x16x32_bf16 v[16:19], a[180:183], a[108:111], v[16:19]\n"
"ds_read_b128 a[148:151], v177 offset:38912\n"
"v_mfma_f32_16x16x32_bf16 v[36:39], a[184:187], a[108:111], v[36:39]\n"
"s_waitcnt lgkmcnt(5)\n"
"s_barrier\n"
"v_mfma_f32_16x16x32_bf16 v[0:3], a[188:191], a[112:115], v[0:3]\n"
"ds_read_b128 a[92:95], v137 offset:36864\n"
"s_waitcnt vmcnt(15)\n"
"v_mfma_f32_16x16x32_bf16 v[20:23], a[192:195], a[112:115], v[20:23]\n"
"s_lshl_b32 s99, s1, 7\n"
"s_add_u32 s99, s99, 0x400\n"
"s_add_u32 m0, s88, 0x14000\n"
"s_nop 0\n"
"buffer_load_dwordx4 v133, s[32:35], s99 offen lds\n"
"v_mfma_f32_16x16x32_bf16 v[4:7], a[188:191], a[116:119], v[4:7]\n"
"ds_read_b128 a[96:99], v137 offset:37888\n"
"buffer_load_dwordx4 v134, s[32:35], s99 offen offset:1024 lds\n"
"v_mfma_f32_16x16x32_bf16 v[24:27], a[192:195], a[116:119], v[24:27]\n"
"buffer_load_dwordx4 v135, s[32:35], s99 offen offset:2048 lds\n"
"v_mfma_f32_16x16x32_bf16 v[8:11], a[188:191], a[120:123], v[8:11]\n"
"ds_read_b128 a[100:103], v137 offset:38912\n"
"v_mfma_f32_16x16x32_bf16 v[28:31], a[192:195], a[120:123], v[28:31]\n"
"s_mul_i32 s74, s1, 0x2c00\n"
"s_add_u32 s74, s74, s92\n"
"s_add_u32 s74, s74, 0x7800\n"
"s_add_u32 s74, s74, s0\n"
"buffer_load_dwordx4 a[236:239], v128, s[24:27], s74 offen sc1\n"
"v_mfma_f32_16x16x32_bf16 v[12:15], a[188:191], a[124:127], v[12:15]\n"
"ds_read_b128 a[104:107], v137 offset:39936\n"
"v_mfma_f32_16x16x32_bf16 v[32:35], a[192:195], a[124:127], v[32:35]\n"
"buffer_load_dwordx4 a[240:243], v128, s[24:27], s74 offen offset:1024 sc1\n"
"v_mfma_f32_16x16x32_bf16 v[16:19], a[188:191], a[128:131], v[16:19]\n"
"ds_read_b128 a[108:111], v137 offset:40960\n"
"v_mfma_f32_16x16x32_bf16 v[36:39], a[192:195], a[128:131], v[36:39]\n"
"s_waitcnt vmcnt(18) lgkmcnt(5)\n"
"v_mfma_f32_16x16x32_bf16 v[0:3], a[196:199], a[132:135], v[0:3]\n"
"ds_read_b128 a[112:115], v137 offset:41984\n"
"v_mfma_f32_16x16x32_bf16 v[20:23], a[200:203], a[132:135], v[20:23]\n"
"buffer_load_dwordx4 v136, s[32:35], s99 offen offset:3072 lds\n"
"v_mfma_f32_16x16x32_bf16 v[4:7], a[196:199], a[136:139], v[4:7]\n"
"ds_read_b128 a[116:119], v137 offset:43008\n"
"v_mfma_f32_16x16x32_bf16 v[24:27], a[200:203], a[136:139], v[24:27]\n"
"s_add_u32 m0, s88, 0x15000\n"
"s_nop 0\n"
"buffer_load_dwordx4 v129, s[32:35], s99 offen lds\n"
"v_mfma_f32_16x16x32_bf16 v[8:11], a[196:199], a[140:143], v[8:11]\n"
"ds_read_b128 a[120:123], v137 offset:44032\n"
"v_mfma_f32_16x16x32_bf16 v[28:31], a[200:203], a[140:143], v[28:31]\n"
"buffer_load_dwordx4 a[244:247], v128, s[24:27], s74 offen offset:2048 sc1\n"
"v_mfma_f32_16x16x32_bf16 v[12:15], a[196:199], a[144:147], v[12:15]\n"
"ds_read_b128 a[124:127], v137 offset:45056\n"
"v_mfma_f32_16x16x32_bf16 v[32:35], a[200:203], a[144:147], v[32:35]\n"
"s_mul_i32 s74, s1, 0x1000\n"
"s_add_u32 s74, s74, s92\n"
"s_add_u32 s74, s74, 0x11800\n"
"s_add_u32 s74, s74, s0\n"
"buffer_load_dwordx4 a[248:251], v128, s[24:27], s74 offen sc1\n"
"v_mfma_f32_16x16x32_bf16 v[16:19], a[196:199], a[148:151], v[16:19]\n"
"ds_read_b128 a[128:131], v137 offset:46080\n"
"v_mfma_f32_16x16x32_bf16 v[36:39], a[200:203], a[148:151], v[36:39]\n"
"s_waitcnt vmcnt(16) lgkmcnt(5)\n"
"v_mfma_f32_16x16x32_bf16 v[20:23], a[204:207], a[92:95], v[20:23]\n"
"ds_read_b128 a[132:135], v137 offset:47104\n"
"v_mfma_f32_16x16x32_bf16 v[24:27], a[204:207], a[96:99], v[24:27]\n"
"ds_read_b128 a[136:139], v137 offset:48128\n"
"buffer_load_dwordx4 a[252:255], v128, s[24:27], s74 offen offset:1024 sc1\n"
"v_mfma_f32_16x16x32_bf16 v[28:31], a[204:207], a[100:103], v[28:31]\n"
"ds_read_b128 a[140:143], v137 offset:49152\n"
"v_mfma_f32_16x16x32_bf16 v[32:35], a[204:207], a[104:107], v[32:35]\n"
"ds_read_b128 a[144:147], v137 offset:50176\n"
"v_mfma_f32_16x16x32_bf16 v[36:39], a[204:207], a[108:111], v[36:39]\n"
"ds_read_b128 a[148:151], v137 offset:51200\n"
"s_waitcnt vmcnt(16) lgkmcnt(5)\n"
"v_mfma_f32_16x16x32_bf16 v[20:23], a[208:211], a[112:115], v[20:23]\n"
"ds_read_b128 a[92:95], v137 offset:52224\n"
"v_mfma_f32_16x16x32_bf16 v[24:27], a[208:211], a[116:119], v[24:27]\n"
"ds_read_b128 a[96:99], v137 offset:53248\n"
"buffer_load_dwordx4 a[0:3], v128, s[24:27], s74 offen offset:2048 sc1\n"
"v_mfma_f32_16x16x32_bf16 v[28:31], a[208:211], a[120:123], v[28:31]\n"
"ds_read_b128 a[100:103], v137 offset:54272\n"
"v_mfma_f32_16x16x32_bf16 v[32:35], a[208:211], a[124:127], v[32:35]\n"
"ds_read_b128 a[104:107], v137 offset:55296\n"
"v_mfma_f32_16x16x32_bf16 v[36:39], a[208:211], a[128:131], v[36:39]\n"
"ds_read_b128 a[108:111], v137 offset:56320\n"
"s_barrier\n"
"s_waitcnt lgkmcnt(5)\n"
"s_waitcnt vmcnt(16)\n"
"v_mfma_f32_16x16x32_bf16 v[20:23], a[212:215], a[132:135], v[20:23]\n"
"ds_read_b128 a[112:115], v128 offset:40960\n"
"v_mfma_f32_16x16x32_bf16 v[24:27], a[212:215], a[136:139], v[24:27]\n"
"ds_read_b128 a[116:119], v128 offset:41984\n"
"buffer_load_dwordx4 a[4:7], v128, s[24:27], s74 offen offset:3072 sc1\n"
"v_mfma_f32_16x16x32_bf16 v[28:31], a[212:215], a[140:143], v[28:31]\n"
"ds_read_b128 a[120:123], v128 offset:43008\n"
"v_mfma_f32_16x16x32_bf16 v[32:35], a[212:215], a[144:147], v[32:35]\n"
"ds_read_b128 a[124:127], v128 offset:44032\n"
"v_mfma_f32_16x16x32_bf16 v[36:39], a[212:215], a[148:151], v[36:39]\n"
"ds_read_b128 a[128:131], v128 offset:45056\n"
"s_waitcnt vmcnt(16) lgkmcnt(5)\n"
"v_mfma_f32_16x16x32_bf16 v[20:23], a[216:219], a[92:95], v[20:23]\n"
"ds_read_b128 a[132:135], v128 offset:46080\n"
"v_mfma_f32_16x16x32_bf16 v[24:27], a[216:219], a[96:99], v[24:27]\n"
"s_mul_i32 s74, s1, 0x1000\n"
"s_add_u32 s74, s74, s92\n"
"s_add_u32 s74, s74, 0x13800\n"
"s_add_u32 s74, s74, s0\n"
"ds_read_b128 a[136:139], v128 offset:47104\n"
"buffer_load_dwordx4 a[8:11], v128, s[24:27], s74 offen sc1\n"
"v_mfma_f32_16x16x32_bf16 v[28:31], a[216:219], a[100:103], v[28:31]\n"
"ds_read_b128 a[140:143], v128 offset:48128\n"
"v_mfma_f32_16x16x32_bf16 v[32:35], a[216:219], a[104:107], v[32:35]\n"
"ds_read_b128 a[144:147], v128 offset:49152\n"
"v_mfma_f32_16x16x32_bf16 v[36:39], a[216:219], a[108:111], v[36:39]\n"
"ds_read_b128 a[148:151], v128 offset:50176\n"
"s_waitcnt vmcnt(16) lgkmcnt(5)\n"
"v_mfma_f32_16x16x32_bf16 v[20:23], a[220:223], a[112:115], v[20:23]\n"
"s_lshl_b32 s80, s98, 2\n"
"ds_read_b128 a[92:95], v128 offset:51200\n"
"s_add_u32 s80, s80, s64\n"
"s_lshl_b32 s80, s80, 7\n"
"s_add_u32 s80, s80, 0x64000\n"
"v_and_b32 v140, 15, v253\n"
"v_lshl_add_u32 v140, v140, 3, s80\n"
"v_mfma_f32_16x16x32_bf16 v[24:27], a[220:223], a[116:119], v[24:27]\n"
"ds_read_b128 a[96:99], v128 offset:52224\n"
"buffer_load_dwordx4 a[12:15], v128, s[24:27], s74 offen offset:1024 sc1\n"
"v_mfma_f32_16x16x32_bf16 v[28:31], a[220:223], a[120:123], v[28:31]\n"
"ds_read_b128 a[100:103], v128 offset:53248\n"
"buffer_load_dwordx2 v[142:143], v140, s[24:27], 0 offen sc1\n"
"v_mfma_f32_16x16x32_bf16 v[32:35], a[220:223], a[124:127], v[32:35]\n"
"ds_read_b128 a[104:107], v128 offset:54272\n"
"v_mfma_f32_16x16x32_bf16 v[36:39], a[220:223], a[128:131], v[36:39]\n"
"ds_read_b128 a[108:111], v128 offset:55296\n"
"s_waitcnt vmcnt(17) lgkmcnt(5)\n"
"v_mfma_f32_16x16x32_bf16 v[20:23], a[224:227], a[132:135], v[20:23]\n"
"ds_read_b128 a[112:115], v128 offset:56320\n"
"v_mfma_f32_16x16x32_bf16 v[24:27], a[224:227], a[136:139], v[24:27]\n"
"ds_read_b128 a[116:119], v128 offset:57344\n"
"buffer_load_dwordx4 a[16:19], v128, s[24:27], s74 offen offset:2048 sc1\n"
"v_mfma_f32_16x16x32_bf16 v[28:31], a[224:227], a[140:143], v[28:31]\n"
"ds_read_b128 a[120:123], v128 offset:58368\n"
"v_mfma_f32_16x16x32_bf16 v[32:35], a[224:227], a[144:147], v[32:35]\n"
"ds_read_b128 a[124:127], v128 offset:59392\n"
"v_mfma_f32_16x16x32_bf16 v[36:39], a[224:227], a[148:151], v[36:39]\n"
"ds_read_b128 a[128:131], v128 offset:60416\n"
"s_waitcnt vmcnt(17) lgkmcnt(5)\n"
"v_mfma_f32_16x16x32_bf16 v[20:23], a[228:231], a[92:95], v[20:23]\n"
"ds_read_b128 a[132:135], v128 offset:61440\n"
"v_mfma_f32_16x16x32_bf16 v[24:27], a[228:231], a[96:99], v[24:27]\n"
"ds_read_b128 a[136:139], v128 offset:62464\n"
"buffer_load_dwordx4 a[20:23], v128, s[24:27], s74 offen offset:3072 sc1\n"
"v_mfma_f32_16x16x32_bf16 v[28:31], a[228:231], a[100:103], v[28:31]\n"
"ds_read_b128 a[140:143], v128 offset:63488\n"
"v_mfma_f32_16x16x32_bf16 v[32:35], a[228:231], a[104:107], v[32:35]\n"
"ds_read_b128 a[144:147], v128 offset:64512\n"
"v_mfma_f32_16x16x32_bf16 v[36:39], a[228:231], a[108:111], v[36:39]\n"
"ds_read_b128 a[148:151], v137\n"
"s_waitcnt vmcnt(17) lgkmcnt(5)\n"
"v_mfma_f32_16x16x32_bf16 v[20:23], a[232:235], a[112:115], v[20:23]\n"
"ds_read_b128 a[92:95], v137 offset:1024\n"
"v_mfma_f32_16x16x32_bf16 v[24:27], a[232:235], a[116:119], v[24:27]\n"
"ds_read_b128 a[96:99], v137 offset:2048\n"
"v_mfma_f32_16x16x32_bf16 v[28:31], a[232:235], a[120:123], v[28:31]\n"
"ds_read_b128 a[100:103], v137 offset:3072\n"
"v_mfma_f32_16x16x32_bf16 v[32:35], a[232:235], a[124:127], v[32:35]\n"
"ds_read_b128 a[104:107], v137 offset:4096\n"
"v_mfma_f32_16x16x32_bf16 v[36:39], a[232:235], a[128:131], v[36:39]\n"
"ds_read_b128 a[108:111], v137 offset:5120\n"
"s_waitcnt vmcnt(13) lgkmcnt(5)\n"
"v_mfma_f32_16x16x32_bf16 v[20:23], a[236:239], a[132:135], v[20:23]\n"
"ds_read_b128 a[112:115], v137 offset:6144\n"
"v_mfma_f32_16x16x32_bf16 v[24:27], a[236:239], a[136:139], v[24:27]\n"
"ds_read_b128 a[116:119], v137 offset:7168\n"
"v_mfma_f32_16x16x32_bf16 v[28:31], a[236:239], a[140:143], v[28:31]\n"
"ds_read_b128 a[120:123], v137 offset:8192\n"
"v_mfma_f32_16x16x32_bf16 v[32:35], a[236:239], a[144:147], v[32:35]\n"
"ds_read_b128 a[124:127], v137 offset:9216\n"
"v_mfma_f32_16x16x32_bf16 v[36:39], a[236:239], a[148:151], v[36:39]\n"
"ds_read_b128 a[128:131], v137 offset:10240\n"
"s_waitcnt vmcnt(10)\n"
"s_barrier\n"
"s_waitcnt lgkmcnt(5)\n"
"v_mfma_f32_16x16x32_bf16 v[20:23], a[240:243], a[92:95], v[20:23]\n"
"ds_read_b128 a[132:135], v175 offset:57344\n"
"v_mfma_f32_16x16x32_bf16 v[24:27], a[240:243], a[96:99], v[24:27]\n"
"ds_read_b128 a[136:139], v175 offset:59392\n"
"v_mfma_f32_16x16x32_bf16 v[28:31], a[240:243], a[100:103], v[28:31]\n"
"ds_read_b128 a[140:143], v175 offset:61440\n"
"v_mfma_f32_16x16x32_bf16 v[32:35], a[240:243], a[104:107], v[32:35]\n"
"ds_read_b128 a[144:147], v175 offset:63488\n"
"v_mfma_f32_16x16x32_bf16 v[36:39], a[240:243], a[108:111], v[36:39]\n"
"ds_read_b128 a[148:151], v176\n"
"s_waitcnt vmcnt(9) lgkmcnt(5)\n"
"v_mfma_f32_16x16x32_bf16 v[20:23], a[244:247], a[112:115], v[20:23]\n"
"ds_read_b128 a[92:95], v178 offset:57344\n"
"v_mfma_f32_16x16x32_bf16 v[24:27], a[244:247], a[116:119], v[24:27]\n"
"ds_read_b128 a[96:99], v178 offset:59392\n"
"v_mfma_f32_16x16x32_bf16 v[28:31], a[244:247], a[120:123], v[28:31]\n"
"ds_read_b128 a[100:103], v178 offset:61440\n"
"v_mfma_f32_16x16x32_bf16 v[32:35], a[244:247], a[124:127], v[32:35]\n"
"ds_read_b128 a[104:107], v178 offset:63488\n"
"v_mfma_f32_16x16x32_bf16 v[36:39], a[244:247], a[128:131], v[36:39]\n"
"ds_read_b128 a[108:111], v179\n"
"s_waitcnt vmcnt(8) lgkmcnt(5)\n"
"v_mfma_f32_16x16x32_bf16 v[20:23], a[248:251], a[132:135], v[20:23]\n"
"ds_read_b128 a[112:115], v176 offset:2048\n"
"v_mfma_f32_16x16x32_bf16 v[24:27], a[248:251], a[136:139], v[24:27]\n"
"ds_read_b128 a[116:119], v176 offset:4096\n"
"v_mfma_f32_16x16x32_bf16 v[28:31], a[248:251], a[140:143], v[28:31]\n"
"s_add_u32 s80, s76, 3\n"
"ds_read_b128 a[120:123], v176 offset:6144\n"
"s_waitcnt vmcnt(2)\n"
"v_subrev_u32 v141, s80, v142\n"
"v_cmp_lt_u32 vcc, 0, v141\n"
"v_cmp_ne_u32 s[82:83], s77, v143\n"
"s_nop 0\n"
"s_or_b32 s82, s82, vcc_lo\n"
"s_and_b32 s82, s82, 0x7800\n"
"s_cbranch_scc1 L%=_f2eS_slow\n"
"L%=_f2eS_back:\n"
"v_mfma_f32_16x16x32_bf16 v[32:35], a[248:251], a[144:147], v[32:35]\n"
"ds_read_b128 a[124:127], v176 offset:8192\n"
"v_mfma_f32_16x16x32_bf16 v[36:39], a[248:251], a[148:151], v[36:39]\n"
"ds_read_b128 a[128:131], v176 offset:10240\n"
"s_waitcnt lgkmcnt(5)\n"
"v_mfma_f32_16x16x32_bf16 v[20:23], a[252:255], a[92:95], v[20:23]\n"
"ds_read_b128 a[132:135], v179 offset:2048\n"
"v_mfma_f32_16x16x32_bf16 v[24:27], a[252:255], a[96:99], v[24:27]\n"
"s_mul_i32 s74, s1, 0x1000\n"
"s_add_u32 s74, s74, s92\n"
"s_add_u32 s74, s74, 0x15800\n"
"s_add_u32 s74, s74, s0\n"
"ds_read_b128 a[136:139], v179 offset:4096\n"
"buffer_load_dwordx4 a[24:27], v128, s[24:27], s74 offen sc1\n"
"v_mfma_f32_16x16x32_bf16 v[28:31], a[252:255], a[100:103], v[28:31]\n"
"ds_read_b128 a[140:143], v179 offset:6144\n"
"v_mfma_f32_16x16x32_bf16 v[32:35], a[252:255], a[104:107], v[32:35]\n"
"ds_read_b128 a[144:147], v179 offset:8192\n"
"buffer_load_dwordx4 a[28:31], v128, s[24:27], s74 offen offset:1024 sc1\n"
"v_mfma_f32_16x16x32_bf16 v[36:39], a[252:255], a[108:111], v[36:39]\n"
"ds_read_b128 a[148:151], v179 offset:10240\n"
"s_waitcnt lgkmcnt(5)\n"
"v_mfma_f32_16x16x32_bf16 v[20:23], a[0:3], a[112:115], v[20:23]\n"
"ds_read_b128 a[92:95], v176 offset:12288\n"
"v_mfma_f32_16x16x32_bf16 v[24:27], a[0:3], a[116:119], v[24:27]\n"
"ds_read_b128 a[96:99], v176 offset:14336\n"
"buffer_load_dwordx4 a[32:35], v128, s[24:27], s74 offen offset:2048 sc1\n"
"v_mfma_f32_16x16x32_bf16 v[28:31], a[0:3], a[120:123], v[28:31]\n"
"ds_read_b128 a[100:103], v176 offset:16384\n"
"v_mfma_f32_16x16x32_bf16 v[32:35], a[0:3], a[124:127], v[32:35]\n"
"ds_read_b128 a[104:107], v176 offset:18432\n"
"buffer_load_dwordx4 a[36:39], v128, s[24:27], s74 offen offset:3072 sc1\n"
"v_mfma_f32_16x16x32_bf16 v[36:39], a[0:3], a[128:131], v[36:39]\n"
"ds_read_b128 a[108:111], v176 offset:20480\n"
"s_waitcnt lgkmcnt(5)\n"
"v_mfma_f32_16x16x32_bf16 v[20:23], a[4:7], a[132:135], v[20:23]\n"
"ds_read_b128 a[112:115], v179 offset:12288\n"
"v_mfma_f32_16x16x32_bf16 v[24:27], a[4:7], a[136:139], v[24:27]\n"
"s_mul_i32 s89, s1, 0x2c00\n"
"s_add_u32 s89, s89, s92\n"
"s_add_u32 s89, s89, 0x5800\n"
"ds_read_b128 a[116:119], v179 offset:14336\n"
"buffer_load_dwordx4 a[40:43], v128, s[24:27], s89 offen sc1\n"
"v_mfma_f32_16x16x32_bf16 v[28:31], a[4:7], a[140:143], v[28:31]\n"
"ds_read_b128 a[120:123], v179 offset:16384\n"
"v_mfma_f32_16x16x32_bf16 v[32:35], a[4:7], a[144:147], v[32:35]\n"
"ds_read_b128 a[124:127], v179 offset:18432\n"
"buffer_load_dwordx4 a[44:47], v128, s[24:27], s89 offen offset:1024 sc1\n"
"v_mfma_f32_16x16x32_bf16 v[36:39], a[4:7], a[148:151], v[36:39]\n"
"ds_read_b128 a[128:131], v179 offset:20480\n"
"s_waitcnt lgkmcnt(5)\n"
"v_mfma_f32_16x16x32_bf16 v[20:23], a[8:11], a[92:95], v[20:23]\n"
"ds_read_b128 a[132:135], v176 offset:22528\n"
"v_mov_b32 v65, s76\n"
"v_mov_b32 v67, s76\n"
"v_mfma_f32_16x16x32_bf16 v[24:27], a[8:11], a[96:99], v[24:27]\n"
"ds_read_b128 a[136:139], v176 offset:24576\n"
"buffer_load_dwordx4 a[48:51], v128, s[24:27], s89 offen offset:2048 sc1\n"
"v_mfma_f32_16x16x32_bf16 v[28:31], a[8:11], a[100:103], v[28:31]\n"
"ds_read_b128 a[140:143], v176 offset:26624\n"
"v_mov_b32 v69, s76\n"
"v_mov_b32 v71, s76\n"
"v_mfma_f32_16x16x32_bf16 v[32:35], a[8:11], a[104:107], v[32:35]\n"
"ds_read_b128 a[144:147], v176 offset:28672\n"
"buffer_load_dwordx4 a[52:55], v128, s[24:27], s89 offen offset:3072 sc1\n"
"v_mfma_f32_16x16x32_bf16 v[36:39], a[8:11], a[108:111], v[36:39]\n"
"ds_read_b128 a[148:151], v176 offset:30720\n"
"v_mov_b32 v73, s76\n"
"v_mov_b32 v75, s76\n"
"s_waitcnt lgkmcnt(5)\n"
"v_mfma_f32_16x16x32_bf16 v[20:23], a[12:15], a[112:115], v[20:23]\n"
"ds_read_b128 a[92:95], v179 offset:22528\n"
"v_mov_b32 v77, s76\n"
"v_mov_b32 v79, s76\n"
"v_mfma_f32_16x16x32_bf16 v[24:27], a[12:15], a[116:119], v[24:27]\n"
"s_mul_i32 s89, s1, 0x2c00\n"
"s_add_u32 s89, s89, s92\n"
"s_add_u32 s89, s89, 0x6800\n"
"ds_read_b128 a[96:99], v179 offset:24576\n"
"buffer_load_dwordx4 a[56:59], v128, s[24:27], s89 offen sc1\n"
"v_mfma_f32_16x16x32_bf16 v[28:31], a[12:15], a[120:123], v[28:31]\n"
"ds_read_b128 a[100:103], v179 offset:26624\n"
"v_mov_b32 v81, s76\n"
"v_mov_b32 v83, s76\n"
"v_mfma_f32_16x16x32_bf16 v[32:35], a[12:15], a[124:127], v[32:35]\n"
"ds_read_b128 a[104:107], v179 offset:28672\n"
"buffer_load_dwordx4 a[60:63], v128, s[24:27], s89 offen offset:1024 sc1\n"
"v_mfma_f32_16x16x32_bf16 v[36:39], a[12:15], a[128:131], v[36:39]\n"
"s_xor_b32 s80, s3, 4\n"
"ds_read_b128 a[108:111], v179 offset:30720\n"
"s_lshl_b32 s80, s80, 2\n"
"s_add_u32 s80, s80, s64\n"
"s_mul_i32 s80, s80, 0x1400\n"
"s_add_u32 s68, s80, 0x1000000\n"
"s_waitcnt vmcnt(11) lgkmcnt(5)\n"
"v_mfma_f32_16x16x32_bf16 v[20:23], a[16:19], a[132:135], v[20:23]\n"
"s_lshl_b32 s80, s3, 2\n"
"ds_read_b128 a[112:115], v175 offset:16384\n"
"s_add_u32 s80, s80, s64\n"
"s_mul_i32 s80, s80, 0x1400\n"
"s_add_u32 s69, s80, 0x1000000\n"
"v_mfma_f32_16x16x32_bf16 v[24:27], a[16:19], a[136:139], v[24:27]\n"
"ds_read_b128 a[116:119], v175 offset:18432\n"
"buffer_load_dwordx4 a[64:67], v128, s[24:27], s89 offen offset:2048 sc1\n"
"v_mfma_f32_16x16x32_bf16 v[28:31], a[16:19], a[140:143], v[28:31]\n"
"ds_read_b128 a[120:123], v175 offset:20480\n"
"v_mfma_f32_16x16x32_bf16 v[32:35], a[16:19], a[144:147], v[32:35]\n"
"ds_read_b128 a[124:127], v175 offset:22528\n"
"v_mfma_f32_16x16x32_bf16 v[36:39], a[16:19], a[148:151], v[36:39]\n"
"ds_read_b128 a[128:131], v175 offset:24576\n"
"s_waitcnt vmcnt(11) lgkmcnt(5)\n"
"v_mfma_f32_16x16x32_bf16 v[20:23], a[20:23], a[92:95], v[20:23]\n"
"ds_read_b128 a[132:135], v178 offset:16384\n"
"v_mfma_f32_16x16x32_bf16 v[24:27], a[20:23], a[96:99], v[24:27]\n"
"ds_read_b128 a[136:139], v178 offset:18432\n"
"buffer_load_dwordx4 a[68:71], v128, s[24:27], s89 offen offset:3072 sc1\n"
"v_mfma_f32_16x16x32_bf16 v[28:31], a[20:23], a[100:103], v[28:31]\n"
"ds_read_b128 a[140:143], v178 offset:20480\n"
"v_mfma_f32_16x16x32_bf16 v[32:35], a[20:23], a[104:107], v[32:35]\n"
"ds_read_b128 a[144:147], v178 offset:22528\n"
"v_mfma_f32_16x16x32_bf16 v[36:39], a[20:23], a[108:111], v[36:39]\n"
"ds_read_b128 a[148:151], v178 offset:24576\n"
"s_waitcnt vmcnt(11) lgkmcnt(5)\n"
"v_mfma_f32_16x16x32_bf16 v[20:23], a[24:27], a[112:115], v[20:23]\n"
"ds_read_b128 a[92:95], v175 offset:26624\n"
"v_mfma_f32_16x16x32_bf16 v[24:27], a[24:27], a[116:119], v[24:27]\n"
"s_mul_i32 s89, s1, 0x2c00\n"
"s_add_u32 s89, s89, s92\n"
"s_add_u32 s89, s89, 0x7800\n"
"ds_read_b128 a[96:99], v175 offset:28672\n"
"buffer_load_dwordx4 a[72:75], v128, s[24:27], s89 offen sc1\n"
"v_mfma_f32_16x16x32_bf16 v[28:31], a[24:27], a[120:123], v[28:31]\n"
"ds_read_b128 a[100:103], v175 offset:30720\n"
"v_mfma_f32_16x16x32_bf16 v[32:35], a[24:27], a[124:127], v[32:35]\n"
"ds_read_b128 a[104:107], v175 offset:32768\n"
"v_mfma_f32_16x16x32_bf16 v[36:39], a[24:27], a[128:131], v[36:39]\n"
"ds_read_b128 a[108:111], v175 offset:34816\n"
"s_waitcnt vmcnt(11) lgkmcnt(5)\n"
"v_mfma_f32_16x16x32_bf16 v[20:23], a[28:31], a[132:135], v[20:23]\n"
"ds_read_b128 a[112:115], v178 offset:26624\n"
"v_mfma_f32_16x16x32_bf16 v[24:27], a[28:31], a[136:139], v[24:27]\n"
"ds_read_b128 a[116:119], v178 offset:28672\n"
"buffer_load_dwordx4 a[76:79], v128, s[24:27], s89 offen offset:1024 sc1\n"
"v_mfma_f32_16x16x32_bf16 v[28:31], a[28:31], a[140:143], v[28:31]\n"
"ds_read_b128 a[120:123], v178 offset:30720\n"
"v_mfma_f32_16x16x32_bf16 v[32:35], a[28:31], a[144:147], v[32:35]\n"
"ds_read_b128 a[124:127], v178 offset:32768\n"
"v_mfma_f32_16x16x32_bf16 v[36:39], a[28:31], a[148:151], v[36:39]\n"
"ds_read_b128 a[128:131], v178 offset:34816\n"
"s_waitcnt vmcnt(11) lgkmcnt(5)\n"
"v_mfma_f32_16x16x32_bf16 v[20:23], a[32:35], a[92:95], v[20:23]\n"
"ds_read_b128 a[132:135], v137 offset:36864\n"
"v_mfma_f32_16x16x32_bf16 v[24:27], a[32:35], a[96:99], v[24:27]\n"
"ds_read_b128 a[136:139], v137 offset:37888\n"
"buffer_load_dwordx4 a[80:83], v128, s[24:27], s89 offen offset:2048 sc1\n"
"v_mfma_f32_16x16x32_bf16 v[28:31], a[32:35], a[100:103], v[28:31]\n"
"ds_read_b128 a[140:143], v137 offset:38912\n"
"v_mfma_f32_16x16x32_bf16 v[32:35], a[32:35], a[104:107], v[32:35]\n"
"ds_read_b128 a[144:147], v137 offset:39936\n"
"v_mfma_f32_16x16x32_bf16 v[36:39], a[32:35], a[108:111], v[36:39]\n"
"ds_read_b128 a[148:151], v137 offset:40960\n"
"s_waitcnt vmcnt(11) lgkmcnt(5)\n"
"v_mfma_f32_16x16x32_bf16 v[20:23], a[36:39], a[112:115], v[20:23]\n"
"ds_read_b128 a[92:95], v137 offset:41984\n"
"v_mfma_f32_16x16x32_bf16 v[24:27], a[36:39], a[116:119], v[24:27]\n"
"s_mul_i32 s89, s1, 0x1000\n"
"s_add_u32 s89, s89, s92\n"
"s_add_u32 s89, s89, 0x11800\n"
"ds_read_b128 a[96:99], v137 offset:43008\n"
"buffer_load_dwordx4 a[84:87], v128, s[24:27], s89 offen sc1\n"
"v_mfma_f32_16x16x32_bf16 v[28:31], a[36:39], a[120:123], v[28:31]\n"
"ds_read_b128 a[100:103], v137 offset:44032\n"
"v_mfma_f32_16x16x32_bf16 v[32:35], a[36:39], a[124:127], v[32:35]\n"
"ds_read_b128 a[104:107], v137 offset:45056\n"
"v_mfma_f32_16x16x32_bf16 v[36:39], a[36:39], a[128:131], v[36:39]\n"
"ds_read_b128 a[108:111], v137 offset:46080\n"
"s_waitcnt vmcnt(11) lgkmcnt(5)\n"
"v_mfma_f32_16x16x32_bf16 v[0:3], a[40:43], a[132:135], v[0:3]\n"
"ds_read_b128 a[112:115], v137 offset:47104\n"
"v_mfma_f32_16x16x32_bf16 v[4:7], a[40:43], a[136:139], v[4:7]\n"
"ds_read_b128 a[116:119], v137 offset:48128\n"
"buffer_load_dwordx4 a[88:91], v128, s[24:27], s89 offen offset:1024 sc1\n"
"v_mfma_f32_16x16x32_bf16 v[8:11], a[40:43], a[140:143], v[8:11]\n"
"ds_read_b128 a[120:123], v137 offset:49152\n"
"v_cvt_pk_bf16_f32 v64, v20, v21\n"
"v_cvt_pk_bf16_f32 v66, v22, v23\n"
"buffer_store_dwordx4 v[64:67], v128, s[24:27], s68 offen sc1\n"
"v_mfma_f32_16x16x32_bf16 v[12:15], a[40:43], a[144:147], v[12:15]\n"
"ds_read_b128 a[124:127], v137 offset:50176\n"
"v_cvt_pk_bf16_f32 v68, v24, v25\n"
"v_cvt_pk_bf16_f32 v70, v26, v27\n"
"buffer_store_dwordx4 v[68:71], v128, s[24:27], s68 offen offset:1024 sc1\n"
"v_mfma_f32_16x16x32_bf16 v[16:19], a[40:43], a[148:151], v[16:19]\n"
"ds_read_b128 a[128:131], v137 offset:51200\n"
"v_cvt_pk_bf16_f32 v72, v28, v29\n"
"v_cvt_pk_bf16_f32 v74, v30, v31\n"
"buffer_store_dwordx4 v[72:75], v128, s[24:27], s68 offen offset:2048 sc1\n"
"s_waitcnt vmcnt(14) lgkmcnt(5)\n"
"v_mfma_f32_16x16x32_bf16 v[0:3], a[44:47], a[92:95], v[0:3]\n"
"ds_read_b128 a[132:135], v137 offset:52224\n"
"v_cvt_pk_bf16_f32 v76, v32, v33\n"
"v_cvt_pk_bf16_f32 v78, v34, v35\n"
"buffer_store_dwordx4 v[76:79], v128, s[24:27], s68 offen offset:3072 sc1\n"
"v_mfma_f32_16x16x32_bf16 v[4:7], a[44:47], a[96:99], v[4:7]\n"
"ds_read_b128 a[136:139], v137 offset:53248\n"
"buffer_load_dwordx4 a[152:155], v128, s[24:27], s89 offen offset:2048 sc1\n"
"v_mfma_f32_16x16x32_bf16 v[8:11], a[44:47], a[100:103], v[8:11]\n"
"ds_read_b128 a[140:143], v137 offset:54272\n"
"v_cvt_pk_bf16_f32 v80, v36, v37\n"
"v_cvt_pk_bf16_f32 v82, v38, v39\n"
"buffer_store_dwordx4 v[80:83], v130, s[24:27], s68 offen sc1\n"
"v_mfma_f32_16x16x32_bf16 v[12:15], a[44:47], a[104:107], v[12:15]\n"
"ds_read_b128 a[144:147], v137 offset:55296\n"
"v_mfma_f32_16x16x32_bf16 v[16:19], a[44:47], a[108:111], v[16:19]\n"
"ds_read_b128 a[148:151], v137 offset:56320\n"
"s_waitcnt vmcnt(16) lgkmcnt(5)\n"
"v_mfma_f32_16x16x32_bf16 v[0:3], a[48:51], a[112:115], v[0:3]\n"
"ds_read_b128 a[92:95], v128 offset:40960\n"
"v_mfma_f32_16x16x32_bf16 v[4:7], a[48:51], a[116:119], v[4:7]\n"
"ds_read_b128 a[96:99], v128 offset:41984\n"
"buffer_load_dwordx4 a[156:159], v128, s[24:27], s89 offen offset:3072 sc1\n"
"v_mfma_f32_16x16x32_bf16 v[8:11], a[48:51], a[120:123], v[8:11]\n"
"ds_read_b128 a[100:103], v128 offset:43008\n"
"v_mfma_f32_16x16x32_bf16 v[12:15], a[48:51], a[124:127], v[12:15]\n"
"ds_read_b128 a[104:107], v128 offset:44032\n"
"v_mfma_f32_16x16x32_bf16 v[16:19], a[48:51], a[128:131], v[16:19]\n"
"ds_read_b128 a[108:111], v128 offset:45056\n"
"s_waitcnt vmcnt(16) lgkmcnt(5)\n"
"v_mfma_f32_16x16x32_bf16 v[0:3], a[52:55], a[132:135], v[0:3]\n"
"ds_read_b128 a[112:115], v128 offset:46080\n"
"v_mfma_f32_16x16x32_bf16 v[4:7], a[52:55], a[136:139], v[4:7]\n"
"s_mul_i32 s89, s1, 0x1000\n"
"s_add_u32 s89, s89, s92\n"
"s_add_u32 s89, s89, 0x13800\n"
"ds_read_b128 a[116:119], v128 offset:47104\n"
"buffer_load_dwordx4 a[160:163], v128, s[24:27], s89 offen sc1\n"
"v_mfma_f32_16x16x32_bf16 v[8:11], a[52:55], a[140:143], v[8:11]\n"
"ds_read_b128 a[120:123], v128 offset:48128\n"
"v_mfma_f32_16x16x32_bf16 v[12:15], a[52:55], a[144:147], v[12:15]\n"
"ds_read_b128 a[124:127], v128 offset:49152\n"
"v_mfma_f32_16x16x32_bf16 v[16:19], a[52:55], a[148:151], v[16:19]\n"
"ds_read_b128 a[128:131], v128 offset:50176\n"
"s_waitcnt vmcnt(16) lgkmcnt(5)\n"
"v_mfma_f32_16x16x32_bf16 v[0:3], a[56:59], a[92:95], v[0:3]\n"
"ds_read_b128 a[132:135], v128 offset:51200\n"
"v_mfma_f32_16x16x32_bf16 v[4:7], a[56:59], a[96:99], v[4:7]\n"
"ds_read_b128 a[136:139], v128 offset:52224\n"
"buffer_load_dwordx4 a[164:167], v128, s[24:27], s89 offen offset:1024 sc1\n"
"v_mfma_f32_16x16x32_bf16 v[8:11], a[56:59], a[100:103], v[8:11]\n"
"ds_read_b128 a[140:143], v128 offset:53248\n"
"v_mfma_f32_16x16x32_bf16 v[12:15], a[56:59], a[104:107], v[12:15]\n"
"ds_read_b128 a[144:147], v128 offset:54272\n"
"v_mfma_f32_16x16x32_bf16 v[16:19], a[56:59], a[108:111], v[16:19]\n"
"ds_read_b128 a[148:151], v128 offset:55296\n"
"s_waitcnt vmcnt(16) lgkmcnt(5)\n"
"v_mfma_f32_16x16x32_bf16 v[0:3], a[60:63], a[112:115], v[0:3]\n"
"ds_read_b128 a[92:95], v128 offset:56320\n"
"v_mfma_f32_16x16x32_bf16 v[4:7], a[60:63], a[116:119], v[4:7]\n"
"ds_read_b128 a[96:99], v128 offset:57344\n"
"buffer_load_dwordx4 a[168:171], v128, s[24:27], s89 offen offset:2048 sc1\n"
"v_mfma_f32_16x16x32_bf16 v[8:11], a[60:63], a[120:123], v[8:11]\n"
"ds_read_b128 a[100:103], v128 offset:58368\n"
"v_mfma_f32_16x16x32_bf16 v[12:15], a[60:63], a[124:127], v[12:15]\n"
"ds_read_b128 a[104:107], v128 offset:59392\n"
"buffer_load_dwordx4 a[172:175], v128, s[24:27], s89 offen offset:3072 sc1\n"
"v_mfma_f32_16x16x32_bf16 v[16:19], a[60:63], a[128:131], v[16:19]\n"
"ds_read_b128 a[108:111], v128 offset:60416\n"
"s_waitcnt vmcnt(17) lgkmcnt(5)\n"
"v_mfma_f32_16x16x32_bf16 v[0:3], a[64:67], a[132:135], v[0:3]\n"
"ds_read_b128 a[112:115], v128 offset:61440\n"
"v_mfma_f32_16x16x32_bf16 v[4:7], a[64:67], a[136:139], v[4:7]\n"
"s_mul_i32 s89, s1, 0x1000\n"
"s_add_u32 s89, s89, s92\n"
"s_add_u32 s89, s89, 0x15800\n"
"ds_read_b128 a[116:119], v128 offset:62464\n"
"buffer_load_dwordx4 a[176:179], v128, s[24:27], s89 offen sc1\n"
"v_mfma_f32_16x16x32_bf16 v[8:11], a[64:67], a[140:143], v[8:11]\n"
"ds_read_b128 a[120:123], v128 offset:63488\n"
"v_mfma_f32_16x16x32_bf16 v[12:15], a[64:67], a[144:147], v[12:15]\n"
"ds_read_b128 a[124:127], v128 offset:64512\n"
"buffer_load_dwordx4 a[180:183], v128, s[24:27], s89 offen offset:1024 sc1\n"
"v_mfma_f32_16x16x32_bf16 v[16:19], a[64:67], a[148:151], v[16:19]\n"
"ds_read_b128 a[128:131], v137\n"
"s_waitcnt vmcnt(18) lgkmcnt(5)\n"
"v_mfma_f32_16x16x32_bf16 v[0:3], a[68:71], a[92:95], v[0:3]\n"
"ds_read_b128 a[132:135], v137 offset:1024\n"
"v_mfma_f32_16x16x32_bf16 v[4:7], a[68:71], a[96:99], v[4:7]\n"
"ds_read_b128 a[136:139], v137 offset:2048\n"
"buffer_load_dwordx4 a[184:187], v128, s[24:27], s89 offen offset:2048 sc1\n"
"v_mfma_f32_16x16x32_bf16 v[8:11], a[68:71], a[100:103], v[8:11]\n"
"ds_read_b128 a[140:143], v137 offset:3072\n"
"v_mfma_f32_16x16x32_bf16 v[12:15], a[68:71], a[104:107], v[12:15]\n"
"ds_read_b128 a[144:147], v137 offset:4096\n"
"buffer_load_dwordx4 a[188:191], v128, s[24:27], s89 offen offset:3072 sc1\n"
"v_mfma_f32_16x16x32_bf16 v[16:19], a[68:71], a[108:111], v[16:19]\n"
"ds_read_b128 a[148:151], v137 offset:5120\n"
"s_waitcnt vmcnt(19) lgkmcnt(5)\n"
"v_mfma_f32_16x16x32_bf16 v[0:3], a[72:75], a[112:115], v[0:3]\n"
"ds_read_b128 a[92:95], v137 offset:6144\n"
"v_mfma_f32_16x16x32_bf16 v[4:7], a[72:75], a[116:119], v[4:7]\n"
"ds_read_b128 a[96:99], v137 offset:7168\n"
"v_mfma_f32_16x16x32_bf16 v[8:11], a[72:75], a[120:123], v[8:11]\n"
"ds_read_b128 a[100:103], v137 offset:8192\n"
"v_mfma_f32_16x16x32_bf16 v[12:15], a[72:75], a[124:127], v[12:15]\n"
"ds_read_b128 a[104:107], v137 offset:9216\n"
"v_mfma_f32_16x16x32_bf16 v[16:19], a[72:75], a[128:131], v[16:19]\n"
"ds_read_b128 a[108:111], v137 offset:10240\n"
"s_waitcnt vmcnt(18) lgkmcnt(5)\n"
"v_mfma_f32_16x16x32_bf16 v[0:3], a[76:79], a[132:135], v[0:3]\n"
"ds_read_b128 a[112:115], v175 offset:57344\n"
"v_mfma_f32_16x16x32_bf16 v[4:7], a[76:79], a[136:139], v[4:7]\n"
"ds_read_b128 a[116:119], v175 offset:59392\n"
"v_mfma_f32_16x16x32_bf16 v[8:11], a[76:79], a[140:143], v[8:11]\n"
"ds_read_b128 a[120:123], v175 offset:61440\n"
"v_mfma_f32_16x16x32_bf16 v[12:15], a[76:79], a[144:147], v[12:15]\n"
"ds_read_b128 a[124:127], v175 offset:63488\n"
"v_mfma_f32_16x16x32_bf16 v[16:19], a[76:79], a[148:151], v[16:19]\n"
"ds_read_b128 a[128:131], v176\n"
"s_waitcnt vmcnt(17) lgkmcnt(5)\n"
"v_mfma_f32_16x16x32_bf16 v[0:3], a[80:83], a[92:95], v[0:3]\n"
"ds_read_b128 a[132:135], v178 offset:57344\n"
"v_mfma_f32_16x16x32_bf16 v[4:7], a[80:83], a[96:99], v[4:7]\n"
"ds_read_b128 a[136:139], v178 offset:59392\n"
"v_mfma_f32_16x16x32_bf16 v[8:11], a[80:83], a[100:103], v[8:11]\n"
"ds_read_b128 a[140:143], v178 offset:61440\n"
"v_mfma_f32_16x16x32_bf16 v[12:15], a[80:83], a[104:107], v[12:15]\n"
"ds_read_b128 a[144:147], v178 offset:63488\n"
"v_mfma_f32_16x16x32_bf16 v[16:19], a[80:83], a[108:111], v[16:19]\n"
"ds_read_b128 a[148:151], v179\n"
"s_waitcnt vmcnt(16) lgkmcnt(5)\n"
"v_mfma_f32_16x16x32_bf16 v[0:3], a[84:87], a[112:115], v[0:3]\n"
"ds_read_b128 a[92:95], v176 offset:2048\n"
"v_mfma_f32_16x16x32_bf16 v[4:7], a[84:87], a[116:119], v[4:7]\n"
"ds_read_b128 a[96:99], v176 offset:4096\n"
"v_mfma_f32_16x16x32_bf16 v[8:11], a[84:87], a[120:123], v[8:11]\n"
"ds_read_b128 a[100:103], v176 offset:6144\n"
"v_mfma_f32_16x16x32_bf16 v[12:15], a[84:87], a[124:127], v[12:15]\n"
"ds_read_b128 a[104:107], v176 offset:8192\n"
"v_mfma_f32_16x16x32_bf16 v[16:19], a[84:87], a[128:131], v[16:19]\n"
"ds_read_b128 a[108:111], v176 offset:10240\n"
"s_waitcnt vmcnt(15) lgkmcnt(5)\n"
"v_mfma_f32_16x16x32_bf16 v[0:3], a[88:91], a[132:135], v[0:3]\n"
"ds_read_b128 a[112:115], v179 offset:2048\n"
"v_mfma_f32_16x16x32_bf16 v[4:7], a[88:91], a[136:139], v[4:7]\n"
"ds_read_b128 a[116:119], v179 offset:4096\n"
"v_mfma_f32_16x16x32_bf16 v[8:11], a[88:91], a[140:143], v[8:11]\n"
"ds_read_b128 a[120:123], v179 offset:6144\n"
"v_mfma_f32_16x16x32_bf16 v[12:15], a[88:91], a[144:147], v[12:15]\n"
"ds_read_b128 a[124:127], v179 offset:8192\n"
"v_mfma_f32_16x16x32_bf16 v[16:19], a[88:91], a[148:151], v[16:19]\n"
"ds_read_b128 a[128:131], v179 offset:10240\n"
"s_waitcnt vmcnt(10) lgkmcnt(5)\n"
"v_mfma_f32_16x16x32_bf16 v[0:3], a[152:155], a[92:95], v[0:3]\n"
"ds_read_b128 a[132:135], v176 offset:12288\n"
"buffer_load_dwordx4 v[84:87], v128, s[24:27], s69 offen sc1\n"
"v_mfma_f32_16x16x32_bf16 v[4:7], a[152:155], a[96:99], v[4:7]\n"
"ds_read_b128 a[136:139], v176 offset:14336\n"
"buffer_load_dwordx4 v[88:91], v128, s[24:27], s69 offen offset:1024 sc1\n"
"v_mfma_f32_16x16x32_bf16 v[8:11], a[152:155], a[100:103], v[8:11]\n"
"ds_read_b128 a[140:143], v176 offset:16384\n"
"buffer_load_dwordx4 v[92:95], v128, s[24:27], s69 offen offset:2048 sc1\n"
"v_mfma_f32_16x16x32_bf16 v[12:15], a[152:155], a[104:107], v[12:15]\n"
"ds_read_b128 a[144:147], v176 offset:18432\n"
"buffer_load_dwordx4 v[96:99], v128, s[24:27], s69 offen offset:3072 sc1\n"
"v_mfma_f32_16x16x32_bf16 v[16:19], a[152:155], a[108:111], v[16:19]\n"
"ds_read_b128 a[148:151], v176 offset:20480\n"
"buffer_load_dwordx4 v[100:103], v130, s[24:27], s69 offen sc1\n"
"s_waitcnt vmcnt(13) lgkmcnt(5)\n"
"v_mfma_f32_16x16x32_bf16 v[0:3], a[156:159], a[112:115], v[0:3]\n"
"ds_read_b128 a[92:95], v179 offset:12288\n"
"v_mfma_f32_16x16x32_bf16 v[4:7], a[156:159], a[116:119], v[4:7]\n"
"ds_read_b128 a[96:99], v179 offset:14336\n"
"v_mfma_f32_16x16x32_bf16 v[8:11], a[156:159], a[120:123], v[8:11]\n"
"ds_read_b128 a[100:103], v179 offset:16384\n"
"v_mfma_f32_16x16x32_bf16 v[12:15], a[156:159], a[124:127], v[12:15]\n"
"ds_read_b128 a[104:107], v179 offset:18432\n"
"v_mfma_f32_16x16x32_bf16 v[16:19], a[156:159], a[128:131], v[16:19]\n"
"ds_read_b128 a[108:111], v179 offset:20480\n"
"s_waitcnt vmcnt(12) lgkmcnt(5)\n"
"v_mfma_f32_16x16x32_bf16 v[0:3], a[160:163], a[132:135], v[0:3]\n"
"ds_read_b128 a[112:115], v176 offset:22528\n"
"v_mfma_f32_16x16x32_bf16 v[4:7], a[160:163], a[136:139], v[4:7]\n"
"ds_read_b128 a[116:119], v179 offset:22528\n"
"v_mfma_f32_16x16x32_bf16 v[8:11], a[160:163], a[140:143], v[8:11]\n"
"ds_read_b128 a[120:123], v175 offset:16384\n"
"v_mfma_f32_16x16x32_bf16 v[12:15], a[160:163], a[144:147], v[12:15]\n"
"ds_read_b128 a[124:127], v178 offset:16384\n"
"v_mfma_f32_16x16x32_bf16 v[16:19], a[160:163], a[148:151], v[16:19]\n"
"ds_read_b128 a[128:131], v175 offset:26624\n"
"s_waitcnt vmcnt(11) lgkmcnt(5)\n"
"v_mfma_f32_16x16x32_bf16 v[0:3], a[164:167], a[92:95], v[0:3]\n"
"ds_read_b128 a[132:135], v178 offset:26624\n"
"v_mfma_f32_16x16x32_bf16 v[4:7], a[164:167], a[96:99], v[4:7]\n"
"ds_read_b128 a[136:139], v176 offset:24576\n"
"v_mfma_f32_16x16x32_bf16 v[8:11], a[164:167], a[100:103], v[8:11]\n"
"ds_read_b128 a[140:143], v179 offset:24576\n"
"v_mfma_f32_16x16x32_bf16 v[12:15], a[164:167], a[104:107], v[12:15]\n"
"ds_read_b128 a[144:147], v175 offset:18432\n"
"v_mfma_f32_16x16x32_bf16 v[16:19], a[164:167], a[108:111], v[16:19]\n"
"ds_read_b128 a[148:151], v178 offset:18432\n"
"s_waitcnt vmcnt(10) lgkmcnt(5)\n"
"v_mfma_f32_16x16x32_bf16 v[0:3], a[168:171], a[112:115], v[0:3]\n"
"ds_read_b128 a[92:95], v175 offset:28672\n"
"s_waitcnt vmcnt(9)\n"
"v_mfma_f32_16x16x32_bf16 v[0:3], a[172:175], a[116:119], v[0:3]\n"
"ds_read_b128 a[96:99], v178 offset:28672\n"
"s_waitcnt vmcnt(8)\n"
"v_mfma_f32_16x16x32_bf16 v[0:3], a[176:179], a[120:123], v[0:3]\n"
"ds_read_b128 a[100:103], v176 offset:26624\n"
"s_waitcnt vmcnt(7)\n"
"v_mfma_f32_16x16x32_bf16 v[0:3], a[180:183], a[124:127], v[0:3]\n"
"ds_read_b128 a[104:107], v179 offset:26624\n"
"s_waitcnt vmcnt(6)\n"
"v_mfma_f32_16x16x32_bf16 v[0:3], a[184:187], a[128:131], v[0:3]\n"
"ds_read_b128 a[108:111], v175 offset:20480\n"
"s_waitcnt vmcnt(5) lgkmcnt(5)\n"
"v_mfma_f32_16x16x32_bf16 v[0:3], a[188:191], a[132:135], v[0:3]\n"
"ds_read_b128 a[112:115], v178 offset:20480\n"
"v_mfma_f32_16x16x32_bf16 v[4:7], a[168:171], a[136:139], v[4:7]\n"
"ds_read_b128 a[116:119], v175 offset:30720\n"
"v_mfma_f32_16x16x32_bf16 v[4:7], a[172:175], a[140:143], v[4:7]\n"
"ds_read_b128 a[120:123], v178 offset:30720\n"
"v_mfma_f32_16x16x32_bf16 v[4:7], a[176:179], a[144:147], v[4:7]\n"
"ds_read_b128 a[124:127], v176 offset:28672\n"
"v_mfma_f32_16x16x32_bf16 v[4:7], a[180:183], a[148:151], v[4:7]\n"
"ds_read_b128 a[128:131], v179 offset:28672\n"
"s_waitcnt lgkmcnt(5)\n"
"v_mfma_f32_16x16x32_bf16 v[4:7], a[184:187], a[92:95], v[4:7]\n"
"ds_read_b128 a[132:135], v175 offset:22528\n"
"v_mfma_f32_16x16x32_bf16 v[4:7], a[188:191], a[96:99], v[4:7]\n"
"ds_read_b128 a[136:139], v178 offset:22528\n"
"v_mfma_f32_16x16x32_bf16 v[8:11], a[168:171], a[100:103], v[8:11]\n"
"ds_read_b128 a[140:143], v175 offset:32768\n"
"v_mfma_f32_16x16x32_bf16 v[8:11], a[172:175], a[104:107], v[8:11]\n"
"ds_read_b128 a[144:147], v178 offset:32768\n"
"v_mfma_f32_16x16x32_bf16 v[8:11], a[176:179], a[108:111], v[8:11]\n"
"ds_read_b128 a[148:151], v176 offset:30720\n"
"s_waitcnt lgkmcnt(5)\n"
"v_mfma_f32_16x16x32_bf16 v[8:11], a[180:183], a[112:115], v[8:11]\n"
"ds_read_b128 a[92:95], v179 offset:30720\n"
"v_mfma_f32_16x16x32_bf16 v[8:11], a[184:187], a[116:119], v[8:11]\n"
"ds_read_b128 a[96:99], v175 offset:24576\n"
"v_mfma_f32_16x16x32_bf16 v[8:11], a[188:191], a[120:123], v[8:11]\n"
"ds_read_b128 a[100:103], v178 offset:24576\n"
"v_mfma_f32_16x16x32_bf16 v[12:15], a[168:171], a[124:127], v[12:15]\n"
"ds_read_b128 a[104:107], v175 offset:34816\n"
"v_mfma_f32_16x16x32_bf16 v[12:15], a[172:175], a[128:131], v[12:15]\n"
"ds_read_b128 a[108:111], v178 offset:34816\n"
"s_waitcnt lgkmcnt(5)\n"
"v_mfma_f32_16x16x32_bf16 v[12:15], a[176:179], a[132:135], v[12:15]\n"
"s_waitcnt vmcnt(4)\n"
"v_xor_b32 v244, s76, v85\n"
"v_bitop3_b32 v244, v87, s76, v244 bitop3:0xbe\n"
"s_waitcnt vmcnt(3)\n"
"v_bitop3_b32 v244, v89, s76, v244 bitop3:0xbe\n"
"v_bitop3_b32 v244, v91, s76, v244 bitop3:0xbe\n"
"s_waitcnt vmcnt(2)\n"
"v_bitop3_b32 v244, v93, s76, v244 bitop3:0xbe\n"
"v_bitop3_b32 v244, v95, s76, v244 bitop3:0xbe\n"
"s_waitcnt vmcnt(1)\n"
"v_bitop3_b32 v244, v97, s76, v244 bitop3:0xbe\n"
"v_bitop3_b32 v244, v99, s76, v244 bitop3:0xbe\n"
"s_waitcnt vmcnt(0)\n"
"v_bitop3_b32 v244, v101, s76, v244 bitop3:0xbe\n"
"v_bitop3_b32 v244, v103, s76, v244 bitop3:0xbe\n"
"v_cmp_ne_u32 vcc, 0, v244\n"
"s_nop 0\n"
"s_cbranch_vccnz L%=_stxS_slow\n"
"L%=_stxS_back:\n"
"v_mfma_f32_16x16x32_bf16 v[12:15], a[180:183], a[136:139], v[12:15]\n"
"v_lshlrev_b32 v244, 16, v84\n"
"v_and_b32 v245, 0xffff0000, v84\n"
"v_add_f32 v152, v152, v244\n"
"v_add_f32 v153, v153, v245\n"
"v_mfma_f32_16x16x32_bf16 v[12:15], a[184:187], a[140:143], v[12:15]\n"
"v_lshlrev_b32 v244, 16, v86\n"
"v_and_b32 v245, 0xffff0000, v86\n"
"v_add_f32 v154, v154, v244\n"
"v_add_f32 v155, v155, v245\n"
"v_mfma_f32_16x16x32_bf16 v[12:15], a[188:191], a[144:147], v[12:15]\n"
"v_lshlrev_b32 v244, 16, v88\n"
"v_and_b32 v245, 0xffff0000, v88\n"
"v_add_f32 v156, v156, v244\n"
"v_add_f32 v157, v157, v245\n"
"v_mfma_f32_16x16x32_bf16 v[16:19], a[168:171], a[148:151], v[16:19]\n"
"v_lshlrev_b32 v244, 16, v90\n"
"v_and_b32 v245, 0xffff0000, v90\n"
"v_add_f32 v158, v158, v244\n"
"v_add_f32 v159, v159, v245\n"
"s_waitcnt lgkmcnt(0)\n"
"v_mfma_f32_16x16x32_bf16 v[16:19], a[172:175], a[92:95], v[16:19]\n"
"v_lshlrev_b32 v244, 16, v92\n"
"v_and_b32 v245, 0xffff0000, v92\n"
"v_add_f32 v160, v160, v244\n"
"v_add_f32 v161, v161, v245\n"
"v_mfma_f32_16x16x32_bf16 v[16:19], a[176:179], a[96:99], v[16:19]\n"
"v_lshlrev_b32 v244, 16, v94\n"
"v_and_b32 v245, 0xffff0000, v94\n"
"v_add_f32 v162, v162, v244\n"
"v_add_f32 v163, v163, v245\n"
"v_mfma_f32_16x16x32_bf16 v[16:19], a[180:183], a[100:103], v[16:19]\n"
"v_lshlrev_b32 v244, 16, v96\n"
"v_and_b32 v245, 0xffff0000, v96\n"
"v_add_f32 v164, v164, v244\n"
"v_add_f32 v165, v165, v245\n"
"v_mfma_f32_16x16x32_bf16 v[16:19], a[184:187], a[104:107], v[16:19]\n"
"v_lshlrev_b32 v244, 16, v98\n"
"v_and_b32 v245, 0xffff0000, v98\n"
"v_add_f32 v166, v166, v244\n"
"v_add_f32 v167, v167, v245\n"
"v_mfma_f32_16x16x32_bf16 v[16:19], a[188:191], a[108:111], v[16:19]\n"
"v_lshlrev_b32 v244, 16, v100\n"
"v_and_b32 v245, 0xffff0000, v100\n"
"v_add_f32 v168, v168, v244\n"
"v_add_f32 v169, v169, v245\n"
"v_lshlrev_b32 v244, 16, v102\n"
"v_and_b32 v245, 0xffff0000, v102\n"
"v_add_f32 v170, v170, v244\n"
"v_add_f32 v171, v171, v245\n"
"v_add_f32 v152, v152, v0\n"
"v_add_f32 v153, v153, v1\n"
"v_add_f32 v154, v154, v2\n"
"v_add_f32 v155, v155, v3\n"
"buffer_store_dword v152, v227, s[40:43], 0 offen\n"
"buffer_store_dword v153, v227, s[40:43], 0 offen offset:2560\n"
"buffer_store_dword v154, v228, s[40:43], 0 offen\n"
"buffer_store_dword v155, v228, s[40:43], 0 offen offset:2560\n"
"v_add_f32 v156, v156, v4\n"
"v_add_f32 v157, v157, v5\n"
"v_add_f32 v158, v158, v6\n"
"v_add_f32 v159, v159, v7\n"
"buffer_store_dword v156, v227, s[40:43], 0 offen offset:64\n"
"buffer_store_dword v157, v227, s[40:43], 0 offen offset:2624\n"
"buffer_store_dword v158, v228, s[40:43], 0 offen offset:64\n"
"buffer_store_dword v159, v228, s[40:43], 0 offen offset:2624\n"
"v_add_f32 v160, v160, v8\n"
"v_add_f32 v161, v161, v9\n"
"v_add_f32 v162, v162, v10\n"
"v_add_f32 v163, v163, v11\n"
"buffer_store_dword v160, v227, s[40:43], 0 offen offset:128\n"
"buffer_store_dword v161, v227, s[40:43], 0 offen offset:2688\n"
"buffer_store_dword v162, v228, s[40:43], 0 offen offset:128\n"
"buffer_store_dword v163, v228, s[40:43], 0 offen offset:2688\n"
"v_add_f32 v164, v164, v12\n"
"v_add_f32 v165, v165, v13\n"
"v_add_f32 v166, v166, v14\n"
"v_add_f32 v167, v167, v15\n"
"buffer_store_dword v164, v227, s[40:43], 0 offen offset:192\n"
"buffer_store_dword v165, v227, s[40:43], 0 offen offset:2752\n"
"buffer_store_dword v166, v228, s[40:43], 0 offen offset:192\n"
"buffer_store_dword v167, v228, s[40:43], 0 offen offset:2752\n"
"v_add_f32 v168, v168, v16\n"
"v_add_f32 v169, v169, v17\n"
"v_add_f32 v170, v170, v18\n"
"v_add_f32 v171, v171, v19\n"
"buffer_store_dword v168, v227, s[40:43], 0 offen offset:256\n"
"buffer_store_dword v169, v227, s[40:43], 0 offen offset:2816\n"
"buffer_store_dword v170, v228, s[40:43], 0 offen offset:256\n"
"buffer_store_dword v171, v228, s[40:43], 0 offen offset:2816\n"
"s_endpgm\n"
"L%=_stM_slow:\n"
"s_nop 7\n"
"s_mov_b32 s91, 0x9c40\n"
"L%=_stM_loop:\n"
"s_sleep 2\n"
"buffer_load_dwordx4 v[108:111], v247, s[24:27], 0 offen sc1\n"
"s_lshl_b32 s81, s95, 10\n"
"buffer_load_dwordx4 v[112:115], v248, s[24:27], s81 offen sc1\n"
"s_lshl_b32 s81, s93, 9\n"
"buffer_load_dwordx4 v[116:119], v249, s[24:27], s81 offen sc1\n"
"s_lshl_b32 s81, s96, 10\n"
"buffer_load_dwordx4 v[120:123], v248, s[24:27], s81 offen sc1\n"
"s_waitcnt vmcnt(0)\n"
"v_xor_b32 v240, s76, v109\n"
"v_bitop3_b32 v240, v111, s78, v240 bitop3:0xbe\n"
"v_bitop3_b32 v240, v113, s76, v240 bitop3:0xbe\n"
"v_bitop3_b32 v240, v115, s76, v240 bitop3:0xbe\n"
"v_bitop3_b32 v240, v117, s76, v240 bitop3:0xbe\n"
"v_bitop3_b32 v240, v119, s76, v240 bitop3:0xbe\n"
"v_bitop3_b32 v240, v121, s76, v240 bitop3:0xbe\n"
"v_bitop3_b32 v240, v123, s76, v240 bitop3:0xbe\n"
"v_cmp_ne_u32 vcc, 0, v240\n"
"s_nop 0\n"
"s_cbranch_vccnz L%=_stM_retry\n"
"s_nop 4\n"
"s_branch L%=_stM_back\n"
"L%=_stM_retry:\n"
"s_sub_u32 s91, s91, 1\n"
"s_cmp_lg_u32 s91, 0\n"
"s_cbranch_scc1 L%=_stM_loop\n"
"s_nop 4\n"
"s_branch L%=_stM_back\n"
"L%=_f0M_slow:\n"
"s_nop 7\n"
"s_mov_b32 s91, 0x9c40\n"
"L%=_f0M_loop:\n"
"s_sleep 2\n"
"s_mov_b32 s87, 0x1000\n"
"buffer_load_dwordx4 v[112:115], v128, s[24:27], s87 offen sc1\n"
"buffer_load_dwordx4 v[116:119], v128, s[24:27], s87 offen offset:1024 sc1\n"
"s_waitcnt vmcnt(0)\n"
"v_cmp_ne_u64 s[80:81], s[76:77], v[112:113]\n"
"v_cmp_ne_u64 s[82:83], s[76:77], v[114:115]\n"
"v_cmp_ne_u64 s[84:85], s[76:77], v[116:117]\n"
"v_cmp_ne_u64 vcc, s[76:77], v[118:119]\n"
"s_or_b64 s[80:81], s[80:81], s[82:83]\n"
"s_or_b64 s[84:85], s[84:85], vcc\n"
"s_and_b32 s84, s84, 0xffffff\n"
"s_mov_b32 s85, 0\n"
"s_or_b64 s[80:81], s[80:81], s[84:85]\n"
"s_cbranch_scc1 L%=_f0M_retry\n"
"s_nop 4\n"
"s_branch L%=_f0M_back\n"
"L%=_f0M_retry:\n"
"s_sub_u32 s91, s91, 1\n"
"s_cmp_lg_u32 s91, 0\n"
"s_cbranch_scc1 L%=_f0M_loop\n"
"s_nop 4\n"
"s_branch L%=_f0M_back\n"
"L%=_tM_slow:\n"
"s_nop 7\n"
"s_mov_b32 s91, 0x9c40\n"
"L%=_tM_loop:\n"
"s_sleep 2\n"
"buffer_load_dwordx4 v[96:99], v217, s[24:27], s68 offen sc1\n"
"buffer_load_dwordx4 v[100:103], v218, s[24:27], s68 offen sc1\n"
"s_waitcnt vmcnt(0)\n"
"v_xor_b32 v104, v97, v219\n"
"v_bitop3_b32 v104, v99, v220, v104 bitop3:0xbe\n"
"v_bitop3_b32 v104, v101, v221, v104 bitop3:0xbe\n"
"v_bitop3_b32 v104, v103, v222, v104 bitop3:0xbe\n"
"v_cmp_ne_u32 vcc, 0, v104\n"
"s_nop 0\n"
"s_cbranch_vccnz L%=_tM_retry\n"
"s_nop 4\n"
"s_branch L%=_tM_back\n"
"L%=_tM_retry:\n"
"s_sub_u32 s91, s91, 1\n"
"s_cmp_lg_u32 s91, 0\n"
"s_cbranch_scc1 L%=_tM_loop\n"
"s_nop 4\n"
"s_branch L%=_tM_back\n"
"L%=_f2aM_slow:\n"
"s_nop 7\n"
"s_mov_b32 s91, 0x9c40\n"
"L%=_f2aM_loop:\n"
"s_sleep 2\n"
"buffer_load_dwordx2 v[122:123], v120, s[24:27], 0 offen sc1\n"
"s_waitcnt vmcnt(0)\n"
"s_add_u32 s80, s76, 1\n"
"v_subrev_u32 v121, s80, v122\n"
"v_cmp_lt_u32 vcc, 2, v121\n"
"v_cmp_ne_u32 s[82:83], s77, v123\n"
"s_nop 0\n"
"s_or_b32 s82, s82, vcc_lo\n"
"s_and_b32 s82, s82, 0x7ff\n"
"s_cbranch_scc1 L%=_f2aM_retry\n"
"s_nop 4\n"
"s_branch L%=_f2aM_back\n"
"L%=_f2aM_retry:\n"
"s_sub_u32 s91, s91, 1\n"
"s_cmp_lg_u32 s91, 0\n"
"s_cbranch_scc1 L%=_f2aM_loop\n"
"s_nop 4\n"
"s_branch L%=_f2aM_back\n"
"L%=_f0S_slow:\n"
"s_nop 7\n"
"s_mov_b32 s91, 0x9c40\n"
"L%=_f0S_loop:\n"
"s_sleep 2\n"
"s_mov_b32 s87, 0x1000\n"
"buffer_load_dwordx4 v[244:247], v128, s[24:27], s87 offen sc1\n"
"buffer_load_dwordx4 v[8:11], v128, s[24:27], s87 offen offset:1024 sc1\n"
"s_waitcnt vmcnt(0)\n"
"v_cmp_ne_u64 s[80:81], s[76:77], v[244:245]\n"
"v_cmp_ne_u64 s[82:83], s[76:77], v[246:247]\n"
"v_cmp_ne_u64 s[84:85], s[76:77], v[8:9]\n"
"v_cmp_ne_u64 vcc, s[76:77], v[10:11]\n"
"s_or_b64 s[80:81], s[80:81], s[82:83]\n"
"s_or_b64 s[84:85], s[84:85], vcc\n"
"s_and_b32 s84, s84, 0xffffff\n"
"s_mov_b32 s85, 0\n"
"s_or_b64 s[80:81], s[80:81], s[84:85]\n"
"s_cbranch_scc1 L%=_f0S_retry\n"
"s_nop 4\n"
"s_branch L%=_f0S_back\n"
"L%=_f0S_retry:\n"
"s_sub_u32 s91, s91, 1\n"
"s_cmp_lg_u32 s91, 0\n"
"s_cbranch_scc1 L%=_f0S_loop\n"
"s_nop 4\n"
"s_branch L%=_f0S_back\n"
"L%=_f2aS_slow:\n"
"s_nop 7\n"
"s_mov_b32 s91, 0x9c40\n"
"L%=_f2aS_loop:\n"
"s_sleep 2\n"
"buffer_load_dwordx2 v[246:247], v244, s[24:27], 0 offen sc1\n"
"s_waitcnt vmcnt(0)\n"
"s_add_u32 s80, s76, 1\n"
"v_subrev_u32 v245, s80, v246\n"
"v_cmp_lt_u32 vcc, 2, v245\n"
"v_cmp_ne_u32 s[82:83], s77, v247\n"
"s_nop 0\n"
"s_or_b32 s82, s82, vcc_lo\n"
"s_and_b32 s82, s82, 0x7ff\n"
"s_cbranch_scc1 L%=_f2aS_retry\n"
"s_nop 4\n"
"s_branch L%=_f2aS_back\n"
"L%=_f2aS_retry:\n"
"s_sub_u32 s91, s91, 1\n"
"s_cmp_lg_u32 s91, 0\n"
"s_cbranch_scc1 L%=_f2aS_loop\n"
"s_nop 4\n"
"s_branch L%=_f2aS_back\n"
"L%=_stS_slow:\n"
"s_nop 7\n"
"s_mov_b32 s91, 0x9c40\n"
"L%=_stS_loop:\n"
"s_sleep 2\n"
"buffer_load_dwordx4 v[104:107], v247, s[24:27], 0 offen sc1\n"
"s_lshl_b32 s81, s93, 9\n"
"buffer_load_dwordx4 v[108:111], v249, s[24:27], s81 offen sc1\n"
"s_lshl_b32 s81, s94, 9\n"
"buffer_load_dwordx4 v[112:115], v249, s[24:27], s81 offen sc1\n"
"s_lshl_b32 s81, s96, 10\n"
"buffer_load_dwordx4 v[116:119], v248, s[24:27], s81 offen sc1\n"
"s_waitcnt vmcnt(0)\n"
"v_xor_b32 v240, s76, v105\n"
"v_bitop3_b32 v240, v107, s78, v240 bitop3:0xbe\n"
"v_bitop3_b32 v240, v109, s76, v240 bitop3:0xbe\n"
"v_bitop3_b32 v240, v111, s76, v240 bitop3:0xbe\n"
"v_bitop3_b32 v240, v113, s76, v240 bitop3:0xbe\n"
"v_bitop3_b32 v240, v115, s76, v240 bitop3:0xbe\n"
"v_bitop3_b32 v240, v117, s76, v240 bitop3:0xbe\n"
"v_bitop3_b32 v240, v119, s76, v240 bitop3:0xbe\n"
"v_cmp_ne_u32 vcc, 0, v240\n"
"s_nop 0\n"
"s_cbranch_vccnz L%=_stS_retry\n"
"s_nop 4\n"
"s_branch L%=_stS_back\n"
"L%=_stS_retry:\n"
"s_sub_u32 s91, s91, 1\n"
"s_cmp_lg_u32 s91, 0\n"
"s_cbranch_scc1 L%=_stS_loop\n"
"s_nop 4\n"
"s_branch L%=_stS_back\n"
"L%=_tS0_slow:\n"
"s_nop 7\n"
"s_mov_b32 s91, 0x9c40\n"
"L%=_tS0_loop:\n"
"s_sleep 2\n"
"buffer_load_dwordx4 v[16:19], v217, s[24:27], s68 offen sc1\n"
"buffer_load_dwordx4 v[20:23], v218, s[24:27], s68 offen sc1\n"
"s_waitcnt vmcnt(0)\n"
"v_xor_b32 v24, v17, v219\n"
"v_bitop3_b32 v24, v19, v220, v24 bitop3:0xbe\n"
"v_bitop3_b32 v24, v21, v221, v24 bitop3:0xbe\n"
"v_bitop3_b32 v24, v23, v222, v24 bitop3:0xbe\n"
"v_cmp_ne_u32 vcc, 0, v24\n"
"s_nop 0\n"
"s_cbranch_vccnz L%=_tS0_retry\n"
"s_nop 4\n"
"s_branch L%=_tS0_back\n"
"L%=_tS0_retry:\n"
"s_sub_u32 s91, s91, 1\n"
"s_cmp_lg_u32 s91, 0\n"
"s_cbranch_scc1 L%=_tS0_loop\n"
"s_nop 4\n"
"s_branch L%=_tS0_back\n"
"L%=_f2cM_slow:\n"
"s_nop 7\n"
"s_mov_b32 s91, 0x9c40\n"
"L%=_f2cM_loop:\n"
"s_sleep 2\n"
"buffer_load_dwordx2 v[142:143], v140, s[24:27], 0 offen sc1\n"
"s_waitcnt vmcnt(0)\n"
"s_add_u32 s80, s76, 1\n"
"v_subrev_u32 v141, s80, v142\n"
"v_cmp_lt_u32 vcc, 2, v141\n"
"v_cmp_ne_u32 s[82:83], s77, v143\n"
"s_nop 0\n"
"s_or_b32 s82, s82, vcc_lo\n"
"s_and_b32 s82, s82, 0xf80000ff\n"
"s_cbranch_scc1 L%=_f2cM_retry\n"
"s_nop 4\n"
"s_branch L%=_f2cM_back\n"
"L%=_f2cM_retry:\n"
"s_sub_u32 s91, s91, 1\n"
"s_cmp_lg_u32 s91, 0\n"
"s_cbranch_scc1 L%=_f2cM_loop\n"
"s_nop 4\n"
"s_branch L%=_f2cM_back\n"
"L%=_f2eM_slow:\n"
"s_nop 7\n"
"s_mov_b32 s91, 0x9c40\n"
"L%=_f2eM_loop:\n"
"s_sleep 2\n"
"buffer_load_dwordx2 v[142:143], v140, s[24:27], 0 offen sc1\n"
"s_waitcnt vmcnt(0)\n"
"s_add_u32 s80, s76, 3\n"
"v_subrev_u32 v141, s80, v142\n"
"v_cmp_lt_u32 vcc, 0, v141\n"
"v_cmp_ne_u32 s[82:83], s77, v143\n"
"s_nop 0\n"
"s_or_b32 s82, s82, vcc_lo\n"
"s_and_b32 s82, s82, 0x7800\n"
"s_cbranch_scc1 L%=_f2eM_retry\n"
"s_nop 4\n"
"s_branch L%=_f2eM_back\n"
"L%=_f2eM_retry:\n"
"s_sub_u32 s91, s91, 1\n"
"s_cmp_lg_u32 s91, 0\n"
"s_cbranch_scc1 L%=_f2eM_loop\n"
"s_nop 4\n"
"s_branch L%=_f2eM_back\n"
"L%=_f2dM_slow:\n"
"s_nop 7\n"
"s_mov_b32 s91, 0x9c40\n"
"L%=_f2dM_loop:\n"
"s_sleep 2\n"
"buffer_load_dwordx2 v[142:143], v140, s[24:27], 0 offen sc1\n"
"s_waitcnt vmcnt(0)\n"
"s_add_u32 s80, s76, 2\n"
"v_subrev_u32 v141, s80, v142\n"
"v_cmp_lt_u32 vcc, 1, v141\n"
"v_cmp_ne_u32 s[82:83], s77, v143\n"
"s_nop 0\n"
"s_or_b32 s82, s82, vcc_lo\n"
"s_and_b32 s82, s82, 0xffff07ff\n"
"s_cbranch_scc1 L%=_f2dM_retry\n"
"s_nop 4\n"
"s_branch L%=_f2dM_back\n"
"L%=_f2dM_retry:\n"
"s_sub_u32 s91, s91, 1\n"
"s_cmp_lg_u32 s91, 0\n"
"s_cbranch_scc1 L%=_f2dM_loop\n"
"s_nop 4\n"
"s_branch L%=_f2dM_back\n"
"L%=_f0PM_slow:\n"
"s_nop 7\n"
"s_mov_b32 s91, 0x9c40\n"
"L%=_f0PM_loop:\n"
"s_sleep 2\n"
"s_mov_b32 s87, 0x1000\n"
"buffer_load_dwordx4 v[124:127], v128, s[24:27], s87 offen sc1\n"
"buffer_load_dwordx4 v[236:239], v128, s[24:27], s87 offen offset:1024 sc1\n"
"s_waitcnt vmcnt(0)\n"
"v_cmp_ne_u64 s[80:81], s[76:77], v[124:125]\n"
"v_cmp_ne_u64 s[82:83], s[76:77], v[126:127]\n"
"v_cmp_ne_u64 s[84:85], s[76:77], v[236:237]\n"
"v_cmp_ne_u64 vcc, s[76:77], v[238:239]\n"
"s_or_b64 s[80:81], s[80:81], s[82:83]\n"
"s_or_b64 s[84:85], s[84:85], vcc\n"
"s_or_b64 s[80:81], s[80:81], s[84:85]\n"
"s_or_b32 s80, s80, s81\n"
"s_cmp_eq_u32 s3, 0\n"
"s_cselect_b32 s81, -1, 0\n"
"s_and_b32 s80, s80, s81\n"
"s_cbranch_scc1 L%=_f0PM_retry\n"
"s_nop 4\n"
"s_branch L%=_f0PM_back\n"
"L%=_f0PM_retry:\n"
"s_sub_u32 s91, s91, 1\n"
"s_cmp_lg_u32 s91, 0\n"
"s_cbranch_scc1 L%=_f0PM_loop\n"
"s_nop 4\n"
"s_branch L%=_f0PM_back\n"
"L%=_stxM_slow:\n"
"s_nop 7\n"
"s_mov_b32 s91, 0x9c40\n"
"L%=_stxM_loop:\n"
"s_sleep 2\n"
"buffer_load_dwordx4 v[20:23], v128, s[24:27], s69 offen sc1\n"
"buffer_load_dwordx4 v[24:27], v128, s[24:27], s69 offen offset:1024 sc1\n"
"buffer_load_dwordx4 v[28:31], v128, s[24:27], s69 offen offset:2048 sc1\n"
"buffer_load_dwordx4 v[32:35], v128, s[24:27], s69 offen offset:3072 sc1\n"
"buffer_load_dwordx4 v[36:39], v130, s[24:27], s69 offen sc1\n"
"s_waitcnt vmcnt(0)\n"
"v_xor_b32 v244, s76, v21\n"
"v_bitop3_b32 v244, v23, s76, v244 bitop3:0xbe\n"
"v_bitop3_b32 v244, v25, s76, v244 bitop3:0xbe\n"
"v_bitop3_b32 v244, v27, s76, v244 bitop3:0xbe\n"
"v_bitop3_b32 v244, v29, s76, v244 bitop3:0xbe\n"
"v_bitop3_b32 v244, v31, s76, v244 bitop3:0xbe\n"
"v_bitop3_b32 v244, v33, s76, v244 bitop3:0xbe\n"
"v_bitop3_b32 v244, v35, s76, v244 bitop3:0xbe\n"
"v_bitop3_b32 v244, v37, s76, v244 bitop3:0xbe\n"
"v_bitop3_b32 v244, v39, s76, v244 bitop3:0xbe\n"
"v_cmp_ne_u32 vcc, 0, v244\n"
"s_nop 0\n"
"s_cbranch_vccnz L%=_stxM_retry\n"
"s_nop 4\n"
"s_branch L%=_stxM_back\n"
"L%=_stxM_retry:\n"
"s_sub_u32 s91, s91, 1\n"
"s_cmp_lg_u32 s91, 0\n"
"s_cbranch_scc1 L%=_stxM_loop\n"
"s_nop 4\n"
"s_branch L%=_stxM_back\n"
"L%=_f2cS_slow:\n"
"s_nop 7\n"
"s_mov_b32 s91, 0x9c40\n"
"L%=_f2cS_loop:\n"
"s_sleep 2\n"
"buffer_load_dwordx2 v[142:143], v140, s[24:27], 0 offen sc1\n"
"s_waitcnt vmcnt(0)\n"
"s_add_u32 s80, s76, 1\n"
"v_subrev_u32 v141, s80, v142\n"
"v_cmp_lt_u32 vcc, 2, v141\n"
"v_cmp_ne_u32 s[82:83], s77, v143\n"
"s_nop 0\n"
"s_or_b32 s82, s82, vcc_lo\n"
"s_and_b32 s82, s82, 0xf80000ff\n"
"s_cbranch_scc1 L%=_f2cS_retry\n"
"s_nop 4\n"
"s_branch L%=_f2cS_back\n"
"L%=_f2cS_retry:\n"
"s_sub_u32 s91, s91, 1\n"
"s_cmp_lg_u32 s91, 0\n"
"s_cbranch_scc1 L%=_f2cS_loop\n"
"s_nop 4\n"
"s_branch L%=_f2cS_back\n"
"L%=_f2eS_slow:\n"
"s_nop 7\n"
"s_mov_b32 s91, 0x9c40\n"
"L%=_f2eS_loop:\n"
"s_sleep 2\n"
"buffer_load_dwordx2 v[142:143], v140, s[24:27], 0 offen sc1\n"
"s_waitcnt vmcnt(0)\n"
"s_add_u32 s80, s76, 3\n"
"v_subrev_u32 v141, s80, v142\n"
"v_cmp_lt_u32 vcc, 0, v141\n"
"v_cmp_ne_u32 s[82:83], s77, v143\n"
"s_nop 0\n"
"s_or_b32 s82, s82, vcc_lo\n"
"s_and_b32 s82, s82, 0x7800\n"
"s_cbranch_scc1 L%=_f2eS_retry\n"
"s_nop 4\n"
"s_branch L%=_f2eS_back\n"
"L%=_f2eS_retry:\n"
"s_sub_u32 s91, s91, 1\n"
"s_cmp_lg_u32 s91, 0\n"
"s_cbranch_scc1 L%=_f2eS_loop\n"
"s_nop 4\n"
"s_branch L%=_f2eS_back\n"
"L%=_f2dS_slow:\n"
"s_nop 7\n"
"s_mov_b32 s91, 0x9c40\n"
"L%=_f2dS_loop:\n"
"s_sleep 2\n"
"buffer_load_dwordx2 v[142:143], v140, s[24:27], 0 offen sc1\n"
"s_waitcnt vmcnt(0)\n"
"s_add_u32 s80, s76, 2\n"
"v_subrev_u32 v141, s80, v142\n"
"v_cmp_lt_u32 vcc, 1, v141\n"
"v_cmp_ne_u32 s[82:83], s77, v143\n"
"s_nop 0\n"
"s_or_b32 s82, s82, vcc_lo\n"
"s_and_b32 s82, s82, 0xffff07ff\n"
"s_cbranch_scc1 L%=_f2dS_retry\n"
"s_nop 4\n"
"s_branch L%=_f2dS_back\n"
"L%=_f2dS_retry:\n"
"s_sub_u32 s91, s91, 1\n"
"s_cmp_lg_u32 s91, 0\n"
"s_cbranch_scc1 L%=_f2dS_loop\n"
"s_nop 4\n"
"s_branch L%=_f2dS_back\n"
"L%=_f0PS_slow:\n"
"s_nop 7\n"
"s_mov_b32 s91, 0x9c40\n"
"L%=_f0PS_loop:\n"
"s_sleep 2\n"
"s_mov_b32 s87, 0x1000\n"
"buffer_load_dwordx4 v[56:59], v128, s[24:27], s87 offen sc1\n"
"buffer_load_dwordx4 v[60:63], v128, s[24:27], s87 offen offset:1024 sc1\n"
"s_waitcnt vmcnt(0)\n"
"v_cmp_ne_u64 s[80:81], s[76:77], v[56:57]\n"
"v_cmp_ne_u64 s[82:83], s[76:77], v[58:59]\n"
"v_cmp_ne_u64 s[84:85], s[76:77], v[60:61]\n"
"v_cmp_ne_u64 vcc, s[76:77], v[62:63]\n"
"s_or_b64 s[80:81], s[80:81], s[82:83]\n"
"s_or_b64 s[84:85], s[84:85], vcc\n"
"s_or_b64 s[80:81], s[80:81], s[84:85]\n"
"s_or_b32 s80, s80, s81\n"
"s_cmp_eq_u32 s3, 0\n"
"s_cselect_b32 s81, -1, 0\n"
"s_and_b32 s80, s80, s81\n"
"s_cbranch_scc1 L%=_f0PS_retry\n"
"s_nop 4\n"
"s_branch L%=_f0PS_back\n"
"L%=_f0PS_retry:\n"
"s_sub_u32 s91, s91, 1\n"
"s_cmp_lg_u32 s91, 0\n"
"s_cbranch_scc1 L%=_f0PS_loop\n"
"s_nop 4\n"
"s_branch L%=_f0PS_back\n"
"L%=_tS1_slow:\n"
"s_nop 7\n"
"s_mov_b32 s91, 0x9c40\n"
"L%=_tS1_loop:\n"
"s_sleep 2\n"
"buffer_load_dwordx4 v[40:43], v217, s[60:63], s69 offen sc1\n"
"buffer_load_dwordx4 v[44:47], v218, s[60:63], s69 offen sc1\n"
"s_waitcnt vmcnt(0)\n"
"v_xor_b32 v48, v41, v223\n"
"v_bitop3_b32 v48, v43, v224, v48 bitop3:0xbe\n"
"v_bitop3_b32 v48, v45, v225, v48 bitop3:0xbe\n"
"v_bitop3_b32 v48, v47, v226, v48 bitop3:0xbe\n"
"v_cmp_ne_u32 vcc, 0, v48\n"
"s_nop 0\n"
"s_cbranch_vccnz L%=_tS1_retry\n"
"s_nop 4\n"
"s_branch L%=_tS1_back\n"
"L%=_tS1_retry:\n"
"s_sub_u32 s91, s91, 1\n"
"s_cmp_lg_u32 s91, 0\n"
"s_cbranch_scc1 L%=_tS1_loop\n"
"s_nop 4\n"
"s_branch L%=_tS1_back\n"
"L%=_stxS_slow:\n"
"s_nop 7\n"
"s_mov_b32 s91, 0x9c40\n"
"L%=_stxS_loop:\n"
"s_sleep 2\n"
"buffer_load_dwordx4 v[84:87], v128, s[24:27], s69 offen sc1\n"
"buffer_load_dwordx4 v[88:91], v128, s[24:27], s69 offen offset:1024 sc1\n"
"buffer_load_dwordx4 v[92:95], v128, s[24:27], s69 offen offset:2048 sc1\n"
"buffer_load_dwordx4 v[96:99], v128, s[24:27], s69 offen offset:3072 sc1\n"
"buffer_load_dwordx4 v[100:103], v130, s[24:27], s69 offen sc1\n"
"s_waitcnt vmcnt(0)\n"
"v_xor_b32 v244, s76, v85\n"
"v_bitop3_b32 v244, v87, s76, v244 bitop3:0xbe\n"
"v_bitop3_b32 v244, v89, s76, v244 bitop3:0xbe\n"
"v_bitop3_b32 v244, v91, s76, v244 bitop3:0xbe\n"
"v_bitop3_b32 v244, v93, s76, v244 bitop3:0xbe\n"
"v_bitop3_b32 v244, v95, s76, v244 bitop3:0xbe\n"
"v_bitop3_b32 v244, v97, s76, v244 bitop3:0xbe\n"
"v_bitop3_b32 v244, v99, s76, v244 bitop3:0xbe\n"
"v_bitop3_b32 v244, v101, s76, v244 bitop3:0xbe\n"
"v_bitop3_b32 v244, v103, s76, v244 bitop3:0xbe\n"
"v_cmp_ne_u32 vcc, 0, v244\n"
"s_nop 0\n"
"s_cbranch_vccnz L%=_stxS_retry\n"
"s_nop 4\n"
"s_branch L%=_stxS_back\n"
"L%=_stxS_retry:\n"
"s_sub_u32 s91, s91, 1\n"
"s_cmp_lg_u32 s91, 0\n"
"s_cbranch_scc1 L%=_stxS_loop\n"
"s_nop 4\n"
"s_branch L%=_stxS_back\n"
"L%=_end:\n"
        :
        : "s"(__builtin_amdgcn_kernarg_segment_ptr()), "s"(blockIdx.x), "v"(threadIdx.x), "s"((uint32_t)(size_t)lds)
        : "v1", "v2", "v3", "v4", "v5", "v6", "v7", "v8", "v9", "v10", "v11", "v12", "v13", "v14", "v15", "v16", "v17", "v18", "v19", "v20", "v21", "v22", "v23", "v24", "v25", "v26", "v27", "v28", "v29", "v30", "v31", "v32", "v33", "v34", "v35", "v36", "v37", "v38", "v39", "v40", "v41", "v42", "v43", "v44", "v45", "v46", "v47", "v48", "v49", "v50", "v51", "v52", "v53", "v54", "v55", "v56", "v57", "v58", "v59", "v60", "v61", "v62", "v63", "v64", "v65", "v66", "v67", "v68", "v69", "v70", "v71", "v72", "v73", "v74", "v75", "v76", "v77", "v78", "v79", "v80", "v81", "v82", "v83", "v84", "v85", "v86", "v87", "v88", "v89", "v90", "v91", "v92", "v93", "v94", "v95", "v96", "v97", "v98", "v99", "v100", "v101", "v102", "v103", "v104", "v105", "v106", "v107", "v108", "v109", "v110", "v111", "v112", "v113", "v114", "v115", "v116", "v117", "v118", "v119", "v120", "v121", "v122", "v123", "v124", "v125", "v126", "v127", "v128", "v129", "v130", "v131", "v132", "v133", "v134", "v135", "v136", "v137", "v138", "v139", "v140", "v141", "v142", "v143", "v144", "v145", "v146", "v147", "v148", "v149", "v150", "v151", "v152", "v153", "v154", "v155", "v156", "v157", "v158", "v159", "v160", "v161", "v162", "v163", "v164", "v165", "v166", "v167", "v168", "v169", "v170", "v171", "v172", "v173", "v174", "v175", "v176", "v177", "v178", "v179", "v180", "v181", "v182", "v183", "v184", "v185", "v186", "v187", "v188", "v189", "v190", "v191", "v192", "v193", "v194", "v195", "v196", "v197", "v198", "v199", "v200", "v201", "v202", "v203", "v204", "v205", "v206", "v207", "v208", "v209", "v210", "v211", "v212", "v213", "v214", "v215", "v216", "v217", "v218", "v219", "v220", "v221", "v222", "v223", "v224", "v225", "v226", "v227", "v228", "v229", "v230", "v231", "v232", "v233", "v234", "v235", "v236", "v237", "v238", "v239", "v240", "v241", "v242", "v243", "v244", "v245", "v246", "v247", "v248", "v249", "v250", "v251", "v252", "v253", "v254", "v255", "a0", "a1", "a2", "a3", "a4", "a5", "a6", "a7", "a8", "a9", "a10", "a11", "a12", "a13", "a14", "a15", "a16", "a17", "a18", "a19", "a20", "a21", "a22", "a23", "a24", "a25", "a26", "a27", "a28", "a29", "a30", "a31", "a32", "a33", "a34", "a35", "a36", "a37", "a38", "a39", "a40", "a41", "a42", "a43", "a44", "a45", "a46", "a47", "a48", "a49", "a50", "a51", "a52", "a53", "a54", "a55", "a56", "a57", "a58", "a59", "a60", "a61", "a62", "a63", "a64", "a65", "a66", "a67", "a68", "a69", "a70", "a71", "a72", "a73", "a74", "a75", "a76", "a77", "a78", "a79", "a80", "a81", "a82", "a83", "a84", "a85", "a86", "a87", "a88", "a89", "a90", "a91", "a92", "a93", "a94", "a95", "a96", "a97", "a98", "a99", "a100", "a101", "a102", "a103", "a104", "a105", "a106", "a107", "a108", "a109", "a110", "a111", "a112", "a113", "a114", "a115", "a116", "a117", "a118", "a119", "a120", "a121", "a122", "a123", "a124", "a125", "a126", "a127", "a128", "a129", "a130", "a131", "a132", "a133", "a134", "a135", "a136", "a137", "a138", "a139", "a140", "a141", "a142", "a143", "a144", "a145", "a146", "a147", "a148", "a149", "a150", "a151", "a152", "a153", "a154", "a155", "a156", "a157", "a158", "a159", "a160", "a161", "a162", "a163", "a164", "a165", "a166", "a167", "a168", "a169", "a170", "a171", "a172", "a173", "a174", "a175", "a176", "a177", "a178", "a179", "a180", "a181", "a182", "a183", "a184", "a185", "a186", "a187", "a188", "a189", "a190", "a191", "a192", "a193", "a194", "a195", "a196", "a197", "a198", "a199", "a200", "a201", "a202", "a203", "a204", "a205", "a206", "a207", "a208", "a209", "a210", "a211", "a212", "a213", "a214", "a215", "a216", "a217", "a218", "a219", "a220", "a221", "a222", "a223", "a224", "a225", "a226", "a227", "a228", "a229", "a230", "a231", "a232", "a233", "a234", "a235", "a236", "a237", "a238", "a239", "a240", "a241", "a242", "a243", "a244", "a245", "a246", "a247", "a248", "a249", "a250", "a251", "a252", "a253", "a254", "a255", "s4", "s5", "s6", "s7", "s8", "s9", "s10", "s11", "s12", "s13", "s14", "s15", "s16", "s17", "s18", "s19", "s20", "s21", "s22", "s23", "s24", "s25", "s26", "s27", "s28", "s29", "s30", "s31", "s32", "s33", "s34", "s35", "s36", "s37", "s38", "s39", "s40", "s41", "s42", "s43", "s44", "s45", "s46", "s47", "s48", "s49", "s50", "s51", "s52", "s53", "s54", "s55", "s56", "s57", "s58", "s59", "s60", "s61", "s62", "s63", "s64", "s65", "s66", "s67", "s68", "s69", "s70", "s71", "s72", "s73", "s74", "s75", "s76", "s77", "s78", "s79", "s80", "s81", "s82", "s83", "s84", "s85", "s86", "s87", "s88", "s89", "s90", "s91", "s92", "s93", "s94", "s95", "s96", "s97", "s98", "s99", "s100", "s101", "vcc", "scc", "memory");
}

extern "C" void kernel_launch(void* const* d_in, const int* in_sizes, int n_in, void* d_out, int out_size, void* d_ws, size_t ws_size,
                              hipStream_t stream) {
    const int idx[14] = {0, 10, 11, 3, 4, -1, 12, 5, 6, -2, 1, 2, 7, 8};
    Args a;
    for (int k = 0; k < 14; ++k) {
        const int j = idx[k];
        a.b[k].p = j >= 0 ? d_in[j] : (j == -1 ? d_ws : d_out);
        a.b[k].bytes = j >= 0 ? (uint32_t)in_sizes[j] * 2u : (j == -1 ? 0x1600000u : (uint32_t)out_size * 4u);
        a.b[k].fmt = 0x20000u;
    }
    const uint8_t job[80] = {3, 7, 65, 69, 41, 45, 49, 53, 30, 34, 70, 14, 18, 22, 26, 57, 0, 4, 8, 66, 11, 15, 19, 23, 27, 31, 35, 71, 38, 42, 46, 50, 1, 5, 9, 67, 12, 16, 20, 24, 28, 32, 36, 72, 39, 43, 47, 51, 2, 6, 10, 68, 13, 17, 21, 25, 29, 33, 37, 73, 40, 44, 48, 52, 61, 54, 58, 62, 55, 59, 63, 56, 60, 64, 255, 255, 255, 255, 255, 255};
    for (int k = 0; k < 80; ++k) a.job[k] = job[k];
    fused_ln_s5_swiglu<<<256, 256, 0, stream>>>(a);
}
```
